```python
import torch

def _softmax(x, axis=-1):
    return torch.softmax(x, dim=axis)
def _gelu(x):
    return torch.nn.functional.gelu(x, approximate="tanh")
def _silu(x):
    return torch.nn.functional.silu(x)
def _sigmoid(x):
    return torch.sigmoid(x)
def _layernorm(x, axis=-1, eps=1e-5):
    m = x.mean(dim=axis, keepdim=True)
    v = x.var(dim=axis, keepdim=True, unbiased=False)
    return (x - m) / torch.sqrt(v + eps)
def _scatter_add(tgt, idx, src, axis=0):
    return tgt.clone().index_add_(axis, idx, src)
def _segment_sum(src, seg, n):
    out = torch.zeros((n,) + src.shape[1:], dtype=src.dtype)
    return out.index_add_(0, seg, src)
def _pad(x, pw):
    flat = tuple(v for pair in reversed(pw) for v in pair)
    return torch.nn.functional.pad(x, flat)

kernel_name = 'recurrent_scan__sliding_block_cached__glu_gated_glu__prompt'

def setup_inputs():

    _w = (lambda a: (lambda t: t.to(torch.bfloat16) if t.is_floating_point() else t)(torch.as_tensor(a)))
    def _A_setup():
        B, Sq, D = 1, 2048, 640
        Hq, Hkv, Sk, Dh = 32, 32, 2048, 128
        assert Hq % Hkv == 0, (Hq, Hkv)
        return {
            "x": _w((lambda *s: torch.randn(*s, dtype=torch.bfloat16) * 0.05)(B, Sq, D)),
            "k_cache": _w((lambda *s: torch.randn(*s, dtype=torch.bfloat16) * 0.8)(B, Hkv, Sk, Dh)),
            "v_cache": _w((lambda *s: torch.randn(*s, dtype=torch.bfloat16) * 0.2)(B, Hkv, Sk, Dh)),
            "wq": _w((lambda *s: torch.randn(*s, dtype=torch.bfloat16) * 0.8)(D, Hq * Dh) * (D ** -0.5)),
            "wo": _w((lambda *s: torch.randn(*s, dtype=torch.bfloat16) * 0.2)(Hq * Dh, D) * ((Hq * Dh) ** -0.5)),
        }
    _A_in = _A_setup()

    def _B_setup():
        B, S, D, F = 1, 2048, 640, 1728
        return {
            "x": _w((lambda *s: torch.randn(*s, dtype=torch.bfloat16) * 0.05)(B, S, D)),
            "w_gate": _w((lambda *s: torch.randn(*s, dtype=torch.bfloat16) * 0.2)(D, F)),
            "w_up": _w((lambda *s: torch.randn(*s, dtype=torch.bfloat16) * 0.1)(D, F)),
            "w_down": _w((lambda *s: torch.randn(*s, dtype=torch.bfloat16) * 0.1)(F, D)),
        }
    _B_in = _B_setup()

    return {
        "A_x": _A_in["x"],
        "A_k_cache": _A_in["k_cache"],
        "A_v_cache": _A_in["v_cache"],
        "A_wq": _A_in["wq"],
        "A_wo": _A_in["wo"],
        "B_x": _B_in["x"],
        "B_w_gate": _B_in["w_gate"],
        "B_w_up": _B_in["w_up"],
        "B_w_down": _B_in["w_down"],
    }

def reference(A_x, A_k_cache, A_v_cache, A_wq, A_wo, B_x, B_w_gate, B_w_up, B_w_down):
    def _A_ref(x, k_cache, v_cache, wq, wo):
        B, Sq, D = x.shape
        Hq, Dh = 32, 128
        Hkv, Sk = k_cache.shape[1], k_cache.shape[2]
        W = min(4096, max(256, Sk // 4))
        rep = Hq // Hkv
        q = (lambda x, *d: x.permute(*d))((x @ wq).reshape(B, Sq, Hq, Dh), 0, 2, 1, 3)
        k = (lambda x, n, axis: torch.repeat_interleave(x, n, dim=axis))(k_cache[:, :, Sk - W:, :], rep, axis=1)
        v = (lambda x, n, axis: torch.repeat_interleave(x, n, dim=axis))(v_cache[:, :, Sk - W:, :], rep, axis=1)
        scores = torch.einsum("bhqd,bhkd->bhqk", q, k) * (Dh ** -0.5)
        p = _softmax(scores, axis=-1)
        o = torch.einsum("bhqk,bhkd->bhqd", p, v)
        return (lambda x, *d: x.permute(*d))(o, 0, 2, 1, 3).reshape(B, Sq, Hq * Dh) @ wo

    def _B_ref(x, w_gate, w_up, w_down):
        g = torch.einsum("bsd,df->bsf", x, w_gate)
        u = torch.einsum("bsd,df->bsf", x, w_up)
        h = _sigmoid(g) * u
        return torch.einsum("bsf,fd->bsd", h, w_down)

    x = A_x
    xn = _layernorm(x, axis=-1)
    s = _A_ref(xn, A_k_cache, A_v_cache, A_wq, A_wo)
    g = _silu(_B_ref(xn, B_w_gate, B_w_up, B_w_down))
    return x + s * g
```

```cpp
#include <hip/hip_runtime.h>
#include <hip/hip_bf16.h>
#include <stdint.h>

#define LAS __attribute__((address_space(3)))
typedef short s16x4 __attribute__((ext_vector_type(4)));
typedef unsigned u32x4 __attribute__((ext_vector_type(4)));
typedef unsigned u32x2 __attribute__((ext_vector_type(2)));
typedef unsigned short u16;

static constexpr unsigned WS_XN = 0u, WS_H = 2621440u, WS_O = 9699328u, WS_W = 26476544u, WS_END = 33931264u;
#define K0GRID (128 + 182)
static constexpr int TOUCH = 0;
static constexpr float QSCALE = 0.08838834764831845f * 1.4426950408889634f;

__device__ __forceinline__ float bf2f(unsigned v) { return __uint_as_float(v << 16); }
__device__ __forceinline__ unsigned pk2(float a, float b) {
  unsigned r; asm("v_cvt_pk_bf16_f32 %0, %1, %2" : "=v"(r) : "v"(a), "v"(b)); return r;
}

typedef __attribute__((address_space(1))) unsigned gu32;
extern "C" __device__ unsigned long long llvm_dispatch_id() __asm("llvm.amdgcn.dispatch.id");
static constexpr unsigned WS_FL = WS_END;

__device__ __forceinline__ __amdgpu_buffer_rsrc_t mk_rsrc(const void* p, unsigned bytes) {
  return __builtin_amdgcn_make_buffer_rsrc((void*)p, 0, bytes, 0x00020000);
}
__device__ __forceinline__ void st16_wt(__amdgpu_buffer_rsrc_t r, unsigned off, u32x4 v) {
  __builtin_amdgcn_raw_buffer_store_b128(v, r, off, 0, 16);
}
__device__ __forceinline__ void publish(char* ws, unsigned off, unsigned nonce, bool leader) {
  asm volatile("s_waitcnt vmcnt(0)" ::: "memory");
  __syncthreads();
  if (leader) __hip_atomic_store((gu32*)(ws + off), nonce, __ATOMIC_RELAXED, __HIP_MEMORY_SCOPE_AGENT);
}

__device__ __forceinline__ void unit_load(int ks, int cp, unsigned slot, const u16* wd, const u16* wo, const unsigned (&vo)[10]) {
  const u16* src = (ks < 54 ? wd + (size_t)ks * 32 * 640 : wo + (size_t)(ks - 54) * 32 * 640) + cp * 160;
  const u32x4 r = {(unsigned)(size_t)src, (unsigned)((size_t)src >> 32) & 0xffffu, 40000u, 0x00020000u};
#pragma unroll
  for (int i = 0; i < 10; ++i)
    asm volatile("s_mov_b32 m0, %2\n\ts_nop 0\n\tbuffer_load_dwordx4 %0, %1, 0 offen lds" :: "v"(vo[i]), "s"(r), "s"(slot + i * 1024) : "memory");
}
template <bool MAYLOAD>
__device__ __forceinline__ void unit_store(int ks, int cp, unsigned slot, __amdgpu_buffer_rsrc_t rws, int l, bool load, int ks2, const u16* wd,
                                           const u16* wo, const unsigned (&vo)[10]) {
  const int g = l >> 4, q = (l >> 2) & 3, p = l & 3;
  const unsigned base = slot + (4 * g + q) * 320 + (p >> 1) * 16 + (p & 1) * 8;
  u32x4 o[10];
#pragma unroll
  for (int i = 0; i < 10; ++i) {
    const u32x2 a2 = __builtin_bit_cast(u32x2, __builtin_amdgcn_ds_read_tr16_b64_v4i16((LAS s16x4*)(size_t)(base + i * 32)));
    const u32x2 b2 = __builtin_bit_cast(u32x2, __builtin_amdgcn_ds_read_tr16_b64_v4i16((LAS s16x4*)(size_t)(base + i * 32 + 16 * 320)));
    o[i] = u32x4{a2.x, a2.y, b2.x, b2.y};
  }
  if (MAYLOAD && load) {
    asm volatile("s_waitcnt lgkmcnt(0)" : "+v"(o[0]), "+v"(o[1]), "+v"(o[2]), "+v"(o[3]), "+v"(o[4]), "+v"(o[5]), "+v"(o[6]), "+v"(o[7]), "+v"(o[8]), "+v"(o[9]) :: "memory");
    unit_load(ks2, cp, slot, wd, wo, vo);
  }
  __builtin_amdgcn_sched_barrier(0);
#pragma unroll
  for (int i = 0; i < 10; ++i)
    st16_wt(rws, WS_W + (unsigned)((2 * cp + i / 5) * 182 + ks) * 5120u + (i % 5) * 1024 + l * 16, o[i]);
}

struct Args { const u16* x; const u16* kc; const u16* vc; const u16* wq; const u16* wo; const u16* wg; const u16* wu; const u16* wd; char* ws; u16* out; };
typedef __attribute__((address_space(4))) const Args* KArgs;
__device__ __forceinline__ KArgs kargs() {
  KArgs p = (KArgs)__builtin_amdgcn_kernarg_segment_ptr();
  asm volatile("" : "+s"(p));
  return p;
}

__device__ __forceinline__ void relayout(KArgs a, char* ws, char* lds, int w, int l, int b, unsigned nonce) {
      const int rb = (b >> 3) & 7, x8 = b & 7;
      const int j = rb * 5 + ((x8 - rb) & 7);
      const bool fifth = j + 160 < 182;
      const unsigned slot0 = (unsigned)(size_t)(LAS char*)lds + w * 40960;
      const __amdgpu_buffer_rsrc_t rws = mk_rsrc(ws, WS_END);
      const u16* wd = a->wd; const u16* wo = a->wo;
      unsigned vo[10];
#pragma unroll
      for (int i = 0; i < 10; ++i) { const int L = i * 64 + l, r = L / 20; vo[i] = r * 1280 + (L - r * 20) * 16; }
#pragma unroll 1
      for (int k = 0; k < 4; ++k) unit_load(j + 40 * k, w, slot0 + k * 10240, wd, wo, vo);
      gu32* wf = (gu32*)(ws + WS_FL + 4096 + w * 768 + j * 4);
#pragma unroll 1
      for (int k = 0; k < 4; ++k) {
        if (k < 3) { if (fifth && k) asm volatile("s_waitcnt vmcnt(40)" ::: "memory"); else asm volatile("s_waitcnt vmcnt(30)" ::: "memory"); }
        unit_store<true>(j + 40 * k, w, slot0 + k * 10240, rws, l, k == 0 && fifth, j + 160, wd, wo, vo);
        if (k == 2) {
          asm volatile("s_waitcnt vmcnt(10)" ::: "memory");
          if (l < 2) __hip_atomic_store(wf + 40 * l, nonce, __ATOMIC_RELAXED, __HIP_MEMORY_SCOPE_AGENT);
        }
      }
      if (fifth) unit_store<false>(j + 160, w, slot0, rws, l, false, 0, wd, wo, vo);
      asm volatile("s_waitcnt vmcnt(0)" ::: "memory");
      if (l < (fifth ? 3 : 2)) __hip_atomic_store(wf + 80 + 40 * l, nonce, __ATOMIC_RELAXED, __HIP_MEMORY_SCOPE_AGENT);
      if (j == 39 && w == 0) {
        bool all = false;
        for (int it = 0; it < 0x40000 && !all; ++it) {
          bool ok = true;
#pragma unroll
          for (int i = 0; i < 4; ++i) ok &= __hip_atomic_load((gu32*)(ws + WS_FL + (l * 4 + i) * 4), __ATOMIC_RELAXED, __HIP_MEMORY_SCOPE_AGENT) == nonce;
          all = __all(ok);
        }
        if (all && l == 0) __hip_atomic_store((gu32*)(ws + WS_FL + 3584), nonce, __ATOMIC_RELAXED, __HIP_MEMORY_SCOPE_AGENT);
      }
}

__global__ __launch_bounds__(256, 1) void fused_block(const u16* x_, const u16* kc_, const u16* vc_, const u16* wq_, const u16* wo_, const u16* wg_,
                                                      const u16* wu_, const u16* wd_, char* ws_, u16* out_) {
  extern __shared__ __attribute__((aligned(16))) char lds[];
  const int b = blockIdx.x, xcd = b & 7, idx = b >> 3, rb = idx & 7, h = 4 * xcd + (idx >> 3);
  const int w = __builtin_amdgcn_readfirstlane(threadIdx.x >> 6);
  const unsigned id = (unsigned)llvm_dispatch_id();
  unsigned nonce;
  const unsigned M = ((0x1Fu << rb) | (0x1Fu >> (8 - rb))) & 0xFFu;
  const int x8 = h >> 2;
  const unsigned skip = __builtin_amdgcn_readfirstlane(((h & 3) == 3 ? 1u : 0u) & (M >> x8) & 1u);
  const int c = __builtin_amdgcn_readfirstlane((h - __builtin_popcount(M & ((1u << x8) - 1u))) & (int)(skip - 1u));
  const unsigned tb64 = 4 * rb + w;
  {
    KArgs a = kargs();
    const int tid = threadIdx.x;
    char* ws = a->ws;
    const u16* xp = a->x;
    unsigned epoch;
    asm volatile("s_load_dword %0, %1, 0x0" : "=s"(epoch) : "s"(ws + WS_FL + 3584), "s"(xp));
    const __amdgpu_buffer_rsrc_t rws = mk_rsrc(ws, WS_END);
    if (w < 2) {
      const int tok = rb * 256 + h * 8 + (tid >> 4), q = tid & 15;
      const u32x4* xr = (const u32x4*)(xp + (size_t)tok * 640);
      u32x4 v[5]; float f[40];
#pragma unroll
      for (int i = 0; i < 5; ++i) v[i] = xr[q + 16 * i];
      float s = 0.f;
#pragma unroll
      for (int i = 0; i < 5; ++i)
#pragma unroll
        for (int j = 0; j < 4; ++j) { f[8*i+2*j] = bf2f(v[i][j] & 0xffffu); f[8*i+2*j+1] = __uint_as_float(v[i][j] & 0xffff0000u); s += f[8*i+2*j] + f[8*i+2*j+1]; }
#pragma unroll
      for (int m = 1; m < 16; m <<= 1) s += __shfl_xor(s, m);
      const float mean = s * (1.f / 640.f);
      float vs = 0.f;
#pragma unroll
      for (int i = 0; i < 40; ++i) { f[i] -= mean; vs += f[i] * f[i]; }
#pragma unroll
      for (int m = 1; m < 16; m <<= 1) vs += __shfl_xor(vs, m);
      const float r = QSCALE / sqrtf(vs * (1.f / 640.f) + 1e-5f);
      const int t64 = tok >> 6, j4 = (tok >> 4) & 3, t = tok & 15;
#pragma unroll
      for (int i = 0; i < 5; ++i) {
        const int ch = q + 16 * i, ks = ch >> 2, g = ch & 3;
        u32x4 o = {pk2(f[8*i]*r, f[8*i+1]*r), pk2(f[8*i+2]*r, f[8*i+3]*r), pk2(f[8*i+4]*r, f[8*i+5]*r), pk2(f[8*i+6]*r, f[8*i+7]*r)};
        st16_wt(rws, WS_XN + (unsigned)((t64 * 20 + ks) * 4 + j4) * 1024u + (g * 16 + t) * 16, o);
      }
    }
    asm volatile("s_waitcnt lgkmcnt(0)" : "+s"(epoch));
    nonce = epoch * 0x9E3779B1u + id * 0x85EBCA77u + 0x7F4A7C15u;
    unsigned d0, d1, d2, d3, d4, d5, d6 = 0, d7 = 0, d8 = 0, d9 = 0;
    if (w >= 2) {
      const int t = tid - 128, part = t & 7;
      const char* pq = (const char*)a->wq + h * 256; const char* pg = (const char*)a->wg + c * 128; const char* pu = (const char*)a->wu + c * 128;
      asm volatile("" : "+s"(pq), "+s"(pg), "+s"(pu));
      const char* p0 = part < 4 ? pq + part * 64 : (part < 6 ? pg : pu) + (part & 1) * 64;
      const unsigned st = part < 4 ? 8192u : 3456u;
      p0 += (size_t)(rb * 80 + (t >> 3)) * st;
      asm volatile("global_load_dword %0, %1, off" : "=v"(d0) : "v"(p0));
      asm volatile("global_load_dword %0, %1, off" : "=v"(d1) : "v"(p0 + 16 * st));
      asm volatile("global_load_dword %0, %1, off" : "=v"(d2) : "v"(p0 + 32 * st));
      asm volatile("global_load_dword %0, %1, off" : "=v"(d3) : "v"(p0 + 48 * st));
      asm volatile("global_load_dword %0, %1, off" : "=v"(d4) : "v"(p0 + 64 * st));
      unsigned long long pc; unsigned sz;
      asm volatile("s_getpc_b64 %0\nK9pf:\n\ts_mov_b32 %1, K9end-K9pf" : "=s"(pc), "=s"(sz));
      const unsigned o = min((unsigned)(idx * 32 + (t & 31)) * 64u, sz - 4u);
      asm volatile("global_load_dword %0, %1, off" : "=v"(d5) : "v"((const char*)pc + o));
      if (TOUCH == 1) {
        const char* q0 = (b < 76 ? (const char*)a->wd + min(b * 455, 34560 - 512) * 64 : (const char*)a->wo + min((b - 76) * 456, 81920 - 512) * 64) + t * 64;
        asm volatile("global_load_dword %0, %1, off" : "=v"(d6) : "v"(q0));
        asm volatile("global_load_dword %0, %1, off" : "=v"(d7) : "v"(q0 + 8192));
        asm volatile("global_load_dword %0, %1, off" : "=v"(d8) : "v"(q0 + 16384));
        asm volatile("global_load_dword %0, %1, off" : "=v"(d9) : "v"(q0 + 24576));
      }
    } else {
      d0 = d1 = d2 = d3 = d4 = d5 = 0;
      asm volatile("s_waitcnt vmcnt(0)" ::: "memory");
    }
    __builtin_amdgcn_s_barrier();
    if (tid == 0) __hip_atomic_store((gu32*)(ws + WS_FL + rb * 128 + h * 4), nonce, __ATOMIC_RELAXED, __HIP_MEMORY_SCOPE_AGENT);
    asm volatile("s_waitcnt vmcnt(0)" :: "v"(d0), "v"(d1), "v"(d2), "v"(d3), "v"(d4), "v"(d5), "v"(d6), "v"(d7), "v"(d8), "v"(d9) : "memory");
    if (skip) {
      relayout(a, ws, lds, w, tid & 63, b, nonce);
      __syncthreads();
    }
  }
  unsigned bad;
  asm volatile(
      "s_load_dwordx2 s[56:57], %1, 64\n\t"
      "s_mov_b32 s58, 0x209c000\n\t"
      "s_mov_b32 s59, 0x20000\n\t"
      "s_load_dwordx2 s[72:73], %1, 8\n\t"
      "s_mov_b32 s74, 0x1000000\n\t"
      "s_mov_b32 s75, 0x20000\n\t"
      "s_load_dwordx2 s[76:77], %1, 16\n\t"
      "s_mov_b32 s78, 0x1000000\n\t"
      "s_mov_b32 s79, 0x20000\n\t"
      "s_load_dwordx2 s[60:61], %1, 24\n\t"
      "s_mov_b32 s62, 0x500000\n\t"
      "s_mov_b32 s63, 0x20000\n\t"
      "s_load_dwordx2 s[64:65], %1, 40\n\t"
      "s_mov_b32 s66, 0x21c000\n\t"
      "s_mov_b32 s67, 0x20000\n\t"
      "s_load_dwordx2 s[68:69], %1, 48\n\t"
      "s_mov_b32 s70, 0x21c000\n\t"
      "s_mov_b32 s71, 0x20000\n\t"
      "s_mov_b32 s99, %4\n\t"
      "s_mov_b32 s53, %6\n\t"
      "s_mov_b32 s52, %8\n\t"
      "s_lshl_b32 s50, %9, 7\n\t"
      "s_add_u32 s50, s50, 0x205c000\n\t"
      "s_lshl_b32 s47, %2, 2\n\t"
      "s_add_u32 s47, s47, s50\n\t"
      "s_mul_i32 s80, %5, 0x14000\n\t"
      "s_lshl_b32 s81, %2, 8\n\t"
      "s_lshl_b32 s82, %3, 7\n\t"
      "s_mul_i32 s55, %5, 0x36000\n\t"
      "s_lshl_b32 s93, %3, 13\n\t"
      "s_add_u32 s55, s55, s93\n\t"
      "s_add_u32 s55, s55, 0x280000\n\t"
      "s_mul_i32 s54, %5, 0x80000\n\t"
      "s_lshl_b32 s93, %2, 14\n\t"
      "s_add_u32 s54, s54, s93\n\t"
      "s_add_u32 s54, s54, 0x940000\n\t"
      "s_mul_i32 s95, %2, 0x80000\n\t"
      "s_add_u32 s95, s95, 0x60000\n\t"
      "s_mov_b32 s51, %7\n\t"
      "v_mbcnt_lo_u32_b32 v48, -1, 0\n\t"
      "v_mbcnt_hi_u32_b32 v48, -1, v48\n\t"
      "v_lshlrev_b32 v0, 4, v48\n\t"
      "v_lshrrev_b32 v49, 4, v48\n\t"
      "v_bfe_u32 v50, v48, 2, 2\n\t"
      "v_and_b32 v51, 3, v48\n\t"
      "v_and_b32 v55, 1, v49\n\t"
      "v_lshlrev_b32 v52, 12, v49\n\t"
      "v_lshl_add_u32 v52, v50, 6, v52\n\t"
      "v_add_u32 v52, 0x8000, v52\n\t"
      "v_lshlrev_b32 v53, 1, v55\n\t"
      "v_xor_b32 v54, v51, v53\n\t"
      "v_lshl_add_u32 v1, v54, 4, v52\n\t"
      "v_lshrrev_b32 v54, 1, v51\n\t"
      "v_add_u32 v54, v54, v53\n\t"
      "v_lshl_add_u32 v2, v54, 4, v52\n\t"
      "v_and_b32 v54, 1, v51\n\t"
      "v_lshl_add_u32 v2, v54, 3, v2\n\t"
      "v_xor_b32 v3, 32, v2\n\t"
      "v_bfe_u32 v56, v48, 2, 3\n\t"
      "v_lshrrev_b32 v57, 5, v48\n\t"
      "s_lshl_b32 s93, s99, 4\n\t"
      "v_add_u32 v52, s93, v56\n\t"
      "v_add_u32 v52, 0, v52\n\t"
      "v_xor_b32 v53, 0, v51\n\t"
      "v_lshlrev_b32 v53, 4, v53\n\t"
      "v_lshl_add_u32 v53, v57, 6, v53\n\t"
      "v_lshl_add_u32 v4, v52, 13, v53\n\t"
      "v_mul_u32_u24 v52, 0xd80, v52\n\t"
      "v_add_u32 v6, v52, v53\n\t"
      "v_add_u32 v52, s93, v56\n\t"
      "v_add_u32 v52, 8, v52\n\t"
      "v_xor_b32 v53, 2, v51\n\t"
      "v_lshlrev_b32 v53, 4, v53\n\t"
      "v_lshl_add_u32 v53, v57, 6, v53\n\t"
      "v_lshl_add_u32 v5, v52, 13, v53\n\t"
      "v_mul_u32_u24 v52, 0xd80, v52\n\t"
      "v_add_u32 v7, v52, v53\n\t"
      "v_add_u32 v52, s93, v56\n\t"
      "v_lshrrev_b32 v53, 2, v56\n\t"
      "v_lshlrev_b32 v53, 1, v53\n\t"
      "v_xor_b32 v53, v53, v51\n\t"
      "v_lshlrev_b32 v53, 4, v53\n\t"
      "v_lshl_add_u32 v53, v57, 6, v53\n\t"
      "v_lshl_add_u32 v12, v52, 8, v53\n\t"
      "v_and_b32 v54, 15, v48\n\t"
      "v_add_u32 v52, s93, v49\n\t"
      "v_xor_b32 v53, v54, v49\n\t"
      "v_xor_b32 v56, 0, v53\n\t"
      "v_lshlrev_b32 v56, 4, v56\n\t"
      "v_lshl_add_u32 v8, v52, 8, v56\n\t"
      "v_xor_b32 v56, 4, v53\n\t"
      "v_lshlrev_b32 v56, 4, v56\n\t"
      "v_lshl_add_u32 v9, v52, 8, v56\n\t"
      "v_xor_b32 v56, 8, v53\n\t"
      "v_lshlrev_b32 v56, 4, v56\n\t"
      "v_lshl_add_u32 v10, v52, 8, v56\n\t"
      "v_xor_b32 v56, 12, v53\n\t"
      "v_lshlrev_b32 v56, 4, v56\n\t"
      "v_lshl_add_u32 v11, v52, 8, v56\n\t"
      "v_add_u32 v52, 0, v49\n\t"
      "v_xor_b32 v52, v52, v54\n\t"
      "v_lshlrev_b32 v52, 4, v52\n\t"
      "v_lshl_add_u32 v52, v54, 8, v52\n\t"
      "v_add_u32 v112, 0x18000, v52\n\t"
      "v_add_u32 v52, 4, v49\n\t"
      "v_xor_b32 v52, v52, v54\n\t"
      "v_lshlrev_b32 v52, 4, v52\n\t"
      "v_lshl_add_u32 v52, v54, 8, v52\n\t"
      "v_add_u32 v113, 0x18000, v52\n\t"
      "v_add_u32 v52, 8, v49\n\t"
      "v_xor_b32 v52, v52, v54\n\t"
      "v_lshlrev_b32 v52, 4, v52\n\t"
      "v_lshl_add_u32 v52, v54, 8, v52\n\t"
      "v_add_u32 v114, 0x18000, v52\n\t"
      "v_add_u32 v52, 12, v49\n\t"
      "v_xor_b32 v52, v52, v54\n\t"
      "v_lshlrev_b32 v52, 4, v52\n\t"
      "v_lshl_add_u32 v52, v54, 8, v52\n\t"
      "v_add_u32 v115, 0x18000, v52\n\t"
      "v_lshrrev_b32 v52, 1, v49\n\t"
      "v_lshlrev_b32 v52, 11, v52\n\t"
      "v_lshl_add_u32 v53, v55, 2, v50\n\t"
      "v_lshl_add_u32 v52, v53, 6, v52\n\t"
      "v_lshrrev_b32 v53, 1, v51\n\t"
      "v_lshl_add_u32 v53, v55, 1, v53\n\t"
      "v_lshl_add_u32 v52, v53, 4, v52\n\t"
      "v_and_b32 v53, 1, v51\n\t"
      "v_lshl_add_u32 v52, v53, 3, v52\n\t"
      "v_add_u32 v116, 0x18000, v52\n\t"
      "v_xor_b32 v117, 32, v116\n\t"
      "s_lshl_b32 s93, s99, 13\n\t"
      "s_add_u32 s83, s93, 0x8000\n\t"
      "s_add_u32 s94, s93, 0x18000\n\t"
      "s_lshl_b32 s93, s99, 12\n\t"
      "s_add_u32 s96, s93, 0x18000\n\t"
      "s_mov_b32 s98, 0x8000\n\t"
      "s_mov_b32 s44, 0x8000\n\t"
      "s_waitcnt lgkmcnt(0)\n\t"
      "s_mov_b32 s36, s56\n\t"
      "s_mov_b32 s37, s57\n\t"
      "s_mov_b32 s38, s58\n\t"
      "s_mov_b32 s39, s59\n\t"
      "s_mov_b32 s86, 0x3e0293ee\n\t"
      "s_mov_b32 s87, 0x3e0293ee\n\t"
      "s_mov_b32 s88, 0xc13504f3\n\t"
      "s_mov_b32 s89, 0xc13504f3\n\t"
      "s_mov_b32 s48, 0x53800000\n\t"
      "s_cmp_lg_u32 s53, 0\n\t"
      "s_cbranch_scc1 K1skip\n\t"
      "s_mov_b32 m0, s83\n\t"
      "s_nop 0\n\t"
      "buffer_load_dwordx4 v4, s[60:63], s81 offen lds\n\t"
      "s_add_u32 m0, s83, 0x380\n\t"
      "s_nop 0\n\t"
      "buffer_load_dwordx4 v4, s[60:63], s81 offen offset:128 lds\n\t"
      "s_add_u32 m0, s83, 0x800\n\t"
      "s_nop 0\n\t"
      "buffer_load_dwordx4 v6, s[64:67], s82 offen lds\n\t"
      "s_add_u32 m0, s83, 0xc00\n\t"
      "s_nop 0\n\t"
      "buffer_load_dwordx4 v6, s[68:71], s82 offen lds\n\t"
      "s_add_u32 m0, s83, 0x1000\n\t"
      "s_nop 0\n\t"
      "buffer_load_dwordx4 v5, s[60:63], s81 offen lds\n\t"
      "s_add_u32 m0, s83, 0x1380\n\t"
      "s_nop 0\n\t"
      "buffer_load_dwordx4 v5, s[60:63], s81 offen offset:128 lds\n\t"
      "s_add_u32 m0, s83, 0x1800\n\t"
      "s_nop 0\n\t"
      "buffer_load_dwordx4 v7, s[64:67], s82 offen lds\n\t"
      "s_add_u32 m0, s83, 0x1c00\n\t"
      "s_nop 0\n\t"
      "buffer_load_dwordx4 v7, s[68:71], s82 offen lds\n\t"
      "s_add_u32 s81, s81, 0x80000\n\t"
      "s_add_u32 s82, s82, 0x36000\n\t"
      "s_add_u32 s83, s83, 0x8000\n\t"
      "s_mov_b32 m0, s83\n\t"
      "s_nop 0\n\t"
      "buffer_load_dwordx4 v4, s[60:63], s81 offen lds\n\t"
      "s_add_u32 m0, s83, 0x380\n\t"
      "s_nop 0\n\t"
      "buffer_load_dwordx4 v4, s[60:63], s81 offen offset:128 lds\n\t"
      "s_add_u32 m0, s83, 0x800\n\t"
      "s_nop 0\n\t"
      "buffer_load_dwordx4 v6, s[64:67], s82 offen lds\n\t"
      "s_add_u32 m0, s83, 0xc00\n\t"
      "s_nop 0\n\t"
      "buffer_load_dwordx4 v6, s[68:71], s82 offen lds\n\t"
      "s_add_u32 m0, s83, 0x1000\n\t"
      "s_nop 0\n\t"
      "buffer_load_dwordx4 v5, s[60:63], s81 offen lds\n\t"
      "s_add_u32 m0, s83, 0x1380\n\t"
      "s_nop 0\n\t"
      "buffer_load_dwordx4 v5, s[60:63], s81 offen offset:128 lds\n\t"
      "s_add_u32 m0, s83, 0x1800\n\t"
      "s_nop 0\n\t"
      "buffer_load_dwordx4 v7, s[64:67], s82 offen lds\n\t"
      "s_add_u32 m0, s83, 0x1c00\n\t"
      "s_nop 0\n\t"
      "buffer_load_dwordx4 v7, s[68:71], s82 offen lds\n\t"
      "v_lshrrev_b32 v14, 2, v0\n\t"
      "v_and_b32 v14, 0x7c, v14\n\t"
      "s_mov_b32 s46, 0\n\t"
      "K1plF:\n\t"
      "buffer_load_dword v15, v14, s[56:59], s50 offen sc1\n\t"
      "s_waitcnt vmcnt(0)\n\t"
      "v_cmp_ne_u32 vcc, s52, v15\n\t"
      "s_nop 1\n\t"
      "s_cbranch_vccz K1plFd\n\t"
      "s_sleep 2\n\t"
      "s_add_u32 s46, s46, 1\n\t"
      "s_cmp_lt_u32 s46, 0x40000\n\t"
      "s_cbranch_scc1 K1plF\n\t"
      "K1plFd:\n\t"
      "buffer_load_dwordx4 v[48:51], v0, s[56:59], s80 offen sc1\n\t"
      "buffer_load_dwordx4 v[52:55], v0, s[56:59], s80 offen offset:1024 sc1\n\t"
      "buffer_load_dwordx4 v[56:59], v0, s[56:59], s80 offen offset:2048 sc1\n\t"
      "buffer_load_dwordx4 v[60:63], v0, s[56:59], s80 offen offset:3072 sc1\n\t"
      "s_add_u32 s80, s80, 0x1000\n\t"
      "buffer_load_dwordx4 v[64:67], v0, s[56:59], s80 offen sc1\n\t"
      "buffer_load_dwordx4 v[68:71], v0, s[56:59], s80 offen offset:1024 sc1\n\t"
      "buffer_load_dwordx4 v[72:75], v0, s[56:59], s80 offen offset:2048 sc1\n\t"
      "buffer_load_dwordx4 v[76:79], v0, s[56:59], s80 offen offset:3072 sc1\n\t"
      "s_add_u32 s80, s80, 0x1000\n\t"
      "buffer_load_dwordx4 v[80:83], v0, s[56:59], s80 offen sc1\n\t"
      "buffer_load_dwordx4 v[84:87], v0, s[56:59], s80 offen offset:1024 sc1\n\t"
      "buffer_load_dwordx4 v[88:91], v0, s[56:59], s80 offen offset:2048 sc1\n\t"
      "buffer_load_dwordx4 v[92:95], v0, s[56:59], s80 offen offset:3072 sc1\n\t"
      "v_accvgpr_write_b32 a0, 0\n\t"
      "v_accvgpr_write_b32 a1, 0\n\t"
      "v_accvgpr_write_b32 a2, 0\n\t"
      "v_accvgpr_write_b32 a3, 0\n\t"
      "v_accvgpr_write_b32 a4, 0\n\t"
      "v_accvgpr_write_b32 a5, 0\n\t"
      "v_accvgpr_write_b32 a6, 0\n\t"
      "v_accvgpr_write_b32 a7, 0\n\t"
      "v_accvgpr_write_b32 a8, 0\n\t"
      "v_accvgpr_write_b32 a9, 0\n\t"
      "v_accvgpr_write_b32 a10, 0\n\t"
      "v_accvgpr_write_b32 a11, 0\n\t"
      "v_accvgpr_write_b32 a12, 0\n\t"
      "v_accvgpr_write_b32 a13, 0\n\t"
      "v_accvgpr_write_b32 a14, 0\n\t"
      "v_accvgpr_write_b32 a15, 0\n\t"
      "v_accvgpr_write_b32 a16, 0\n\t"
      "v_accvgpr_write_b32 a17, 0\n\t"
      "v_accvgpr_write_b32 a18, 0\n\t"
      "v_accvgpr_write_b32 a19, 0\n\t"
      "v_accvgpr_write_b32 a20, 0\n\t"
      "v_accvgpr_write_b32 a21, 0\n\t"
      "v_accvgpr_write_b32 a22, 0\n\t"
      "v_accvgpr_write_b32 a23, 0\n\t"
      "v_accvgpr_write_b32 a24, 0\n\t"
      "v_accvgpr_write_b32 a25, 0\n\t"
      "v_accvgpr_write_b32 a26, 0\n\t"
      "v_accvgpr_write_b32 a27, 0\n\t"
      "v_accvgpr_write_b32 a28, 0\n\t"
      "v_accvgpr_write_b32 a29, 0\n\t"
      "v_accvgpr_write_b32 a30, 0\n\t"
      "v_accvgpr_write_b32 a31, 0\n\t"
      "v_accvgpr_write_b32 a32, 0\n\t"
      "v_accvgpr_write_b32 a33, 0\n\t"
      "v_accvgpr_write_b32 a34, 0\n\t"
      "v_accvgpr_write_b32 a35, 0\n\t"
      "v_accvgpr_write_b32 a36, 0\n\t"
      "v_accvgpr_write_b32 a37, 0\n\t"
      "v_accvgpr_write_b32 a38, 0\n\t"
      "v_accvgpr_write_b32 a39, 0\n\t"
      "v_accvgpr_write_b32 a40, 0\n\t"
      "v_accvgpr_write_b32 a41, 0\n\t"
      "v_accvgpr_write_b32 a42, 0\n\t"
      "v_accvgpr_write_b32 a43, 0\n\t"
      "v_accvgpr_write_b32 a44, 0\n\t"
      "v_accvgpr_write_b32 a45, 0\n\t"
      "v_accvgpr_write_b32 a46, 0\n\t"
      "v_accvgpr_write_b32 a47, 0\n\t"
      "v_accvgpr_write_b32 a48, 0\n\t"
      "v_accvgpr_write_b32 a49, 0\n\t"
      "v_accvgpr_write_b32 a50, 0\n\t"
      "v_accvgpr_write_b32 a51, 0\n\t"
      "v_accvgpr_write_b32 a52, 0\n\t"
      "v_accvgpr_write_b32 a53, 0\n\t"
      "v_accvgpr_write_b32 a54, 0\n\t"
      "v_accvgpr_write_b32 a55, 0\n\t"
      "v_accvgpr_write_b32 a56, 0\n\t"
      "v_accvgpr_write_b32 a57, 0\n\t"
      "v_accvgpr_write_b32 a58, 0\n\t"
      "v_accvgpr_write_b32 a59, 0\n\t"
      "v_accvgpr_write_b32 a60, 0\n\t"
      "v_accvgpr_write_b32 a61, 0\n\t"
      "v_accvgpr_write_b32 a62, 0\n\t"
      "v_accvgpr_write_b32 a63, 0\n\t"
      "v_accvgpr_write_b32 a64, 0\n\t"
      "v_accvgpr_write_b32 a65, 0\n\t"
      "v_accvgpr_write_b32 a66, 0\n\t"
      "v_accvgpr_write_b32 a67, 0\n\t"
      "v_accvgpr_write_b32 a68, 0\n\t"
      "v_accvgpr_write_b32 a69, 0\n\t"
      "v_accvgpr_write_b32 a70, 0\n\t"
      "v_accvgpr_write_b32 a71, 0\n\t"
      "v_accvgpr_write_b32 a72, 0\n\t"
      "v_accvgpr_write_b32 a73, 0\n\t"
      "v_accvgpr_write_b32 a74, 0\n\t"
      "v_accvgpr_write_b32 a75, 0\n\t"
      "v_accvgpr_write_b32 a76, 0\n\t"
      "v_accvgpr_write_b32 a77, 0\n\t"
      "v_accvgpr_write_b32 a78, 0\n\t"
      "v_accvgpr_write_b32 a79, 0\n\t"
      "v_accvgpr_write_b32 a80, 0\n\t"
      "v_accvgpr_write_b32 a81, 0\n\t"
      "v_accvgpr_write_b32 a82, 0\n\t"
      "v_accvgpr_write_b32 a83, 0\n\t"
      "v_accvgpr_write_b32 a84, 0\n\t"
      "v_accvgpr_write_b32 a85, 0\n\t"
      "v_accvgpr_write_b32 a86, 0\n\t"
      "v_accvgpr_write_b32 a87, 0\n\t"
      "v_accvgpr_write_b32 a88, 0\n\t"
      "v_accvgpr_write_b32 a89, 0\n\t"
      "v_accvgpr_write_b32 a90, 0\n\t"
      "v_accvgpr_write_b32 a91, 0\n\t"
      "v_accvgpr_write_b32 a92, 0\n\t"
      "v_accvgpr_write_b32 a93, 0\n\t"
      "v_accvgpr_write_b32 a94, 0\n\t"
      "v_accvgpr_write_b32 a95, 0\n\t"
      "v_accvgpr_write_b32 a96, 0\n\t"
      "v_accvgpr_write_b32 a97, 0\n\t"
      "v_accvgpr_write_b32 a98, 0\n\t"
      "v_accvgpr_write_b32 a99, 0\n\t"
      "v_accvgpr_write_b32 a100, 0\n\t"
      "v_accvgpr_write_b32 a101, 0\n\t"
      "v_accvgpr_write_b32 a102, 0\n\t"
      "v_accvgpr_write_b32 a103, 0\n\t"
      "v_accvgpr_write_b32 a104, 0\n\t"
      "v_accvgpr_write_b32 a105, 0\n\t"
      "v_accvgpr_write_b32 a106, 0\n\t"
      "v_accvgpr_write_b32 a107, 0\n\t"
      "v_accvgpr_write_b32 a108, 0\n\t"
      "v_accvgpr_write_b32 a109, 0\n\t"
      "v_accvgpr_write_b32 a110, 0\n\t"
      "v_accvgpr_write_b32 a111, 0\n\t"
      "v_accvgpr_write_b32 a112, 0\n\t"
      "v_accvgpr_write_b32 a113, 0\n\t"
      "v_accvgpr_write_b32 a114, 0\n\t"
      "v_accvgpr_write_b32 a115, 0\n\t"
      "v_accvgpr_write_b32 a116, 0\n\t"
      "v_accvgpr_write_b32 a117, 0\n\t"
      "v_accvgpr_write_b32 a118, 0\n\t"
      "v_accvgpr_write_b32 a119, 0\n\t"
      "v_accvgpr_write_b32 a120, 0\n\t"
      "v_accvgpr_write_b32 a121, 0\n\t"
      "v_accvgpr_write_b32 a122, 0\n\t"
      "v_accvgpr_write_b32 a123, 0\n\t"
      "v_accvgpr_write_b32 a124, 0\n\t"
      "v_accvgpr_write_b32 a125, 0\n\t"
      "v_accvgpr_write_b32 a126, 0\n\t"
      "v_accvgpr_write_b32 a127, 0\n\t"
      "v_mov_b32 v128, 0\n\t"
      "v_mov_b32 v129, 0\n\t"
      "v_mov_b32 v130, 0\n\t"
      "v_mov_b32 v131, 0\n\t"
      "v_mov_b32 v132, 0\n\t"
      "v_mov_b32 v133, 0\n\t"
      "v_mov_b32 v134, 0\n\t"
      "v_mov_b32 v135, 0\n\t"
      "v_mov_b32 v136, 0\n\t"
      "v_mov_b32 v137, 0\n\t"
      "v_mov_b32 v138, 0\n\t"
      "v_mov_b32 v139, 0\n\t"
      "v_mov_b32 v140, 0\n\t"
      "v_mov_b32 v141, 0\n\t"
      "v_mov_b32 v142, 0\n\t"
      "v_mov_b32 v143, 0\n\t"
      "v_mov_b32 v144, 0\n\t"
      "v_mov_b32 v145, 0\n\t"
      "v_mov_b32 v146, 0\n\t"
      "v_mov_b32 v147, 0\n\t"
      "v_mov_b32 v148, 0\n\t"
      "v_mov_b32 v149, 0\n\t"
      "v_mov_b32 v150, 0\n\t"
      "v_mov_b32 v151, 0\n\t"
      "v_mov_b32 v152, 0\n\t"
      "v_mov_b32 v153, 0\n\t"
      "v_mov_b32 v154, 0\n\t"
      "v_mov_b32 v155, 0\n\t"
      "v_mov_b32 v156, 0\n\t"
      "v_mov_b32 v157, 0\n\t"
      "v_mov_b32 v158, 0\n\t"
      "v_mov_b32 v159, 0\n\t"
      "v_mov_b32 v160, 0\n\t"
      "v_mov_b32 v161, 0\n\t"
      "v_mov_b32 v162, 0\n\t"
      "v_mov_b32 v163, 0\n\t"
      "v_mov_b32 v164, 0\n\t"
      "v_mov_b32 v165, 0\n\t"
      "v_mov_b32 v166, 0\n\t"
      "v_mov_b32 v167, 0\n\t"
      "v_mov_b32 v168, 0\n\t"
      "v_mov_b32 v169, 0\n\t"
      "v_mov_b32 v170, 0\n\t"
      "v_mov_b32 v171, 0\n\t"
      "v_mov_b32 v172, 0\n\t"
      "v_mov_b32 v173, 0\n\t"
      "v_mov_b32 v174, 0\n\t"
      "v_mov_b32 v175, 0\n\t"
      "v_mov_b32 v176, 0\n\t"
      "v_mov_b32 v177, 0\n\t"
      "v_mov_b32 v178, 0\n\t"
      "v_mov_b32 v179, 0\n\t"
      "v_mov_b32 v180, 0\n\t"
      "v_mov_b32 v181, 0\n\t"
      "v_mov_b32 v182, 0\n\t"
      "v_mov_b32 v183, 0\n\t"
      "v_mov_b32 v184, 0\n\t"
      "v_mov_b32 v185, 0\n\t"
      "v_mov_b32 v186, 0\n\t"
      "v_mov_b32 v187, 0\n\t"
      "v_mov_b32 v188, 0\n\t"
      "v_mov_b32 v189, 0\n\t"
      "v_mov_b32 v190, 0\n\t"
      "v_mov_b32 v191, 0\n\t"
      "v_mov_b32 v192, 0\n\t"
      "v_mov_b32 v193, 0\n\t"
      "v_mov_b32 v194, 0\n\t"
      "v_mov_b32 v195, 0\n\t"
      "v_mov_b32 v196, 0\n\t"
      "v_mov_b32 v197, 0\n\t"
      "v_mov_b32 v198, 0\n\t"
      "v_mov_b32 v199, 0\n\t"
      "v_mov_b32 v200, 0\n\t"
      "v_mov_b32 v201, 0\n\t"
      "v_mov_b32 v202, 0\n\t"
      "v_mov_b32 v203, 0\n\t"
      "v_mov_b32 v204, 0\n\t"
      "v_mov_b32 v205, 0\n\t"
      "v_mov_b32 v206, 0\n\t"
      "v_mov_b32 v207, 0\n\t"
      "v_mov_b32 v208, 0\n\t"
      "v_mov_b32 v209, 0\n\t"
      "v_mov_b32 v210, 0\n\t"
      "v_mov_b32 v211, 0\n\t"
      "v_mov_b32 v212, 0\n\t"
      "v_mov_b32 v213, 0\n\t"
      "v_mov_b32 v214, 0\n\t"
      "v_mov_b32 v215, 0\n\t"
      "v_mov_b32 v216, 0\n\t"
      "v_mov_b32 v217, 0\n\t"
      "v_mov_b32 v218, 0\n\t"
      "v_mov_b32 v219, 0\n\t"
      "v_mov_b32 v220, 0\n\t"
      "v_mov_b32 v221, 0\n\t"
      "v_mov_b32 v222, 0\n\t"
      "v_mov_b32 v223, 0\n\t"
      "v_mov_b32 v224, 0\n\t"
      "v_mov_b32 v225, 0\n\t"
      "v_mov_b32 v226, 0\n\t"
      "v_mov_b32 v227, 0\n\t"
      "v_mov_b32 v228, 0\n\t"
      "v_mov_b32 v229, 0\n\t"
      "v_mov_b32 v230, 0\n\t"
      "v_mov_b32 v231, 0\n\t"
      "v_mov_b32 v232, 0\n\t"
      "v_mov_b32 v233, 0\n\t"
      "v_mov_b32 v234, 0\n\t"
      "v_mov_b32 v235, 0\n\t"
      "v_mov_b32 v236, 0\n\t"
      "v_mov_b32 v237, 0\n\t"
      "v_mov_b32 v238, 0\n\t"
      "v_mov_b32 v239, 0\n\t"
      "v_mov_b32 v240, 0\n\t"
      "v_mov_b32 v241, 0\n\t"
      "v_mov_b32 v242, 0\n\t"
      "v_mov_b32 v243, 0\n\t"
      "v_mov_b32 v244, 0\n\t"
      "v_mov_b32 v245, 0\n\t"
      "v_mov_b32 v246, 0\n\t"
      "v_mov_b32 v247, 0\n\t"
      "v_mov_b32 v248, 0\n\t"
      "v_mov_b32 v249, 0\n\t"
      "v_mov_b32 v250, 0\n\t"
      "v_mov_b32 v251, 0\n\t"
      "v_mov_b32 v252, 0\n\t"
      "v_mov_b32 v253, 0\n\t"
      "v_mov_b32 v254, 0\n\t"
      "v_mov_b32 v255, 0\n\t"
      "s_waitcnt vmcnt(20)\n\t"
      "s_barrier\n\t"
      "ds_read_b64_tr_b16 v[16:17], v1\n\t"
      "ds_read_b64_tr_b16 v[18:19], v1 offset:256\n\t"
      "ds_read_b64_tr_b16 v[20:21], v1 offset:8\n\t"
      "ds_read_b64_tr_b16 v[22:23], v1 offset:264\n\t"
      "ds_read_b64_tr_b16 v[24:25], v1 offset:512\n\t"
      "ds_read_b64_tr_b16 v[26:27], v1 offset:768\n\t"
      "ds_read_b64_tr_b16 v[28:29], v1 offset:520\n\t"
      "ds_read_b64_tr_b16 v[30:31], v1 offset:776\n\t"
      "ds_read_b64_tr_b16 v[32:33], v1 offset:1024\n\t"
      "ds_read_b64_tr_b16 v[34:35], v1 offset:1280\n\t"
      "ds_read_b64_tr_b16 v[36:37], v1 offset:1032\n\t"
      "ds_read_b64_tr_b16 v[38:39], v1 offset:1288\n\t"
      "ds_read_b64_tr_b16 v[40:41], v1 offset:1536\n\t"
      "ds_read_b64_tr_b16 v[42:43], v1 offset:1792\n\t"
      "s_mov_b32 s92, 4\n\t"
      "K1proj:\n\t"
      "s_waitcnt vmcnt(8) lgkmcnt(12)\n\t"
      "v_mfma_f32_16x16x32_bf16 a[0:3], v[16:19], v[48:51], a[0:3]\n\t"
      "ds_read_b64_tr_b16 v[44:45], v1 offset:1544\n\t"
      "v_mfma_f32_16x16x32_bf16 a[4:7], v[16:19], v[52:55], a[4:7]\n\t"
      "ds_read_b64_tr_b16 v[46:47], v1 offset:1800\n\t"
      "v_mfma_f32_16x16x32_bf16 a[8:11], v[16:19], v[56:59], a[8:11]\n\t"
      "v_mfma_f32_16x16x32_bf16 a[12:15], v[16:19], v[60:63], a[12:15]\n\t"
      "s_waitcnt lgkmcnt(12)\n\t"
      "v_mfma_f32_16x16x32_bf16 a[16:19], v[20:23], v[48:51], a[16:19]\n\t"
      "ds_read_b64_tr_b16 v[16:17], v2 offset:2048\n\t"
      "v_mfma_f32_16x16x32_bf16 a[20:23], v[20:23], v[52:55], a[20:23]\n\t"
      "ds_read_b64_tr_b16 v[18:19], v2 offset:2304\n\t"
      "v_mfma_f32_16x16x32_bf16 a[24:27], v[20:23], v[56:59], a[24:27]\n\t"
      "s_add_u32 s80, s80, 0x1000\n\t"
      "v_mfma_f32_16x16x32_bf16 a[28:31], v[20:23], v[60:63], a[28:31]\n\t"
      "buffer_load_dwordx4 v[96:99], v0, s[56:59], s80 offen sc1\n\t"
      "s_waitcnt lgkmcnt(12)\n\t"
      "v_mfma_f32_16x16x32_bf16 a[32:35], v[24:27], v[48:51], a[32:35]\n\t"
      "ds_read_b64_tr_b16 v[20:21], v3 offset:2048\n\t"
      "v_mfma_f32_16x16x32_bf16 a[36:39], v[24:27], v[52:55], a[36:39]\n\t"
      "ds_read_b64_tr_b16 v[22:23], v3 offset:2304\n\t"
      "v_mfma_f32_16x16x32_bf16 a[40:43], v[24:27], v[56:59], a[40:43]\n\t"
      "v_mfma_f32_16x16x32_bf16 a[44:47], v[24:27], v[60:63], a[44:47]\n\t"
      "buffer_load_dwordx4 v[100:103], v0, s[56:59], s80 offen offset:1024 sc1\n\t"
      "s_waitcnt lgkmcnt(12)\n\t"
      "v_mfma_f32_16x16x32_bf16 a[48:51], v[28:31], v[48:51], a[48:51]\n\t"
      "ds_read_b64_tr_b16 v[24:25], v2 offset:2560\n\t"
      "v_mfma_f32_16x16x32_bf16 a[52:55], v[28:31], v[52:55], a[52:55]\n\t"
      "ds_read_b64_tr_b16 v[26:27], v2 offset:2816\n\t"
      "v_mfma_f32_16x16x32_bf16 a[56:59], v[28:31], v[56:59], a[56:59]\n\t"
      "v_mfma_f32_16x16x32_bf16 a[60:63], v[28:31], v[60:63], a[60:63]\n\t"
      "buffer_load_dwordx4 v[104:107], v0, s[56:59], s80 offen offset:2048 sc1\n\t"
      "s_waitcnt lgkmcnt(12)\n\t"
      "v_mfma_f32_16x16x32_bf16 a[64:67], v[32:35], v[48:51], a[64:67]\n\t"
      "ds_read_b64_tr_b16 v[28:29], v3 offset:2560\n\t"
      "v_mfma_f32_16x16x32_bf16 a[68:71], v[32:35], v[52:55], a[68:71]\n\t"
      "ds_read_b64_tr_b16 v[30:31], v3 offset:2816\n\t"
      "v_mfma_f32_16x16x32_bf16 a[72:75], v[32:35], v[56:59], a[72:75]\n\t"
      "v_mfma_f32_16x16x32_bf16 a[76:79], v[32:35], v[60:63], a[76:79]\n\t"
      "buffer_load_dwordx4 v[108:111], v0, s[56:59], s80 offen offset:3072 sc1\n\t"
      "s_waitcnt lgkmcnt(12)\n\t"
      "v_mfma_f32_16x16x32_bf16 a[80:83], v[36:39], v[48:51], a[80:83]\n\t"
      "ds_read_b64_tr_b16 v[32:33], v2 offset:3072\n\t"
      "v_mfma_f32_16x16x32_bf16 a[84:87], v[36:39], v[52:55], a[84:87]\n\t"
      "ds_read_b64_tr_b16 v[34:35], v2 offset:3328\n\t"
      "v_mfma_f32_16x16x32_bf16 a[88:91], v[36:39], v[56:59], a[88:91]\n\t"
      "s_add_u32 s81, s81, 0x80000\n\t"
      "s_add_u32 s82, s82, 0x36000\n\t"
      "s_add_u32 s83, s83, 0x8000\n\t"
      "s_cmp_ge_u32 s83, s94\n\t"
      "s_cselect_b32 s93, 0x18000, 0\n\t"
      "s_sub_u32 s83, s83, s93\n\t"
      "s_mov_b32 m0, s83\n\t"
      "v_mfma_f32_16x16x32_bf16 a[92:95], v[36:39], v[60:63], a[92:95]\n\t"
      "buffer_load_dwordx4 v4, s[60:63], s81 offen lds\n\t"
      "s_waitcnt lgkmcnt(12)\n\t"
      "v_mfma_f32_16x16x32_bf16 a[96:99], v[40:43], v[48:51], a[96:99]\n\t"
      "ds_read_b64_tr_b16 v[36:37], v3 offset:3072\n\t"
      "v_mfma_f32_16x16x32_bf16 a[100:103], v[40:43], v[52:55], a[100:103]\n\t"
      "ds_read_b64_tr_b16 v[38:39], v3 offset:3328\n\t"
      "v_mfma_f32_16x16x32_bf16 a[104:107], v[40:43], v[56:59], a[104:107]\n\t"
      "s_add_u32 m0, s83, 0x380\n\t"
      "v_mfma_f32_16x16x32_bf16 a[108:111], v[40:43], v[60:63], a[108:111]\n\t"
      "buffer_load_dwordx4 v4, s[60:63], s81 offen offset:128 lds\n\t"
      "s_waitcnt lgkmcnt(12)\n\t"
      "v_mfma_f32_16x16x32_bf16 a[112:115], v[44:47], v[48:51], a[112:115]\n\t"
      "ds_read_b64_tr_b16 v[40:41], v2 offset:3584\n\t"
      "v_mfma_f32_16x16x32_bf16 a[116:119], v[44:47], v[52:55], a[116:119]\n\t"
      "ds_read_b64_tr_b16 v[42:43], v2 offset:3840\n\t"
      "v_mfma_f32_16x16x32_bf16 a[120:123], v[44:47], v[56:59], a[120:123]\n\t"
      "s_add_u32 m0, s83, 0x800\n\t"
      "v_mfma_f32_16x16x32_bf16 a[124:127], v[44:47], v[60:63], a[124:127]\n\t"
      "buffer_load_dwordx4 v6, s[64:67], s82 offen lds\n\t"
      "s_waitcnt lgkmcnt(12)\n\t"
      "v_mfma_f32_16x16x32_bf16 v[128:131], v[16:19], v[48:51], v[128:131]\n\t"
      "ds_read_b64_tr_b16 v[44:45], v3 offset:3584\n\t"
      "v_mfma_f32_16x16x32_bf16 v[132:135], v[16:19], v[52:55], v[132:135]\n\t"
      "ds_read_b64_tr_b16 v[46:47], v3 offset:3840\n\t"
      "v_mfma_f32_16x16x32_bf16 v[136:139], v[16:19], v[56:59], v[136:139]\n\t"
      "s_add_u32 m0, s83, 0xc00\n\t"
      "v_mfma_f32_16x16x32_bf16 v[140:143], v[16:19], v[60:63], v[140:143]\n\t"
      "buffer_load_dwordx4 v6, s[68:71], s82 offen lds\n\t"
      "s_waitcnt lgkmcnt(12)\n\t"
      "v_mfma_f32_16x16x32_bf16 v[144:147], v[20:23], v[48:51], v[144:147]\n\t"
      "ds_read_b64_tr_b16 v[16:17], v1 offset:16384\n\t"
      "v_mfma_f32_16x16x32_bf16 v[148:151], v[20:23], v[52:55], v[148:151]\n\t"
      "ds_read_b64_tr_b16 v[18:19], v1 offset:16640\n\t"
      "v_mfma_f32_16x16x32_bf16 v[152:155], v[20:23], v[56:59], v[152:155]\n\t"
      "v_mfma_f32_16x16x32_bf16 v[156:159], v[20:23], v[60:63], v[156:159]\n\t"
      "s_waitcnt lgkmcnt(12)\n\t"
      "v_mfma_f32_16x16x32_bf16 v[160:163], v[24:27], v[48:51], v[160:163]\n\t"
      "ds_read_b64_tr_b16 v[20:21], v1 offset:16392\n\t"
      "v_mfma_f32_16x16x32_bf16 v[164:167], v[24:27], v[52:55], v[164:167]\n\t"
      "ds_read_b64_tr_b16 v[22:23], v1 offset:16648\n\t"
      "v_mfma_f32_16x16x32_bf16 v[168:171], v[24:27], v[56:59], v[168:171]\n\t"
      "v_mfma_f32_16x16x32_bf16 v[172:175], v[24:27], v[60:63], v[172:175]\n\t"
      "s_waitcnt lgkmcnt(12)\n\t"
      "v_mfma_f32_16x16x32_bf16 v[176:179], v[28:31], v[48:51], v[176:179]\n\t"
      "ds_read_b64_tr_b16 v[24:25], v1 offset:16896\n\t"
      "v_mfma_f32_16x16x32_bf16 v[180:183], v[28:31], v[52:55], v[180:183]\n\t"
      "ds_read_b64_tr_b16 v[26:27], v1 offset:17152\n\t"
      "v_mfma_f32_16x16x32_bf16 v[184:187], v[28:31], v[56:59], v[184:187]\n\t"
      "v_mfma_f32_16x16x32_bf16 v[188:191], v[28:31], v[60:63], v[188:191]\n\t"
      "s_waitcnt lgkmcnt(12)\n\t"
      "v_mfma_f32_16x16x32_bf16 v[192:195], v[32:35], v[48:51], v[192:195]\n\t"
      "ds_read_b64_tr_b16 v[28:29], v1 offset:16904\n\t"
      "v_mfma_f32_16x16x32_bf16 v[196:199], v[32:35], v[52:55], v[196:199]\n\t"
      "ds_read_b64_tr_b16 v[30:31], v1 offset:17160\n\t"
      "v_mfma_f32_16x16x32_bf16 v[200:203], v[32:35], v[56:59], v[200:203]\n\t"
      "v_mfma_f32_16x16x32_bf16 v[204:207], v[32:35], v[60:63], v[204:207]\n\t"
      "s_waitcnt lgkmcnt(12)\n\t"
      "v_mfma_f32_16x16x32_bf16 v[208:211], v[36:39], v[48:51], v[208:211]\n\t"
      "ds_read_b64_tr_b16 v[32:33], v1 offset:17408\n\t"
      "v_mfma_f32_16x16x32_bf16 v[212:215], v[36:39], v[52:55], v[212:215]\n\t"
      "ds_read_b64_tr_b16 v[34:35], v1 offset:17664\n\t"
      "v_mfma_f32_16x16x32_bf16 v[216:219], v[36:39], v[56:59], v[216:219]\n\t"
      "v_mfma_f32_16x16x32_bf16 v[220:223], v[36:39], v[60:63], v[220:223]\n\t"
      "s_waitcnt lgkmcnt(12)\n\t"
      "v_mfma_f32_16x16x32_bf16 v[224:227], v[40:43], v[48:51], v[224:227]\n\t"
      "ds_read_b64_tr_b16 v[36:37], v1 offset:17416\n\t"
      "v_mfma_f32_16x16x32_bf16 v[228:231], v[40:43], v[52:55], v[228:231]\n\t"
      "ds_read_b64_tr_b16 v[38:39], v1 offset:17672\n\t"
      "v_mfma_f32_16x16x32_bf16 v[232:235], v[40:43], v[56:59], v[232:235]\n\t"
      "v_mfma_f32_16x16x32_bf16 v[236:239], v[40:43], v[60:63], v[236:239]\n\t"
      "s_waitcnt lgkmcnt(12)\n\t"
      "v_mfma_f32_16x16x32_bf16 v[240:243], v[44:47], v[48:51], v[240:243]\n\t"
      "ds_read_b64_tr_b16 v[40:41], v1 offset:17920\n\t"
      "v_mfma_f32_16x16x32_bf16 v[244:247], v[44:47], v[52:55], v[244:247]\n\t"
      "ds_read_b64_tr_b16 v[42:43], v1 offset:18176\n\t"
      "v_mfma_f32_16x16x32_bf16 v[248:251], v[44:47], v[56:59], v[248:251]\n\t"
      "v_mfma_f32_16x16x32_bf16 v[252:255], v[44:47], v[60:63], v[252:255]\n\t"
      "s_waitcnt vmcnt(12) lgkmcnt(12)\n\t"
      "v_mfma_f32_16x16x32_bf16 a[0:3], v[16:19], v[64:67], a[0:3]\n\t"
      "ds_read_b64_tr_b16 v[44:45], v1 offset:17928\n\t"
      "v_mfma_f32_16x16x32_bf16 a[4:7], v[16:19], v[68:71], a[4:7]\n\t"
      "ds_read_b64_tr_b16 v[46:47], v1 offset:18184\n\t"
      "v_mfma_f32_16x16x32_bf16 a[8:11], v[16:19], v[72:75], a[8:11]\n\t"
      "s_add_u32 s80, s80, 0x1000\n\t"
      "v_mfma_f32_16x16x32_bf16 a[12:15], v[16:19], v[76:79], a[12:15]\n\t"
      "buffer_load_dwordx4 v[48:51], v0, s[56:59], s80 offen sc1\n\t"
      "s_waitcnt lgkmcnt(12)\n\t"
      "v_mfma_f32_16x16x32_bf16 a[16:19], v[20:23], v[64:67], a[16:19]\n\t"
      "ds_read_b64_tr_b16 v[16:17], v2 offset:18432\n\t"
      "v_mfma_f32_16x16x32_bf16 a[20:23], v[20:23], v[68:71], a[20:23]\n\t"
      "ds_read_b64_tr_b16 v[18:19], v2 offset:18688\n\t"
      "v_mfma_f32_16x16x32_bf16 a[24:27], v[20:23], v[72:75], a[24:27]\n\t"
      "v_mfma_f32_16x16x32_bf16 a[28:31], v[20:23], v[76:79], a[28:31]\n\t"
      "buffer_load_dwordx4 v[52:55], v0, s[56:59], s80 offen offset:1024 sc1\n\t"
      "s_waitcnt lgkmcnt(12)\n\t"
      "v_mfma_f32_16x16x32_bf16 a[32:35], v[24:27], v[64:67], a[32:35]\n\t"
      "ds_read_b64_tr_b16 v[20:21], v3 offset:18432\n\t"
      "v_mfma_f32_16x16x32_bf16 a[36:39], v[24:27], v[68:71], a[36:39]\n\t"
      "ds_read_b64_tr_b16 v[22:23], v3 offset:18688\n\t"
      "v_mfma_f32_16x16x32_bf16 a[40:43], v[24:27], v[72:75], a[40:43]\n\t"
      "v_mfma_f32_16x16x32_bf16 a[44:47], v[24:27], v[76:79], a[44:47]\n\t"
      "buffer_load_dwordx4 v[56:59], v0, s[56:59], s80 offen offset:2048 sc1\n\t"
      "s_waitcnt lgkmcnt(12)\n\t"
      "v_mfma_f32_16x16x32_bf16 a[48:51], v[28:31], v[64:67], a[48:51]\n\t"
      "ds_read_b64_tr_b16 v[24:25], v2 offset:18944\n\t"
      "v_mfma_f32_16x16x32_bf16 a[52:55], v[28:31], v[68:71], a[52:55]\n\t"
      "ds_read_b64_tr_b16 v[26:27], v2 offset:19200\n\t"
      "v_mfma_f32_16x16x32_bf16 a[56:59], v[28:31], v[72:75], a[56:59]\n\t"
      "v_mfma_f32_16x16x32_bf16 a[60:63], v[28:31], v[76:79], a[60:63]\n\t"
      "buffer_load_dwordx4 v[60:63], v0, s[56:59], s80 offen offset:3072 sc1\n\t"
      "s_waitcnt lgkmcnt(12)\n\t"
      "v_mfma_f32_16x16x32_bf16 a[64:67], v[32:35], v[64:67], a[64:67]\n\t"
      "ds_read_b64_tr_b16 v[28:29], v3 offset:18944\n\t"
      "v_mfma_f32_16x16x32_bf16 a[68:71], v[32:35], v[68:71], a[68:71]\n\t"
      "ds_read_b64_tr_b16 v[30:31], v3 offset:19200\n\t"
      "v_mfma_f32_16x16x32_bf16 a[72:75], v[32:35], v[72:75], a[72:75]\n\t"
      "s_add_u32 m0, s83, 0x1000\n\t"
      "v_mfma_f32_16x16x32_bf16 a[76:79], v[32:35], v[76:79], a[76:79]\n\t"
      "buffer_load_dwordx4 v5, s[60:63], s81 offen lds\n\t"
      "s_waitcnt lgkmcnt(12)\n\t"
      "v_mfma_f32_16x16x32_bf16 a[80:83], v[36:39], v[64:67], a[80:83]\n\t"
      "ds_read_b64_tr_b16 v[32:33], v2 offset:19456\n\t"
      "v_mfma_f32_16x16x32_bf16 a[84:87], v[36:39], v[68:71], a[84:87]\n\t"
      "ds_read_b64_tr_b16 v[34:35], v2 offset:19712\n\t"
      "v_mfma_f32_16x16x32_bf16 a[88:91], v[36:39], v[72:75], a[88:91]\n\t"
      "s_add_u32 m0, s83, 0x1380\n\t"
      "v_mfma_f32_16x16x32_bf16 a[92:95], v[36:39], v[76:79], a[92:95]\n\t"
      "buffer_load_dwordx4 v5, s[60:63], s81 offen offset:128 lds\n\t"
      "s_waitcnt lgkmcnt(12)\n\t"
      "v_mfma_f32_16x16x32_bf16 a[96:99], v[40:43], v[64:67], a[96:99]\n\t"
      "ds_read_b64_tr_b16 v[36:37], v3 offset:19456\n\t"
      "v_mfma_f32_16x16x32_bf16 a[100:103], v[40:43], v[68:71], a[100:103]\n\t"
      "ds_read_b64_tr_b16 v[38:39], v3 offset:19712\n\t"
      "v_mfma_f32_16x16x32_bf16 a[104:107], v[40:43], v[72:75], a[104:107]\n\t"
      "s_add_u32 m0, s83, 0x1800\n\t"
      "v_mfma_f32_16x16x32_bf16 a[108:111], v[40:43], v[76:79], a[108:111]\n\t"
      "buffer_load_dwordx4 v7, s[64:67], s82 offen lds\n\t"
      "s_waitcnt lgkmcnt(12)\n\t"
      "v_mfma_f32_16x16x32_bf16 a[112:115], v[44:47], v[64:67], a[112:115]\n\t"
      "ds_read_b64_tr_b16 v[40:41], v2 offset:19968\n\t"
      "v_mfma_f32_16x16x32_bf16 a[116:119], v[44:47], v[68:71], a[116:119]\n\t"
      "ds_read_b64_tr_b16 v[42:43], v2 offset:20224\n\t"
      "v_mfma_f32_16x16x32_bf16 a[120:123], v[44:47], v[72:75], a[120:123]\n\t"
      "s_add_u32 m0, s83, 0x1c00\n\t"
      "v_mfma_f32_16x16x32_bf16 a[124:127], v[44:47], v[76:79], a[124:127]\n\t"
      "buffer_load_dwordx4 v7, s[68:71], s82 offen lds\n\t"
      "s_waitcnt lgkmcnt(12)\n\t"
      "v_mfma_f32_16x16x32_bf16 v[128:131], v[16:19], v[64:67], v[128:131]\n\t"
      "ds_read_b64_tr_b16 v[44:45], v3 offset:19968\n\t"
      "v_mfma_f32_16x16x32_bf16 v[132:135], v[16:19], v[68:71], v[132:135]\n\t"
      "ds_read_b64_tr_b16 v[46:47], v3 offset:20224\n\t"
      "v_mfma_f32_16x16x32_bf16 v[136:139], v[16:19], v[72:75], v[136:139]\n\t"
      "v_mfma_f32_16x16x32_bf16 v[140:143], v[16:19], v[76:79], v[140:143]\n\t"
      "s_waitcnt lgkmcnt(12)\n\t"
      "v_mfma_f32_16x16x32_bf16 v[144:147], v[20:23], v[64:67], v[144:147]\n\t"
      "v_mfma_f32_16x16x32_bf16 v[148:151], v[20:23], v[68:71], v[148:151]\n\t"
      "v_mfma_f32_16x16x32_bf16 v[152:155], v[20:23], v[72:75], v[152:155]\n\t"
      "v_mfma_f32_16x16x32_bf16 v[156:159], v[20:23], v[76:79], v[156:159]\n\t"
      "s_waitcnt lgkmcnt(10)\n\t"
      "v_mfma_f32_16x16x32_bf16 v[160:163], v[24:27], v[64:67], v[160:163]\n\t"
      "v_mfma_f32_16x16x32_bf16 v[164:167], v[24:27], v[68:71], v[164:167]\n\t"
      "v_mfma_f32_16x16x32_bf16 v[168:171], v[24:27], v[72:75], v[168:171]\n\t"
      "v_mfma_f32_16x16x32_bf16 v[172:175], v[24:27], v[76:79], v[172:175]\n\t"
      "s_waitcnt vmcnt(16) lgkmcnt(0)\n\t"
      "s_barrier\n\t"
      "s_add_u32 s98, s98, 0x8000\n\t"
      "s_cmp_ge_u32 s98, 0x18000\n\t"
      "s_cselect_b32 s97, 0xffff0000, s44\n\t"
      "s_cselect_b32 s98, 0, s98\n\t"
      "v_add_u32 v1, s97, v1\n\t"
      "v_add_u32 v2, s97, v2\n\t"
      "v_add_u32 v3, s97, v3\n\t"
      "v_mfma_f32_16x16x32_bf16 v[176:179], v[28:31], v[64:67], v[176:179]\n\t"
      "ds_read_b64_tr_b16 v[16:17], v1\n\t"
      "v_mfma_f32_16x16x32_bf16 v[180:183], v[28:31], v[68:71], v[180:183]\n\t"
      "ds_read_b64_tr_b16 v[18:19], v1 offset:256\n\t"
      "v_mfma_f32_16x16x32_bf16 v[184:187], v[28:31], v[72:75], v[184:187]\n\t"
      "v_mfma_f32_16x16x32_bf16 v[188:191], v[28:31], v[76:79], v[188:191]\n\t"
      "v_mfma_f32_16x16x32_bf16 v[192:195], v[32:35], v[64:67], v[192:195]\n\t"
      "ds_read_b64_tr_b16 v[20:21], v1 offset:8\n\t"
      "v_mfma_f32_16x16x32_bf16 v[196:199], v[32:35], v[68:71], v[196:199]\n\t"
      "ds_read_b64_tr_b16 v[22:23], v1 offset:264\n\t"
      "v_mfma_f32_16x16x32_bf16 v[200:203], v[32:35], v[72:75], v[200:203]\n\t"
      "ds_read_b64_tr_b16 v[24:25], v1 offset:512\n\t"
      "v_mfma_f32_16x16x32_bf16 v[204:207], v[32:35], v[76:79], v[204:207]\n\t"
      "ds_read_b64_tr_b16 v[26:27], v1 offset:768\n\t"
      "v_mfma_f32_16x16x32_bf16 v[208:211], v[36:39], v[64:67], v[208:211]\n\t"
      "ds_read_b64_tr_b16 v[28:29], v1 offset:520\n\t"
      "v_mfma_f32_16x16x32_bf16 v[212:215], v[36:39], v[68:71], v[212:215]\n\t"
      "ds_read_b64_tr_b16 v[30:31], v1 offset:776\n\t"
      "v_mfma_f32_16x16x32_bf16 v[216:219], v[36:39], v[72:75], v[216:219]\n\t"
      "ds_read_b64_tr_b16 v[32:33], v1 offset:1024\n\t"
      "v_mfma_f32_16x16x32_bf16 v[220:223], v[36:39], v[76:79], v[220:223]\n\t"
      "ds_read_b64_tr_b16 v[34:35], v1 offset:1280\n\t"
      "v_mfma_f32_16x16x32_bf16 v[224:227], v[40:43], v[64:67], v[224:227]\n\t"
      "ds_read_b64_tr_b16 v[36:37], v1 offset:1032\n\t"
      "v_mfma_f32_16x16x32_bf16 v[228:231], v[40:43], v[68:71], v[228:231]\n\t"
      "ds_read_b64_tr_b16 v[38:39], v1 offset:1288\n\t"
      "v_mfma_f32_16x16x32_bf16 v[232:235], v[40:43], v[72:75], v[232:235]\n\t"
      "v_mfma_f32_16x16x32_bf16 v[236:239], v[40:43], v[76:79], v[236:239]\n\t"
      "v_mfma_f32_16x16x32_bf16 v[240:243], v[44:47], v[64:67], v[240:243]\n\t"
      "ds_read_b64_tr_b16 v[40:41], v1 offset:1536\n\t"
      "v_mfma_f32_16x16x32_bf16 v[244:247], v[44:47], v[68:71], v[244:247]\n\t"
      "ds_read_b64_tr_b16 v[42:43], v1 offset:1792\n\t"
      "v_mfma_f32_16x16x32_bf16 v[248:251], v[44:47], v[72:75], v[248:251]\n\t"
      "v_mfma_f32_16x16x32_bf16 v[252:255], v[44:47], v[76:79], v[252:255]\n\t"
      "s_waitcnt vmcnt(16) lgkmcnt(12)\n\t"
      "v_mfma_f32_16x16x32_bf16 a[0:3], v[16:19], v[80:83], a[0:3]\n\t"
      "ds_read_b64_tr_b16 v[44:45], v1 offset:1544\n\t"
      "v_mfma_f32_16x16x32_bf16 a[4:7], v[16:19], v[84:87], a[4:7]\n\t"
      "ds_read_b64_tr_b16 v[46:47], v1 offset:1800\n\t"
      "v_mfma_f32_16x16x32_bf16 a[8:11], v[16:19], v[88:91], a[8:11]\n\t"
      "v_mfma_f32_16x16x32_bf16 a[12:15], v[16:19], v[92:95], a[12:15]\n\t"
      "s_waitcnt lgkmcnt(12)\n\t"
      "v_mfma_f32_16x16x32_bf16 a[16:19], v[20:23], v[80:83], a[16:19]\n\t"
      "ds_read_b64_tr_b16 v[16:17], v2 offset:2048\n\t"
      "v_mfma_f32_16x16x32_bf16 a[20:23], v[20:23], v[84:87], a[20:23]\n\t"
      "ds_read_b64_tr_b16 v[18:19], v2 offset:2304\n\t"
      "v_mfma_f32_16x16x32_bf16 a[24:27], v[20:23], v[88:91], a[24:27]\n\t"
      "s_add_u32 s80, s80, 0x1000\n\t"
      "v_mfma_f32_16x16x32_bf16 a[28:31], v[20:23], v[92:95], a[28:31]\n\t"
      "buffer_load_dwordx4 v[64:67], v0, s[56:59], s80 offen sc1\n\t"
      "s_waitcnt lgkmcnt(12)\n\t"
      "v_mfma_f32_16x16x32_bf16 a[32:35], v[24:27], v[80:83], a[32:35]\n\t"
      "ds_read_b64_tr_b16 v[20:21], v3 offset:2048\n\t"
      "v_mfma_f32_16x16x32_bf16 a[36:39], v[24:27], v[84:87], a[36:39]\n\t"
      "ds_read_b64_tr_b16 v[22:23], v3 offset:2304\n\t"
      "v_mfma_f32_16x16x32_bf16 a[40:43], v[24:27], v[88:91], a[40:43]\n\t"
      "v_mfma_f32_16x16x32_bf16 a[44:47], v[24:27], v[92:95], a[44:47]\n\t"
      "buffer_load_dwordx4 v[68:71], v0, s[56:59], s80 offen offset:1024 sc1\n\t"
      "s_waitcnt lgkmcnt(12)\n\t"
      "v_mfma_f32_16x16x32_bf16 a[48:51], v[28:31], v[80:83], a[48:51]\n\t"
      "ds_read_b64_tr_b16 v[24:25], v2 offset:2560\n\t"
      "v_mfma_f32_16x16x32_bf16 a[52:55], v[28:31], v[84:87], a[52:55]\n\t"
      "ds_read_b64_tr_b16 v[26:27], v2 offset:2816\n\t"
      "v_mfma_f32_16x16x32_bf16 a[56:59], v[28:31], v[88:91], a[56:59]\n\t"
      "v_mfma_f32_16x16x32_bf16 a[60:63], v[28:31], v[92:95], a[60:63]\n\t"
      "buffer_load_dwordx4 v[72:75], v0, s[56:59], s80 offen offset:2048 sc1\n\t"
      "s_waitcnt lgkmcnt(12)\n\t"
      "v_mfma_f32_16x16x32_bf16 a[64:67], v[32:35], v[80:83], a[64:67]\n\t"
      "ds_read_b64_tr_b16 v[28:29], v3 offset:2560\n\t"
      "v_mfma_f32_16x16x32_bf16 a[68:71], v[32:35], v[84:87], a[68:71]\n\t"
      "ds_read_b64_tr_b16 v[30:31], v3 offset:2816\n\t"
      "v_mfma_f32_16x16x32_bf16 a[72:75], v[32:35], v[88:91], a[72:75]\n\t"
      "v_mfma_f32_16x16x32_bf16 a[76:79], v[32:35], v[92:95], a[76:79]\n\t"
      "buffer_load_dwordx4 v[76:79], v0, s[56:59], s80 offen offset:3072 sc1\n\t"
      "s_waitcnt lgkmcnt(12)\n\t"
      "v_mfma_f32_16x16x32_bf16 a[80:83], v[36:39], v[80:83], a[80:83]\n\t"
      "ds_read_b64_tr_b16 v[32:33], v2 offset:3072\n\t"
      "v_mfma_f32_16x16x32_bf16 a[84:87], v[36:39], v[84:87], a[84:87]\n\t"
      "ds_read_b64_tr_b16 v[34:35], v2 offset:3328\n\t"
      "v_mfma_f32_16x16x32_bf16 a[88:91], v[36:39], v[88:91], a[88:91]\n\t"
      "s_add_u32 s81, s81, 0x80000\n\t"
      "s_add_u32 s82, s82, 0x36000\n\t"
      "s_add_u32 s83, s83, 0x8000\n\t"
      "s_cmp_ge_u32 s83, s94\n\t"
      "s_cselect_b32 s93, 0x18000, 0\n\t"
      "s_sub_u32 s83, s83, s93\n\t"
      "s_mov_b32 m0, s83\n\t"
      "v_mfma_f32_16x16x32_bf16 a[92:95], v[36:39], v[92:95], a[92:95]\n\t"
      "buffer_load_dwordx4 v4, s[60:63], s81 offen lds\n\t"
      "s_waitcnt lgkmcnt(12)\n\t"
      "v_mfma_f32_16x16x32_bf16 a[96:99], v[40:43], v[80:83], a[96:99]\n\t"
      "ds_read_b64_tr_b16 v[36:37], v3 offset:3072\n\t"
      "v_mfma_f32_16x16x32_bf16 a[100:103], v[40:43], v[84:87], a[100:103]\n\t"
      "ds_read_b64_tr_b16 v[38:39], v3 offset:3328\n\t"
      "v_mfma_f32_16x16x32_bf16 a[104:107], v[40:43], v[88:91], a[104:107]\n\t"
      "s_add_u32 m0, s83, 0x380\n\t"
      "v_mfma_f32_16x16x32_bf16 a[108:111], v[40:43], v[92:95], a[108:111]\n\t"
      "buffer_load_dwordx4 v4, s[60:63], s81 offen offset:128 lds\n\t"
      "s_waitcnt lgkmcnt(12)\n\t"
      "v_mfma_f32_16x16x32_bf16 a[112:115], v[44:47], v[80:83], a[112:115]\n\t"
      "ds_read_b64_tr_b16 v[40:41], v2 offset:3584\n\t"
      "v_mfma_f32_16x16x32_bf16 a[116:119], v[44:47], v[84:87], a[116:119]\n\t"
      "ds_read_b64_tr_b16 v[42:43], v2 offset:3840\n\t"
      "v_mfma_f32_16x16x32_bf16 a[120:123], v[44:47], v[88:91], a[120:123]\n\t"
      "s_add_u32 m0, s83, 0x800\n\t"
      "v_mfma_f32_16x16x32_bf16 a[124:127], v[44:47], v[92:95], a[124:127]\n\t"
      "buffer_load_dwordx4 v6, s[64:67], s82 offen lds\n\t"
      "s_waitcnt lgkmcnt(12)\n\t"
      "v_mfma_f32_16x16x32_bf16 v[128:131], v[16:19], v[80:83], v[128:131]\n\t"
      "ds_read_b64_tr_b16 v[44:45], v3 offset:3584\n\t"
      "v_mfma_f32_16x16x32_bf16 v[132:135], v[16:19], v[84:87], v[132:135]\n\t"
      "ds_read_b64_tr_b16 v[46:47], v3 offset:3840\n\t"
      "v_mfma_f32_16x16x32_bf16 v[136:139], v[16:19], v[88:91], v[136:139]\n\t"
      "s_add_u32 m0, s83, 0xc00\n\t"
      "v_mfma_f32_16x16x32_bf16 v[140:143], v[16:19], v[92:95], v[140:143]\n\t"
      "buffer_load_dwordx4 v6, s[68:71], s82 offen lds\n\t"
      "s_waitcnt lgkmcnt(12)\n\t"
      "v_mfma_f32_16x16x32_bf16 v[144:147], v[20:23], v[80:83], v[144:147]\n\t"
      "ds_read_b64_tr_b16 v[16:17], v1 offset:16384\n\t"
      "v_mfma_f32_16x16x32_bf16 v[148:151], v[20:23], v[84:87], v[148:151]\n\t"
      "ds_read_b64_tr_b16 v[18:19], v1 offset:16640\n\t"
      "v_mfma_f32_16x16x32_bf16 v[152:155], v[20:23], v[88:91], v[152:155]\n\t"
      "v_mfma_f32_16x16x32_bf16 v[156:159], v[20:23], v[92:95], v[156:159]\n\t"
      "s_waitcnt lgkmcnt(12)\n\t"
      "v_mfma_f32_16x16x32_bf16 v[160:163], v[24:27], v[80:83], v[160:163]\n\t"
      "ds_read_b64_tr_b16 v[20:21], v1 offset:16392\n\t"
      "v_mfma_f32_16x16x32_bf16 v[164:167], v[24:27], v[84:87], v[164:167]\n\t"
      "ds_read_b64_tr_b16 v[22:23], v1 offset:16648\n\t"
      "v_mfma_f32_16x16x32_bf16 v[168:171], v[24:27], v[88:91], v[168:171]\n\t"
      "v_mfma_f32_16x16x32_bf16 v[172:175], v[24:27], v[92:95], v[172:175]\n\t"
      "s_waitcnt lgkmcnt(12)\n\t"
      "v_mfma_f32_16x16x32_bf16 v[176:179], v[28:31], v[80:83], v[176:179]\n\t"
      "ds_read_b64_tr_b16 v[24:25], v1 offset:16896\n\t"
      "v_mfma_f32_16x16x32_bf16 v[180:183], v[28:31], v[84:87], v[180:183]\n\t"
      "ds_read_b64_tr_b16 v[26:27], v1 offset:17152\n\t"
      "v_mfma_f32_16x16x32_bf16 v[184:187], v[28:31], v[88:91], v[184:187]\n\t"
      "v_mfma_f32_16x16x32_bf16 v[188:191], v[28:31], v[92:95], v[188:191]\n\t"
      "s_waitcnt lgkmcnt(12)\n\t"
      "v_mfma_f32_16x16x32_bf16 v[192:195], v[32:35], v[80:83], v[192:195]\n\t"
      "ds_read_b64_tr_b16 v[28:29], v1 offset:16904\n\t"
      "v_mfma_f32_16x16x32_bf16 v[196:199], v[32:35], v[84:87], v[196:199]\n\t"
      "ds_read_b64_tr_b16 v[30:31], v1 offset:17160\n\t"
      "v_mfma_f32_16x16x32_bf16 v[200:203], v[32:35], v[88:91], v[200:203]\n\t"
      "v_mfma_f32_16x16x32_bf16 v[204:207], v[32:35], v[92:95], v[204:207]\n\t"
      "s_waitcnt lgkmcnt(12)\n\t"
      "v_mfma_f32_16x16x32_bf16 v[208:211], v[36:39], v[80:83], v[208:211]\n\t"
      "ds_read_b64_tr_b16 v[32:33], v1 offset:17408\n\t"
      "v_mfma_f32_16x16x32_bf16 v[212:215], v[36:39], v[84:87], v[212:215]\n\t"
      "ds_read_b64_tr_b16 v[34:35], v1 offset:17664\n\t"
      "v_mfma_f32_16x16x32_bf16 v[216:219], v[36:39], v[88:91], v[216:219]\n\t"
      "v_mfma_f32_16x16x32_bf16 v[220:223], v[36:39], v[92:95], v[220:223]\n\t"
      "s_waitcnt lgkmcnt(12)\n\t"
      "v_mfma_f32_16x16x32_bf16 v[224:227], v[40:43], v[80:83], v[224:227]\n\t"
      "ds_read_b64_tr_b16 v[36:37], v1 offset:17416\n\t"
      "v_mfma_f32_16x16x32_bf16 v[228:231], v[40:43], v[84:87], v[228:231]\n\t"
      "ds_read_b64_tr_b16 v[38:39], v1 offset:17672\n\t"
      "v_mfma_f32_16x16x32_bf16 v[232:235], v[40:43], v[88:91], v[232:235]\n\t"
      "v_mfma_f32_16x16x32_bf16 v[236:239], v[40:43], v[92:95], v[236:239]\n\t"
      "s_waitcnt lgkmcnt(12)\n\t"
      "v_mfma_f32_16x16x32_bf16 v[240:243], v[44:47], v[80:83], v[240:243]\n\t"
      "ds_read_b64_tr_b16 v[40:41], v1 offset:17920\n\t"
      "v_mfma_f32_16x16x32_bf16 v[244:247], v[44:47], v[84:87], v[244:247]\n\t"
      "ds_read_b64_tr_b16 v[42:43], v1 offset:18176\n\t"
      "v_mfma_f32_16x16x32_bf16 v[248:251], v[44:47], v[88:91], v[248:251]\n\t"
      "v_mfma_f32_16x16x32_bf16 v[252:255], v[44:47], v[92:95], v[252:255]\n\t"
      "s_waitcnt vmcnt(20) lgkmcnt(12)\n\t"
      "v_mfma_f32_16x16x32_bf16 a[0:3], v[16:19], v[96:99], a[0:3]\n\t"
      "ds_read_b64_tr_b16 v[44:45], v1 offset:17928\n\t"
      "v_mfma_f32_16x16x32_bf16 a[4:7], v[16:19], v[100:103], a[4:7]\n\t"
      "ds_read_b64_tr_b16 v[46:47], v1 offset:18184\n\t"
      "v_mfma_f32_16x16x32_bf16 a[8:11], v[16:19], v[104:107], a[8:11]\n\t"
      "s_add_u32 s80, s80, 0x1000\n\t"
      "v_mfma_f32_16x16x32_bf16 a[12:15], v[16:19], v[108:111], a[12:15]\n\t"
      "buffer_load_dwordx4 v[80:83], v0, s[56:59], s80 offen sc1\n\t"
      "s_waitcnt lgkmcnt(12)\n\t"
      "v_mfma_f32_16x16x32_bf16 a[16:19], v[20:23], v[96:99], a[16:19]\n\t"
      "ds_read_b64_tr_b16 v[16:17], v2 offset:18432\n\t"
      "v_mfma_f32_16x16x32_bf16 a[20:23], v[20:23], v[100:103], a[20:23]\n\t"
      "ds_read_b64_tr_b16 v[18:19], v2 offset:18688\n\t"
      "v_mfma_f32_16x16x32_bf16 a[24:27], v[20:23], v[104:107], a[24:27]\n\t"
      "v_mfma_f32_16x16x32_bf16 a[28:31], v[20:23], v[108:111], a[28:31]\n\t"
      "buffer_load_dwordx4 v[84:87], v0, s[56:59], s80 offen offset:1024 sc1\n\t"
      "s_waitcnt lgkmcnt(12)\n\t"
      "v_mfma_f32_16x16x32_bf16 a[32:35], v[24:27], v[96:99], a[32:35]\n\t"
      "ds_read_b64_tr_b16 v[20:21], v3 offset:18432\n\t"
      "v_mfma_f32_16x16x32_bf16 a[36:39], v[24:27], v[100:103], a[36:39]\n\t"
      "ds_read_b64_tr_b16 v[22:23], v3 offset:18688\n\t"
      "v_mfma_f32_16x16x32_bf16 a[40:43], v[24:27], v[104:107], a[40:43]\n\t"
      "v_mfma_f32_16x16x32_bf16 a[44:47], v[24:27], v[108:111], a[44:47]\n\t"
      "buffer_load_dwordx4 v[88:91], v0, s[56:59], s80 offen offset:2048 sc1\n\t"
      "s_waitcnt lgkmcnt(12)\n\t"
      "v_mfma_f32_16x16x32_bf16 a[48:51], v[28:31], v[96:99], a[48:51]\n\t"
      "ds_read_b64_tr_b16 v[24:25], v2 offset:18944\n\t"
      "v_mfma_f32_16x16x32_bf16 a[52:55], v[28:31], v[100:103], a[52:55]\n\t"
      "ds_read_b64_tr_b16 v[26:27], v2 offset:19200\n\t"
      "v_mfma_f32_16x16x32_bf16 a[56:59], v[28:31], v[104:107], a[56:59]\n\t"
      "v_mfma_f32_16x16x32_bf16 a[60:63], v[28:31], v[108:111], a[60:63]\n\t"
      "buffer_load_dwordx4 v[92:95], v0, s[56:59], s80 offen offset:3072 sc1\n\t"
      "s_waitcnt lgkmcnt(12)\n\t"
      "v_mfma_f32_16x16x32_bf16 a[64:67], v[32:35], v[96:99], a[64:67]\n\t"
      "ds_read_b64_tr_b16 v[28:29], v3 offset:18944\n\t"
      "v_mfma_f32_16x16x32_bf16 a[68:71], v[32:35], v[100:103], a[68:71]\n\t"
      "ds_read_b64_tr_b16 v[30:31], v3 offset:19200\n\t"
      "v_mfma_f32_16x16x32_bf16 a[72:75], v[32:35], v[104:107], a[72:75]\n\t"
      "s_add_u32 m0, s83, 0x1000\n\t"
      "v_mfma_f32_16x16x32_bf16 a[76:79], v[32:35], v[108:111], a[76:79]\n\t"
      "buffer_load_dwordx4 v5, s[60:63], s81 offen lds\n\t"
      "s_waitcnt lgkmcnt(12)\n\t"
      "v_mfma_f32_16x16x32_bf16 a[80:83], v[36:39], v[96:99], a[80:83]\n\t"
      "ds_read_b64_tr_b16 v[32:33], v2 offset:19456\n\t"
      "v_mfma_f32_16x16x32_bf16 a[84:87], v[36:39], v[100:103], a[84:87]\n\t"
      "ds_read_b64_tr_b16 v[34:35], v2 offset:19712\n\t"
      "v_mfma_f32_16x16x32_bf16 a[88:91], v[36:39], v[104:107], a[88:91]\n\t"
      "s_add_u32 m0, s83, 0x1380\n\t"
      "v_mfma_f32_16x16x32_bf16 a[92:95], v[36:39], v[108:111], a[92:95]\n\t"
      "buffer_load_dwordx4 v5, s[60:63], s81 offen offset:128 lds\n\t"
      "s_waitcnt lgkmcnt(12)\n\t"
      "v_mfma_f32_16x16x32_bf16 a[96:99], v[40:43], v[96:99], a[96:99]\n\t"
      "ds_read_b64_tr_b16 v[36:37], v3 offset:19456\n\t"
      "v_mfma_f32_16x16x32_bf16 a[100:103], v[40:43], v[100:103], a[100:103]\n\t"
      "ds_read_b64_tr_b16 v[38:39], v3 offset:19712\n\t"
      "v_mfma_f32_16x16x32_bf16 a[104:107], v[40:43], v[104:107], a[104:107]\n\t"
      "s_add_u32 m0, s83, 0x1800\n\t"
      "v_mfma_f32_16x16x32_bf16 a[108:111], v[40:43], v[108:111], a[108:111]\n\t"
      "buffer_load_dwordx4 v7, s[64:67], s82 offen lds\n\t"
      "s_waitcnt lgkmcnt(12)\n\t"
      "v_mfma_f32_16x16x32_bf16 a[112:115], v[44:47], v[96:99], a[112:115]\n\t"
      "ds_read_b64_tr_b16 v[40:41], v2 offset:19968\n\t"
      "v_mfma_f32_16x16x32_bf16 a[116:119], v[44:47], v[100:103], a[116:119]\n\t"
      "ds_read_b64_tr_b16 v[42:43], v2 offset:20224\n\t"
      "v_mfma_f32_16x16x32_bf16 a[120:123], v[44:47], v[104:107], a[120:123]\n\t"
      "s_add_u32 m0, s83, 0x1c00\n\t"
      "v_mfma_f32_16x16x32_bf16 a[124:127], v[44:47], v[108:111], a[124:127]\n\t"
      "buffer_load_dwordx4 v7, s[68:71], s82 offen lds\n\t"
      "s_waitcnt lgkmcnt(12)\n\t"
      "v_mfma_f32_16x16x32_bf16 v[128:131], v[16:19], v[96:99], v[128:131]\n\t"
      "ds_read_b64_tr_b16 v[44:45], v3 offset:19968\n\t"
      "v_mfma_f32_16x16x32_bf16 v[132:135], v[16:19], v[100:103], v[132:135]\n\t"
      "ds_read_b64_tr_b16 v[46:47], v3 offset:20224\n\t"
      "v_mfma_f32_16x16x32_bf16 v[136:139], v[16:19], v[104:107], v[136:139]\n\t"
      "v_mfma_f32_16x16x32_bf16 v[140:143], v[16:19], v[108:111], v[140:143]\n\t"
      "s_waitcnt lgkmcnt(12)\n\t"
      "v_mfma_f32_16x16x32_bf16 v[144:147], v[20:23], v[96:99], v[144:147]\n\t"
      "v_mfma_f32_16x16x32_bf16 v[148:151], v[20:23], v[100:103], v[148:151]\n\t"
      "v_mfma_f32_16x16x32_bf16 v[152:155], v[20:23], v[104:107], v[152:155]\n\t"
      "v_mfma_f32_16x16x32_bf16 v[156:159], v[20:23], v[108:111], v[156:159]\n\t"
      "s_waitcnt lgkmcnt(10)\n\t"
      "v_mfma_f32_16x16x32_bf16 v[160:163], v[24:27], v[96:99], v[160:163]\n\t"
      "v_mfma_f32_16x16x32_bf16 v[164:167], v[24:27], v[100:103], v[164:167]\n\t"
      "v_mfma_f32_16x16x32_bf16 v[168:171], v[24:27], v[104:107], v[168:171]\n\t"
      "v_mfma_f32_16x16x32_bf16 v[172:175], v[24:27], v[108:111], v[172:175]\n\t"
      "s_waitcnt vmcnt(16) lgkmcnt(0)\n\t"
      "s_barrier\n\t"
      "s_add_u32 s98, s98, 0x8000\n\t"
      "s_cmp_ge_u32 s98, 0x18000\n\t"
      "s_cselect_b32 s97, 0xffff0000, s44\n\t"
      "s_cselect_b32 s98, 0, s98\n\t"
      "v_add_u32 v1, s97, v1\n\t"
      "v_add_u32 v2, s97, v2\n\t"
      "v_add_u32 v3, s97, v3\n\t"
      "v_mfma_f32_16x16x32_bf16 v[176:179], v[28:31], v[96:99], v[176:179]\n\t"
      "ds_read_b64_tr_b16 v[16:17], v1\n\t"
      "v_mfma_f32_16x16x32_bf16 v[180:183], v[28:31], v[100:103], v[180:183]\n\t"
      "ds_read_b64_tr_b16 v[18:19], v1 offset:256\n\t"
      "v_mfma_f32_16x16x32_bf16 v[184:187], v[28:31], v[104:107], v[184:187]\n\t"
      "v_mfma_f32_16x16x32_bf16 v[188:191], v[28:31], v[108:111], v[188:191]\n\t"
      "v_mfma_f32_16x16x32_bf16 v[192:195], v[32:35], v[96:99], v[192:195]\n\t"
      "ds_read_b64_tr_b16 v[20:21], v1 offset:8\n\t"
      "v_mfma_f32_16x16x32_bf16 v[196:199], v[32:35], v[100:103], v[196:199]\n\t"
      "ds_read_b64_tr_b16 v[22:23], v1 offset:264\n\t"
      "v_mfma_f32_16x16x32_bf16 v[200:203], v[32:35], v[104:107], v[200:203]\n\t"
      "ds_read_b64_tr_b16 v[24:25], v1 offset:512\n\t"
      "v_mfma_f32_16x16x32_bf16 v[204:207], v[32:35], v[108:111], v[204:207]\n\t"
      "ds_read_b64_tr_b16 v[26:27], v1 offset:768\n\t"
      "v_mfma_f32_16x16x32_bf16 v[208:211], v[36:39], v[96:99], v[208:211]\n\t"
      "ds_read_b64_tr_b16 v[28:29], v1 offset:520\n\t"
      "v_mfma_f32_16x16x32_bf16 v[212:215], v[36:39], v[100:103], v[212:215]\n\t"
      "ds_read_b64_tr_b16 v[30:31], v1 offset:776\n\t"
      "v_mfma_f32_16x16x32_bf16 v[216:219], v[36:39], v[104:107], v[216:219]\n\t"
      "ds_read_b64_tr_b16 v[32:33], v1 offset:1024\n\t"
      "v_mfma_f32_16x16x32_bf16 v[220:223], v[36:39], v[108:111], v[220:223]\n\t"
      "ds_read_b64_tr_b16 v[34:35], v1 offset:1280\n\t"
      "v_mfma_f32_16x16x32_bf16 v[224:227], v[40:43], v[96:99], v[224:227]\n\t"
      "ds_read_b64_tr_b16 v[36:37], v1 offset:1032\n\t"
      "v_mfma_f32_16x16x32_bf16 v[228:231], v[40:43], v[100:103], v[228:231]\n\t"
      "ds_read_b64_tr_b16 v[38:39], v1 offset:1288\n\t"
      "v_mfma_f32_16x16x32_bf16 v[232:235], v[40:43], v[104:107], v[232:235]\n\t"
      "v_mfma_f32_16x16x32_bf16 v[236:239], v[40:43], v[108:111], v[236:239]\n\t"
      "v_mfma_f32_16x16x32_bf16 v[240:243], v[44:47], v[96:99], v[240:243]\n\t"
      "ds_read_b64_tr_b16 v[40:41], v1 offset:1536\n\t"
      "v_mfma_f32_16x16x32_bf16 v[244:247], v[44:47], v[100:103], v[244:247]\n\t"
      "ds_read_b64_tr_b16 v[42:43], v1 offset:1792\n\t"
      "v_mfma_f32_16x16x32_bf16 v[248:251], v[44:47], v[104:107], v[248:251]\n\t"
      "v_mfma_f32_16x16x32_bf16 v[252:255], v[44:47], v[108:111], v[252:255]\n\t"
      "s_sub_u32 s92, s92, 1\n\t"
      "s_cmp_lg_u32 s92, 0\n\t"
      "s_cbranch_scc1 K1proj\n\t"
      "s_getpc_b64 s[90:91]\n\t"
      "91:\n\t"
      "s_lshl_b32 s93, s99, 13\n\t"
      "s_add_u32 s93, s93, K1common-91b-8192\n\t"
      "s_cmp_eq_u32 s99, 0\n\t"
      "s_cselect_b32 s93, 0, s93\n\t"
      "v_lshl_add_u32 v14, v0, 3, s93\n\t"
      "v_min_u32 v14, K9end-91b-4, v14\n\t"
      "global_load_dword v15, v14, s[90:91]\n\t"
      "s_waitcnt lgkmcnt(12)\n\t"
      "v_mfma_f32_16x16x32_bf16 a[0:3], v[16:19], v[48:51], a[0:3]\n\t"
      "ds_read_b64_tr_b16 v[44:45], v1 offset:1544\n\t"
      "v_mfma_f32_16x16x32_bf16 a[4:7], v[16:19], v[52:55], a[4:7]\n\t"
      "ds_read_b64_tr_b16 v[46:47], v1 offset:1800\n\t"
      "v_mfma_f32_16x16x32_bf16 a[8:11], v[16:19], v[56:59], a[8:11]\n\t"
      "v_mfma_f32_16x16x32_bf16 a[12:15], v[16:19], v[60:63], a[12:15]\n\t"
      "s_waitcnt lgkmcnt(12)\n\t"
      "v_mfma_f32_16x16x32_bf16 a[16:19], v[20:23], v[48:51], a[16:19]\n\t"
      "ds_read_b64_tr_b16 v[16:17], v2 offset:2048\n\t"
      "v_mfma_f32_16x16x32_bf16 a[20:23], v[20:23], v[52:55], a[20:23]\n\t"
      "ds_read_b64_tr_b16 v[18:19], v2 offset:2304\n\t"
      "v_mfma_f32_16x16x32_bf16 a[24:27], v[20:23], v[56:59], a[24:27]\n\t"
      "s_add_u32 s80, s80, 0x1000\n\t"
      "v_mfma_f32_16x16x32_bf16 a[28:31], v[20:23], v[60:63], a[28:31]\n\t"
      "buffer_load_dwordx4 v[96:99], v0, s[56:59], s80 offen sc1\n\t"
      "s_waitcnt lgkmcnt(12)\n\t"
      "v_mfma_f32_16x16x32_bf16 a[32:35], v[24:27], v[48:51], a[32:35]\n\t"
      "ds_read_b64_tr_b16 v[20:21], v3 offset:2048\n\t"
      "v_mfma_f32_16x16x32_bf16 a[36:39], v[24:27], v[52:55], a[36:39]\n\t"
      "ds_read_b64_tr_b16 v[22:23], v3 offset:2304\n\t"
      "v_mfma_f32_16x16x32_bf16 a[40:43], v[24:27], v[56:59], a[40:43]\n\t"
      "v_mfma_f32_16x16x32_bf16 a[44:47], v[24:27], v[60:63], a[44:47]\n\t"
      "buffer_load_dwordx4 v[100:103], v0, s[56:59], s80 offen offset:1024 sc1\n\t"
      "s_waitcnt lgkmcnt(12)\n\t"
      "v_mfma_f32_16x16x32_bf16 a[48:51], v[28:31], v[48:51], a[48:51]\n\t"
      "ds_read_b64_tr_b16 v[24:25], v2 offset:2560\n\t"
      "v_mfma_f32_16x16x32_bf16 a[52:55], v[28:31], v[52:55], a[52:55]\n\t"
      "ds_read_b64_tr_b16 v[26:27], v2 offset:2816\n\t"
      "v_mfma_f32_16x16x32_bf16 a[56:59], v[28:31], v[56:59], a[56:59]\n\t"
      "v_mfma_f32_16x16x32_bf16 a[60:63], v[28:31], v[60:63], a[60:63]\n\t"
      "buffer_load_dwordx4 v[104:107], v0, s[56:59], s80 offen offset:2048 sc1\n\t"
      "s_waitcnt lgkmcnt(12)\n\t"
      "v_mfma_f32_16x16x32_bf16 a[64:67], v[32:35], v[48:51], a[64:67]\n\t"
      "ds_read_b64_tr_b16 v[28:29], v3 offset:2560\n\t"
      "v_mfma_f32_16x16x32_bf16 a[68:71], v[32:35], v[52:55], a[68:71]\n\t"
      "ds_read_b64_tr_b16 v[30:31], v3 offset:2816\n\t"
      "v_mfma_f32_16x16x32_bf16 a[72:75], v[32:35], v[56:59], a[72:75]\n\t"
      "v_mfma_f32_16x16x32_bf16 a[76:79], v[32:35], v[60:63], a[76:79]\n\t"
      "buffer_load_dwordx4 v[108:111], v0, s[56:59], s80 offen offset:3072 sc1\n\t"
      "s_waitcnt lgkmcnt(12)\n\t"
      "v_mfma_f32_16x16x32_bf16 a[80:83], v[36:39], v[48:51], a[80:83]\n\t"
      "ds_read_b64_tr_b16 v[32:33], v2 offset:3072\n\t"
      "v_mfma_f32_16x16x32_bf16 a[84:87], v[36:39], v[52:55], a[84:87]\n\t"
      "ds_read_b64_tr_b16 v[34:35], v2 offset:3328\n\t"
      "v_mfma_f32_16x16x32_bf16 a[88:91], v[36:39], v[56:59], a[88:91]\n\t"
      "s_mov_b32 m0, s96\n\t"
      "v_mfma_f32_16x16x32_bf16 a[92:95], v[36:39], v[60:63], a[92:95]\n\t"
      "buffer_load_dwordx4 v8, s[72:75], s95 offen lds\n\t"
      "s_waitcnt lgkmcnt(12)\n\t"
      "v_mfma_f32_16x16x32_bf16 a[96:99], v[40:43], v[48:51], a[96:99]\n\t"
      "ds_read_b64_tr_b16 v[36:37], v3 offset:3072\n\t"
      "v_mfma_f32_16x16x32_bf16 a[100:103], v[40:43], v[52:55], a[100:103]\n\t"
      "ds_read_b64_tr_b16 v[38:39], v3 offset:3328\n\t"
      "v_mfma_f32_16x16x32_bf16 a[104:107], v[40:43], v[56:59], a[104:107]\n\t"
      "v_mfma_f32_16x16x32_bf16 a[108:111], v[40:43], v[60:63], a[108:111]\n\t"
      "buffer_load_dwordx4 v9, s[72:75], s95 offen offset:1024 lds\n\t"
      "s_waitcnt lgkmcnt(12)\n\t"
      "v_mfma_f32_16x16x32_bf16 a[112:115], v[44:47], v[48:51], a[112:115]\n\t"
      "ds_read_b64_tr_b16 v[40:41], v2 offset:3584\n\t"
      "v_mfma_f32_16x16x32_bf16 a[116:119], v[44:47], v[52:55], a[116:119]\n\t"
      "ds_read_b64_tr_b16 v[42:43], v2 offset:3840\n\t"
      "v_mfma_f32_16x16x32_bf16 a[120:123], v[44:47], v[56:59], a[120:123]\n\t"
      "v_mfma_f32_16x16x32_bf16 a[124:127], v[44:47], v[60:63], a[124:127]\n\t"
      "buffer_load_dwordx4 v10, s[72:75], s95 offen offset:2048 lds\n\t"
      "s_waitcnt lgkmcnt(12)\n\t"
      "v_mfma_f32_16x16x32_bf16 v[128:131], v[16:19], v[48:51], v[128:131]\n\t"
      "ds_read_b64_tr_b16 v[44:45], v3 offset:3584\n\t"
      "v_mfma_f32_16x16x32_bf16 v[132:135], v[16:19], v[52:55], v[132:135]\n\t"
      "ds_read_b64_tr_b16 v[46:47], v3 offset:3840\n\t"
      "v_mfma_f32_16x16x32_bf16 v[136:139], v[16:19], v[56:59], v[136:139]\n\t"
      "v_mfma_f32_16x16x32_bf16 v[140:143], v[16:19], v[60:63], v[140:143]\n\t"
      "buffer_load_dwordx4 v11, s[72:75], s95 offen offset:3072 lds\n\t"
      "s_waitcnt lgkmcnt(12)\n\t"
      "v_mfma_f32_16x16x32_bf16 v[144:147], v[20:23], v[48:51], v[144:147]\n\t"
      "ds_read_b64_tr_b16 v[16:17], v1 offset:16384\n\t"
      "v_mfma_f32_16x16x32_bf16 v[148:151], v[20:23], v[52:55], v[148:151]\n\t"
      "ds_read_b64_tr_b16 v[18:19], v1 offset:16640\n\t"
      "v_mfma_f32_16x16x32_bf16 v[152:155], v[20:23], v[56:59], v[152:155]\n\t"
      "s_add_u32 m0, s96, 0x4000\n\t"
      "v_mfma_f32_16x16x32_bf16 v[156:159], v[20:23], v[60:63], v[156:159]\n\t"
      "buffer_load_dwordx4 v12, s[76:79], s95 offen lds\n\t"
      "s_waitcnt lgkmcnt(12)\n\t"
      "v_mfma_f32_16x16x32_bf16 v[160:163], v[24:27], v[48:51], v[160:163]\n\t"
      "ds_read_b64_tr_b16 v[20:21], v1 offset:16392\n\t"
      "v_mfma_f32_16x16x32_bf16 v[164:167], v[24:27], v[52:55], v[164:167]\n\t"
      "ds_read_b64_tr_b16 v[22:23], v1 offset:16648\n\t"
      "v_mfma_f32_16x16x32_bf16 v[168:171], v[24:27], v[56:59], v[168:171]\n\t"
      "v_mfma_f32_16x16x32_bf16 v[172:175], v[24:27], v[60:63], v[172:175]\n\t"
      "buffer_load_dwordx4 v12, s[76:79], s95 offen offset:2048 lds\n\t"
      "s_waitcnt lgkmcnt(12)\n\t"
      "v_mfma_f32_16x16x32_bf16 v[176:179], v[28:31], v[48:51], v[176:179]\n\t"
      "ds_read_b64_tr_b16 v[24:25], v1 offset:16896\n\t"
      "v_mfma_f32_16x16x32_bf16 v[180:183], v[28:31], v[52:55], v[180:183]\n\t"
      "ds_read_b64_tr_b16 v[26:27], v1 offset:17152\n\t"
      "v_mfma_f32_16x16x32_bf16 v[184:187], v[28:31], v[56:59], v[184:187]\n\t"
      "v_mfma_f32_16x16x32_bf16 v[188:191], v[28:31], v[60:63], v[188:191]\n\t"
      "s_waitcnt lgkmcnt(12)\n\t"
      "v_mfma_f32_16x16x32_bf16 v[192:195], v[32:35], v[48:51], v[192:195]\n\t"
      "ds_read_b64_tr_b16 v[28:29], v1 offset:16904\n\t"
      "v_mfma_f32_16x16x32_bf16 v[196:199], v[32:35], v[52:55], v[196:199]\n\t"
      "ds_read_b64_tr_b16 v[30:31], v1 offset:17160\n\t"
      "v_mfma_f32_16x16x32_bf16 v[200:203], v[32:35], v[56:59], v[200:203]\n\t"
      "v_mfma_f32_16x16x32_bf16 v[204:207], v[32:35], v[60:63], v[204:207]\n\t"
      "s_waitcnt lgkmcnt(12)\n\t"
      "v_mfma_f32_16x16x32_bf16 v[208:211], v[36:39], v[48:51], v[208:211]\n\t"
      "ds_read_b64_tr_b16 v[32:33], v1 offset:17408\n\t"
      "v_mfma_f32_16x16x32_bf16 v[212:215], v[36:39], v[52:55], v[212:215]\n\t"
      "ds_read_b64_tr_b16 v[34:35], v1 offset:17664\n\t"
      "v_mfma_f32_16x16x32_bf16 v[216:219], v[36:39], v[56:59], v[216:219]\n\t"
      "v_mfma_f32_16x16x32_bf16 v[220:223], v[36:39], v[60:63], v[220:223]\n\t"
      "s_waitcnt lgkmcnt(12)\n\t"
      "v_mfma_f32_16x16x32_bf16 v[224:227], v[40:43], v[48:51], v[224:227]\n\t"
      "ds_read_b64_tr_b16 v[36:37], v1 offset:17416\n\t"
      "v_mfma_f32_16x16x32_bf16 v[228:231], v[40:43], v[52:55], v[228:231]\n\t"
      "ds_read_b64_tr_b16 v[38:39], v1 offset:17672\n\t"
      "v_mfma_f32_16x16x32_bf16 v[232:235], v[40:43], v[56:59], v[232:235]\n\t"
      "v_mfma_f32_16x16x32_bf16 v[236:239], v[40:43], v[60:63], v[236:239]\n\t"
      "s_waitcnt lgkmcnt(12)\n\t"
      "v_mfma_f32_16x16x32_bf16 v[240:243], v[44:47], v[48:51], v[240:243]\n\t"
      "ds_read_b64_tr_b16 v[40:41], v1 offset:17920\n\t"
      "v_mfma_f32_16x16x32_bf16 v[244:247], v[44:47], v[52:55], v[244:247]\n\t"
      "ds_read_b64_tr_b16 v[42:43], v1 offset:18176\n\t"
      "v_mfma_f32_16x16x32_bf16 v[248:251], v[44:47], v[56:59], v[248:251]\n\t"
      "v_mfma_f32_16x16x32_bf16 v[252:255], v[44:47], v[60:63], v[252:255]\n\t"
      "s_waitcnt vmcnt(23) lgkmcnt(12)\n\t"
      "v_mfma_f32_16x16x32_bf16 a[0:3], v[16:19], v[64:67], a[0:3]\n\t"
      "ds_read_b64_tr_b16 v[44:45], v1 offset:17928\n\t"
      "v_mfma_f32_16x16x32_bf16 a[4:7], v[16:19], v[68:71], a[4:7]\n\t"
      "ds_read_b64_tr_b16 v[46:47], v1 offset:18184\n\t"
      "v_mfma_f32_16x16x32_bf16 a[8:11], v[16:19], v[72:75], a[8:11]\n\t"
      "s_add_u32 m0, s96, 0x4380\n\t"
      "v_mfma_f32_16x16x32_bf16 a[12:15], v[16:19], v[76:79], a[12:15]\n\t"
      "buffer_load_dwordx4 v12, s[76:79], s95 offen offset:128 lds\n\t"
      "s_waitcnt lgkmcnt(12)\n\t"
      "v_mfma_f32_16x16x32_bf16 a[16:19], v[20:23], v[64:67], a[16:19]\n\t"
      "ds_read_b64_tr_b16 v[16:17], v2 offset:18432\n\t"
      "v_mfma_f32_16x16x32_bf16 a[20:23], v[20:23], v[68:71], a[20:23]\n\t"
      "ds_read_b64_tr_b16 v[18:19], v2 offset:18688\n\t"
      "v_mfma_f32_16x16x32_bf16 a[24:27], v[20:23], v[72:75], a[24:27]\n\t"
      "v_mfma_f32_16x16x32_bf16 a[28:31], v[20:23], v[76:79], a[28:31]\n\t"
      "buffer_load_dwordx4 v12, s[76:79], s95 offen offset:2176 lds\n\t"
      "s_waitcnt lgkmcnt(12)\n\t"
      "v_mfma_f32_16x16x32_bf16 a[32:35], v[24:27], v[64:67], a[32:35]\n\t"
      "ds_read_b64_tr_b16 v[20:21], v3 offset:18432\n\t"
      "v_mfma_f32_16x16x32_bf16 a[36:39], v[24:27], v[68:71], a[36:39]\n\t"
      "ds_read_b64_tr_b16 v[22:23], v3 offset:18688\n\t"
      "v_mfma_f32_16x16x32_bf16 a[40:43], v[24:27], v[72:75], a[40:43]\n\t"
      "s_add_u32 s96, s96, 0x8000\n\t"
      "s_cmp_ge_u32 s96, 0x28000\n\t"
      "s_cselect_b32 s93, 0x28000, 0\n\t"
      "s_sub_u32 s96, s96, s93\n\t"
      "s_add_u32 s95, s95, 0x4000\n\t"
      "s_mov_b32 m0, s96\n\t"
      "v_mfma_f32_16x16x32_bf16 a[44:47], v[24:27], v[76:79], a[44:47]\n\t"
      "buffer_load_dwordx4 v8, s[72:75], s95 offen lds\n\t"
      "s_waitcnt lgkmcnt(12)\n\t"
      "v_mfma_f32_16x16x32_bf16 a[48:51], v[28:31], v[64:67], a[48:51]\n\t"
      "ds_read_b64_tr_b16 v[24:25], v2 offset:18944\n\t"
      "v_mfma_f32_16x16x32_bf16 a[52:55], v[28:31], v[68:71], a[52:55]\n\t"
      "ds_read_b64_tr_b16 v[26:27], v2 offset:19200\n\t"
      "v_mfma_f32_16x16x32_bf16 a[56:59], v[28:31], v[72:75], a[56:59]\n\t"
      "v_mfma_f32_16x16x32_bf16 a[60:63], v[28:31], v[76:79], a[60:63]\n\t"
      "buffer_load_dwordx4 v9, s[72:75], s95 offen offset:1024 lds\n\t"
      "s_waitcnt lgkmcnt(12)\n\t"
      "v_mfma_f32_16x16x32_bf16 a[64:67], v[32:35], v[64:67], a[64:67]\n\t"
      "ds_read_b64_tr_b16 v[28:29], v3 offset:18944\n\t"
      "v_mfma_f32_16x16x32_bf16 a[68:71], v[32:35], v[68:71], a[68:71]\n\t"
      "ds_read_b64_tr_b16 v[30:31], v3 offset:19200\n\t"
      "v_mfma_f32_16x16x32_bf16 a[72:75], v[32:35], v[72:75], a[72:75]\n\t"
      "v_mfma_f32_16x16x32_bf16 a[76:79], v[32:35], v[76:79], a[76:79]\n\t"
      "buffer_load_dwordx4 v10, s[72:75], s95 offen offset:2048 lds\n\t"
      "s_waitcnt lgkmcnt(12)\n\t"
      "v_mfma_f32_16x16x32_bf16 a[80:83], v[36:39], v[64:67], a[80:83]\n\t"
      "ds_read_b64_tr_b16 v[32:33], v2 offset:19456\n\t"
      "v_mfma_f32_16x16x32_bf16 a[84:87], v[36:39], v[68:71], a[84:87]\n\t"
      "ds_read_b64_tr_b16 v[34:35], v2 offset:19712\n\t"
      "v_mfma_f32_16x16x32_bf16 a[88:91], v[36:39], v[72:75], a[88:91]\n\t"
      "v_mfma_f32_16x16x32_bf16 a[92:95], v[36:39], v[76:79], a[92:95]\n\t"
      "buffer_load_dwordx4 v11, s[72:75], s95 offen offset:3072 lds\n\t"
      "s_waitcnt lgkmcnt(12)\n\t"
      "v_mfma_f32_16x16x32_bf16 a[96:99], v[40:43], v[64:67], a[96:99]\n\t"
      "ds_read_b64_tr_b16 v[36:37], v3 offset:19456\n\t"
      "v_mfma_f32_16x16x32_bf16 a[100:103], v[40:43], v[68:71], a[100:103]\n\t"
      "ds_read_b64_tr_b16 v[38:39], v3 offset:19712\n\t"
      "v_mfma_f32_16x16x32_bf16 a[104:107], v[40:43], v[72:75], a[104:107]\n\t"
      "v_mfma_f32_16x16x32_bf16 a[108:111], v[40:43], v[76:79], a[108:111]\n\t"
      "s_waitcnt lgkmcnt(12)\n\t"
      "v_mfma_f32_16x16x32_bf16 a[112:115], v[44:47], v[64:67], a[112:115]\n\t"
      "ds_read_b64_tr_b16 v[40:41], v2 offset:19968\n\t"
      "v_mfma_f32_16x16x32_bf16 a[116:119], v[44:47], v[68:71], a[116:119]\n\t"
      "ds_read_b64_tr_b16 v[42:43], v2 offset:20224\n\t"
      "v_mfma_f32_16x16x32_bf16 a[120:123], v[44:47], v[72:75], a[120:123]\n\t"
      "v_mfma_f32_16x16x32_bf16 a[124:127], v[44:47], v[76:79], a[124:127]\n\t"
      "s_waitcnt lgkmcnt(12)\n\t"
      "v_mfma_f32_16x16x32_bf16 v[128:131], v[16:19], v[64:67], v[128:131]\n\t"
      "ds_read_b64_tr_b16 v[44:45], v3 offset:19968\n\t"
      "v_mfma_f32_16x16x32_bf16 v[132:135], v[16:19], v[68:71], v[132:135]\n\t"
      "ds_read_b64_tr_b16 v[46:47], v3 offset:20224\n\t"
      "v_mfma_f32_16x16x32_bf16 v[136:139], v[16:19], v[72:75], v[136:139]\n\t"
      "v_mfma_f32_16x16x32_bf16 v[140:143], v[16:19], v[76:79], v[140:143]\n\t"
      "s_waitcnt lgkmcnt(12)\n\t"
      "v_mfma_f32_16x16x32_bf16 v[144:147], v[20:23], v[64:67], v[144:147]\n\t"
      "v_mfma_f32_16x16x32_bf16 v[148:151], v[20:23], v[68:71], v[148:151]\n\t"
      "v_mfma_f32_16x16x32_bf16 v[152:155], v[20:23], v[72:75], v[152:155]\n\t"
      "v_mfma_f32_16x16x32_bf16 v[156:159], v[20:23], v[76:79], v[156:159]\n\t"
      "s_waitcnt lgkmcnt(10)\n\t"
      "v_mfma_f32_16x16x32_bf16 v[160:163], v[24:27], v[64:67], v[160:163]\n\t"
      "v_mfma_f32_16x16x32_bf16 v[164:167], v[24:27], v[68:71], v[164:167]\n\t"
      "v_mfma_f32_16x16x32_bf16 v[168:171], v[24:27], v[72:75], v[168:171]\n\t"
      "v_mfma_f32_16x16x32_bf16 v[172:175], v[24:27], v[76:79], v[172:175]\n\t"
      "s_waitcnt vmcnt(17) lgkmcnt(0)\n\t"
      "s_barrier\n\t"
      "s_add_u32 s98, s98, 0x8000\n\t"
      "s_cmp_ge_u32 s98, 0x18000\n\t"
      "s_cselect_b32 s97, 0xffff0000, s44\n\t"
      "s_cselect_b32 s98, 0, s98\n\t"
      "v_add_u32 v1, s97, v1\n\t"
      "v_add_u32 v2, s97, v2\n\t"
      "v_add_u32 v3, s97, v3\n\t"
      "v_mfma_f32_16x16x32_bf16 v[176:179], v[28:31], v[64:67], v[176:179]\n\t"
      "ds_read_b64_tr_b16 v[16:17], v1\n\t"
      "v_mfma_f32_16x16x32_bf16 v[180:183], v[28:31], v[68:71], v[180:183]\n\t"
      "ds_read_b64_tr_b16 v[18:19], v1 offset:256\n\t"
      "v_mfma_f32_16x16x32_bf16 v[184:187], v[28:31], v[72:75], v[184:187]\n\t"
      "v_mfma_f32_16x16x32_bf16 v[188:191], v[28:31], v[76:79], v[188:191]\n\t"
      "v_mfma_f32_16x16x32_bf16 v[192:195], v[32:35], v[64:67], v[192:195]\n\t"
      "ds_read_b64_tr_b16 v[20:21], v1 offset:8\n\t"
      "v_mfma_f32_16x16x32_bf16 v[196:199], v[32:35], v[68:71], v[196:199]\n\t"
      "ds_read_b64_tr_b16 v[22:23], v1 offset:264\n\t"
      "v_mfma_f32_16x16x32_bf16 v[200:203], v[32:35], v[72:75], v[200:203]\n\t"
      "ds_read_b64_tr_b16 v[24:25], v1 offset:512\n\t"
      "v_mfma_f32_16x16x32_bf16 v[204:207], v[32:35], v[76:79], v[204:207]\n\t"
      "ds_read_b64_tr_b16 v[26:27], v1 offset:768\n\t"
      "v_mfma_f32_16x16x32_bf16 v[208:211], v[36:39], v[64:67], v[208:211]\n\t"
      "ds_read_b64_tr_b16 v[28:29], v1 offset:520\n\t"
      "v_mfma_f32_16x16x32_bf16 v[212:215], v[36:39], v[68:71], v[212:215]\n\t"
      "ds_read_b64_tr_b16 v[30:31], v1 offset:776\n\t"
      "v_mfma_f32_16x16x32_bf16 v[216:219], v[36:39], v[72:75], v[216:219]\n\t"
      "ds_read_b64_tr_b16 v[32:33], v1 offset:1024\n\t"
      "v_mfma_f32_16x16x32_bf16 v[220:223], v[36:39], v[76:79], v[220:223]\n\t"
      "ds_read_b64_tr_b16 v[34:35], v1 offset:1280\n\t"
      "v_mfma_f32_16x16x32_bf16 v[224:227], v[40:43], v[64:67], v[224:227]\n\t"
      "ds_read_b64_tr_b16 v[36:37], v1 offset:1032\n\t"
      "v_mfma_f32_16x16x32_bf16 v[228:231], v[40:43], v[68:71], v[228:231]\n\t"
      "ds_read_b64_tr_b16 v[38:39], v1 offset:1288\n\t"
      "v_mfma_f32_16x16x32_bf16 v[232:235], v[40:43], v[72:75], v[232:235]\n\t"
      "v_mfma_f32_16x16x32_bf16 v[236:239], v[40:43], v[76:79], v[236:239]\n\t"
      "v_mfma_f32_16x16x32_bf16 v[240:243], v[44:47], v[64:67], v[240:243]\n\t"
      "ds_read_b64_tr_b16 v[40:41], v1 offset:1536\n\t"
      "v_mfma_f32_16x16x32_bf16 v[244:247], v[44:47], v[68:71], v[244:247]\n\t"
      "ds_read_b64_tr_b16 v[42:43], v1 offset:1792\n\t"
      "v_mfma_f32_16x16x32_bf16 v[248:251], v[44:47], v[72:75], v[248:251]\n\t"
      "v_mfma_f32_16x16x32_bf16 v[252:255], v[44:47], v[76:79], v[252:255]\n\t"
      "s_waitcnt lgkmcnt(12)\n\t"
      "v_mfma_f32_16x16x32_bf16 a[0:3], v[16:19], v[80:83], a[0:3]\n\t"
      "ds_read_b64_tr_b16 v[44:45], v1 offset:1544\n\t"
      "v_mfma_f32_16x16x32_bf16 a[4:7], v[16:19], v[84:87], a[4:7]\n\t"
      "ds_read_b64_tr_b16 v[46:47], v1 offset:1800\n\t"
      "v_mfma_f32_16x16x32_bf16 a[8:11], v[16:19], v[88:91], a[8:11]\n\t"
      "v_mfma_f32_16x16x32_bf16 a[12:15], v[16:19], v[92:95], a[12:15]\n\t"
      "s_waitcnt lgkmcnt(12)\n\t"
      "v_mfma_f32_16x16x32_bf16 a[16:19], v[20:23], v[80:83], a[16:19]\n\t"
      "ds_read_b64_tr_b16 v[16:17], v2 offset:2048\n\t"
      "v_mfma_f32_16x16x32_bf16 a[20:23], v[20:23], v[84:87], a[20:23]\n\t"
      "ds_read_b64_tr_b16 v[18:19], v2 offset:2304\n\t"
      "v_mfma_f32_16x16x32_bf16 a[24:27], v[20:23], v[88:91], a[24:27]\n\t"
      "s_add_u32 m0, s96, 0x4000\n\t"
      "v_mfma_f32_16x16x32_bf16 a[28:31], v[20:23], v[92:95], a[28:31]\n\t"
      "buffer_load_dwordx4 v12, s[76:79], s95 offen lds\n\t"
      "s_waitcnt lgkmcnt(12)\n\t"
      "v_mfma_f32_16x16x32_bf16 a[32:35], v[24:27], v[80:83], a[32:35]\n\t"
      "ds_read_b64_tr_b16 v[20:21], v3 offset:2048\n\t"
      "v_mfma_f32_16x16x32_bf16 a[36:39], v[24:27], v[84:87], a[36:39]\n\t"
      "ds_read_b64_tr_b16 v[22:23], v3 offset:2304\n\t"
      "v_mfma_f32_16x16x32_bf16 a[40:43], v[24:27], v[88:91], a[40:43]\n\t"
      "v_mfma_f32_16x16x32_bf16 a[44:47], v[24:27], v[92:95], a[44:47]\n\t"
      "buffer_load_dwordx4 v12, s[76:79], s95 offen offset:2048 lds\n\t"
      "s_waitcnt lgkmcnt(12)\n\t"
      "v_mfma_f32_16x16x32_bf16 a[48:51], v[28:31], v[80:83], a[48:51]\n\t"
      "ds_read_b64_tr_b16 v[24:25], v2 offset:2560\n\t"
      "v_mfma_f32_16x16x32_bf16 a[52:55], v[28:31], v[84:87], a[52:55]\n\t"
      "ds_read_b64_tr_b16 v[26:27], v2 offset:2816\n\t"
      "v_mfma_f32_16x16x32_bf16 a[56:59], v[28:31], v[88:91], a[56:59]\n\t"
      "s_add_u32 m0, s96, 0x4380\n\t"
      "v_mfma_f32_16x16x32_bf16 a[60:63], v[28:31], v[92:95], a[60:63]\n\t"
      "buffer_load_dwordx4 v12, s[76:79], s95 offen offset:128 lds\n\t"
      "s_waitcnt lgkmcnt(12)\n\t"
      "v_mfma_f32_16x16x32_bf16 a[64:67], v[32:35], v[80:83], a[64:67]\n\t"
      "ds_read_b64_tr_b16 v[28:29], v3 offset:2560\n\t"
      "v_mfma_f32_16x16x32_bf16 a[68:71], v[32:35], v[84:87], a[68:71]\n\t"
      "ds_read_b64_tr_b16 v[30:31], v3 offset:2816\n\t"
      "v_mfma_f32_16x16x32_bf16 a[72:75], v[32:35], v[88:91], a[72:75]\n\t"
      "v_mfma_f32_16x16x32_bf16 a[76:79], v[32:35], v[92:95], a[76:79]\n\t"
      "buffer_load_dwordx4 v12, s[76:79], s95 offen offset:2176 lds\n\t"
      "s_waitcnt lgkmcnt(12)\n\t"
      "v_mfma_f32_16x16x32_bf16 a[80:83], v[36:39], v[80:83], a[80:83]\n\t"
      "ds_read_b64_tr_b16 v[32:33], v2 offset:3072\n\t"
      "v_mfma_f32_16x16x32_bf16 a[84:87], v[36:39], v[84:87], a[84:87]\n\t"
      "ds_read_b64_tr_b16 v[34:35], v2 offset:3328\n\t"
      "v_mfma_f32_16x16x32_bf16 a[88:91], v[36:39], v[88:91], a[88:91]\n\t"
      "s_add_u32 s96, s96, 0x8000\n\t"
      "s_cmp_ge_u32 s96, 0x28000\n\t"
      "s_cselect_b32 s93, 0x28000, 0\n\t"
      "s_sub_u32 s96, s96, s93\n\t"
      "s_add_u32 s95, s95, 0x4000\n\t"
      "s_mov_b32 m0, s96\n\t"
      "v_mfma_f32_16x16x32_bf16 a[92:95], v[36:39], v[92:95], a[92:95]\n\t"
      "buffer_load_dwordx4 v8, s[72:75], s95 offen lds\n\t"
      "s_waitcnt lgkmcnt(12)\n\t"
      "v_mfma_f32_16x16x32_bf16 a[96:99], v[40:43], v[80:83], a[96:99]\n\t"
      "ds_read_b64_tr_b16 v[36:37], v3 offset:3072\n\t"
      "v_mfma_f32_16x16x32_bf16 a[100:103], v[40:43], v[84:87], a[100:103]\n\t"
      "ds_read_b64_tr_b16 v[38:39], v3 offset:3328\n\t"
      "v_mfma_f32_16x16x32_bf16 a[104:107], v[40:43], v[88:91], a[104:107]\n\t"
      "v_mfma_f32_16x16x32_bf16 a[108:111], v[40:43], v[92:95], a[108:111]\n\t"
      "buffer_load_dwordx4 v9, s[72:75], s95 offen offset:1024 lds\n\t"
      "s_waitcnt lgkmcnt(12)\n\t"
      "v_mfma_f32_16x16x32_bf16 a[112:115], v[44:47], v[80:83], a[112:115]\n\t"
      "ds_read_b64_tr_b16 v[40:41], v2 offset:3584\n\t"
      "v_mfma_f32_16x16x32_bf16 a[116:119], v[44:47], v[84:87], a[116:119]\n\t"
      "ds_read_b64_tr_b16 v[42:43], v2 offset:3840\n\t"
      "v_mfma_f32_16x16x32_bf16 a[120:123], v[44:47], v[88:91], a[120:123]\n\t"
      "v_mfma_f32_16x16x32_bf16 a[124:127], v[44:47], v[92:95], a[124:127]\n\t"
      "s_waitcnt lgkmcnt(12)\n\t"
      "v_mfma_f32_16x16x32_bf16 v[128:131], v[16:19], v[80:83], v[128:131]\n\t"
      "ds_read_b64_tr_b16 v[44:45], v3 offset:3584\n\t"
      "v_mfma_f32_16x16x32_bf16 v[132:135], v[16:19], v[84:87], v[132:135]\n\t"
      "ds_read_b64_tr_b16 v[46:47], v3 offset:3840\n\t"
      "v_mfma_f32_16x16x32_bf16 v[136:139], v[16:19], v[88:91], v[136:139]\n\t"
      "v_mfma_f32_16x16x32_bf16 v[140:143], v[16:19], v[92:95], v[140:143]\n\t"
      "s_waitcnt lgkmcnt(12)\n\t"
      "v_mfma_f32_16x16x32_bf16 v[144:147], v[20:23], v[80:83], v[144:147]\n\t"
      "ds_read_b64_tr_b16 v[16:17], v1 offset:16384\n\t"
      "v_mfma_f32_16x16x32_bf16 v[148:151], v[20:23], v[84:87], v[148:151]\n\t"
      "ds_read_b64_tr_b16 v[18:19], v1 offset:16640\n\t"
      "v_mfma_f32_16x16x32_bf16 v[152:155], v[20:23], v[88:91], v[152:155]\n\t"
      "v_mfma_f32_16x16x32_bf16 v[156:159], v[20:23], v[92:95], v[156:159]\n\t"
      "s_waitcnt lgkmcnt(12)\n\t"
      "v_mfma_f32_16x16x32_bf16 v[160:163], v[24:27], v[80:83], v[160:163]\n\t"
      "ds_read_b64_tr_b16 v[20:21], v1 offset:16392\n\t"
      "v_mfma_f32_16x16x32_bf16 v[164:167], v[24:27], v[84:87], v[164:167]\n\t"
      "ds_read_b64_tr_b16 v[22:23], v1 offset:16648\n\t"
      "v_mfma_f32_16x16x32_bf16 v[168:171], v[24:27], v[88:91], v[168:171]\n\t"
      "v_mfma_f32_16x16x32_bf16 v[172:175], v[24:27], v[92:95], v[172:175]\n\t"
      "s_waitcnt lgkmcnt(12)\n\t"
      "v_mfma_f32_16x16x32_bf16 v[176:179], v[28:31], v[80:83], v[176:179]\n\t"
      "ds_read_b64_tr_b16 v[24:25], v1 offset:16896\n\t"
      "v_mfma_f32_16x16x32_bf16 v[180:183], v[28:31], v[84:87], v[180:183]\n\t"
      "ds_read_b64_tr_b16 v[26:27], v1 offset:17152\n\t"
      "v_mfma_f32_16x16x32_bf16 v[184:187], v[28:31], v[88:91], v[184:187]\n\t"
      "v_mfma_f32_16x16x32_bf16 v[188:191], v[28:31], v[92:95], v[188:191]\n\t"
      "s_waitcnt lgkmcnt(12)\n\t"
      "v_mfma_f32_16x16x32_bf16 v[192:195], v[32:35], v[80:83], v[192:195]\n\t"
      "ds_read_b64_tr_b16 v[28:29], v1 offset:16904\n\t"
      "v_mfma_f32_16x16x32_bf16 v[196:199], v[32:35], v[84:87], v[196:199]\n\t"
      "ds_read_b64_tr_b16 v[30:31], v1 offset:17160\n\t"
      "v_mfma_f32_16x16x32_bf16 v[200:203], v[32:35], v[88:91], v[200:203]\n\t"
      "v_mfma_f32_16x16x32_bf16 v[204:207], v[32:35], v[92:95], v[204:207]\n\t"
      "s_waitcnt lgkmcnt(12)\n\t"
      "v_mfma_f32_16x16x32_bf16 v[208:211], v[36:39], v[80:83], v[208:211]\n\t"
      "ds_read_b64_tr_b16 v[32:33], v1 offset:17408\n\t"
      "v_mfma_f32_16x16x32_bf16 v[212:215], v[36:39], v[84:87], v[212:215]\n\t"
      "ds_read_b64_tr_b16 v[34:35], v1 offset:17664\n\t"
      "v_mfma_f32_16x16x32_bf16 v[216:219], v[36:39], v[88:91], v[216:219]\n\t"
      "v_mfma_f32_16x16x32_bf16 v[220:223], v[36:39], v[92:95], v[220:223]\n\t"
      "s_waitcnt lgkmcnt(12)\n\t"
      "v_mfma_f32_16x16x32_bf16 v[224:227], v[40:43], v[80:83], v[224:227]\n\t"
      "ds_read_b64_tr_b16 v[36:37], v1 offset:17416\n\t"
      "v_mfma_f32_16x16x32_bf16 v[228:231], v[40:43], v[84:87], v[228:231]\n\t"
      "ds_read_b64_tr_b16 v[38:39], v1 offset:17672\n\t"
      "v_mfma_f32_16x16x32_bf16 v[232:235], v[40:43], v[88:91], v[232:235]\n\t"
      "v_mfma_f32_16x16x32_bf16 v[236:239], v[40:43], v[92:95], v[236:239]\n\t"
      "s_waitcnt lgkmcnt(12)\n\t"
      "v_mfma_f32_16x16x32_bf16 v[240:243], v[44:47], v[80:83], v[240:243]\n\t"
      "ds_read_b64_tr_b16 v[40:41], v1 offset:17920\n\t"
      "v_mfma_f32_16x16x32_bf16 v[244:247], v[44:47], v[84:87], v[244:247]\n\t"
      "ds_read_b64_tr_b16 v[42:43], v1 offset:18176\n\t"
      "v_mfma_f32_16x16x32_bf16 v[248:251], v[44:47], v[88:91], v[248:251]\n\t"
      "v_mfma_f32_16x16x32_bf16 v[252:255], v[44:47], v[92:95], v[252:255]\n\t"
      "s_waitcnt vmcnt(18) lgkmcnt(12)\n\t"
      "v_mfma_f32_16x16x32_bf16 a[0:3], v[16:19], v[96:99], a[0:3]\n\t"
      "ds_read_b64_tr_b16 v[44:45], v1 offset:17928\n\t"
      "v_mfma_f32_16x16x32_bf16 a[4:7], v[16:19], v[100:103], a[4:7]\n\t"
      "ds_read_b64_tr_b16 v[46:47], v1 offset:18184\n\t"
      "v_mfma_f32_16x16x32_bf16 a[8:11], v[16:19], v[104:107], a[8:11]\n\t"
      "v_mfma_f32_16x16x32_bf16 a[12:15], v[16:19], v[108:111], a[12:15]\n\t"
      "buffer_load_dwordx4 v10, s[72:75], s95 offen offset:2048 lds\n\t"
      "s_waitcnt lgkmcnt(12)\n\t"
      "v_mfma_f32_16x16x32_bf16 a[16:19], v[20:23], v[96:99], a[16:19]\n\t"
      "ds_read_b64_tr_b16 v[16:17], v2 offset:18432\n\t"
      "v_mfma_f32_16x16x32_bf16 a[20:23], v[20:23], v[100:103], a[20:23]\n\t"
      "ds_read_b64_tr_b16 v[18:19], v2 offset:18688\n\t"
      "v_mfma_f32_16x16x32_bf16 a[24:27], v[20:23], v[104:107], a[24:27]\n\t"
      "v_mfma_f32_16x16x32_bf16 a[28:31], v[20:23], v[108:111], a[28:31]\n\t"
      "buffer_load_dwordx4 v11, s[72:75], s95 offen offset:3072 lds\n\t"
      "s_waitcnt lgkmcnt(12)\n\t"
      "v_mfma_f32_16x16x32_bf16 a[32:35], v[24:27], v[96:99], a[32:35]\n\t"
      "ds_read_b64_tr_b16 v[20:21], v3 offset:18432\n\t"
      "v_mfma_f32_16x16x32_bf16 a[36:39], v[24:27], v[100:103], a[36:39]\n\t"
      "ds_read_b64_tr_b16 v[22:23], v3 offset:18688\n\t"
      "v_mfma_f32_16x16x32_bf16 a[40:43], v[24:27], v[104:107], a[40:43]\n\t"
      "s_add_u32 m0, s96, 0x4000\n\t"
      "v_mfma_f32_16x16x32_bf16 a[44:47], v[24:27], v[108:111], a[44:47]\n\t"
      "buffer_load_dwordx4 v12, s[76:79], s95 offen lds\n\t"
      "s_waitcnt lgkmcnt(12)\n\t"
      "v_mfma_f32_16x16x32_bf16 a[48:51], v[28:31], v[96:99], a[48:51]\n\t"
      "ds_read_b64_tr_b16 v[24:25], v2 offset:18944\n\t"
      "v_mfma_f32_16x16x32_bf16 a[52:55], v[28:31], v[100:103], a[52:55]\n\t"
      "ds_read_b64_tr_b16 v[26:27], v2 offset:19200\n\t"
      "v_mfma_f32_16x16x32_bf16 a[56:59], v[28:31], v[104:107], a[56:59]\n\t"
      "v_mfma_f32_16x16x32_bf16 a[60:63], v[28:31], v[108:111], a[60:63]\n\t"
      "buffer_load_dwordx4 v12, s[76:79], s95 offen offset:2048 lds\n\t"
      "s_waitcnt lgkmcnt(12)\n\t"
      "v_mfma_f32_16x16x32_bf16 a[64:67], v[32:35], v[96:99], a[64:67]\n\t"
      "ds_read_b64_tr_b16 v[28:29], v3 offset:18944\n\t"
      "v_mfma_f32_16x16x32_bf16 a[68:71], v[32:35], v[100:103], a[68:71]\n\t"
      "ds_read_b64_tr_b16 v[30:31], v3 offset:19200\n\t"
      "v_mfma_f32_16x16x32_bf16 a[72:75], v[32:35], v[104:107], a[72:75]\n\t"
      "s_add_u32 m0, s96, 0x4380\n\t"
      "v_mfma_f32_16x16x32_bf16 a[76:79], v[32:35], v[108:111], a[76:79]\n\t"
      "buffer_load_dwordx4 v12, s[76:79], s95 offen offset:128 lds\n\t"
      "s_waitcnt lgkmcnt(12)\n\t"
      "v_mfma_f32_16x16x32_bf16 a[80:83], v[36:39], v[96:99], a[80:83]\n\t"
      "ds_read_b64_tr_b16 v[32:33], v2 offset:19456\n\t"
      "v_mfma_f32_16x16x32_bf16 a[84:87], v[36:39], v[100:103], a[84:87]\n\t"
      "ds_read_b64_tr_b16 v[34:35], v2 offset:19712\n\t"
      "v_mfma_f32_16x16x32_bf16 a[88:91], v[36:39], v[104:107], a[88:91]\n\t"
      "v_mfma_f32_16x16x32_bf16 a[92:95], v[36:39], v[108:111], a[92:95]\n\t"
      "buffer_load_dwordx4 v12, s[76:79], s95 offen offset:2176 lds\n\t"
      "s_waitcnt lgkmcnt(12)\n\t"
      "v_mfma_f32_16x16x32_bf16 a[96:99], v[40:43], v[96:99], a[96:99]\n\t"
      "ds_read_b64_tr_b16 v[36:37], v3 offset:19456\n\t"
      "v_mfma_f32_16x16x32_bf16 a[100:103], v[40:43], v[100:103], a[100:103]\n\t"
      "ds_read_b64_tr_b16 v[38:39], v3 offset:19712\n\t"
      "v_mfma_f32_16x16x32_bf16 a[104:107], v[40:43], v[104:107], a[104:107]\n\t"
      "v_mfma_f32_16x16x32_bf16 a[108:111], v[40:43], v[108:111], a[108:111]\n\t"
      "s_waitcnt lgkmcnt(12)\n\t"
      "v_mfma_f32_16x16x32_bf16 a[112:115], v[44:47], v[96:99], a[112:115]\n\t"
      "ds_read_b64_tr_b16 v[40:41], v2 offset:19968\n\t"
      "v_mfma_f32_16x16x32_bf16 a[116:119], v[44:47], v[100:103], a[116:119]\n\t"
      "ds_read_b64_tr_b16 v[42:43], v2 offset:20224\n\t"
      "v_mfma_f32_16x16x32_bf16 a[120:123], v[44:47], v[104:107], a[120:123]\n\t"
      "v_mfma_f32_16x16x32_bf16 a[124:127], v[44:47], v[108:111], a[124:127]\n\t"
      "s_waitcnt lgkmcnt(12)\n\t"
      "v_mfma_f32_16x16x32_bf16 v[128:131], v[16:19], v[96:99], v[128:131]\n\t"
      "ds_read_b64_tr_b16 v[44:45], v3 offset:19968\n\t"
      "v_mfma_f32_16x16x32_bf16 v[132:135], v[16:19], v[100:103], v[132:135]\n\t"
      "ds_read_b64_tr_b16 v[46:47], v3 offset:20224\n\t"
      "v_mfma_f32_16x16x32_bf16 v[136:139], v[16:19], v[104:107], v[136:139]\n\t"
      "v_mfma_f32_16x16x32_bf16 v[140:143], v[16:19], v[108:111], v[140:143]\n\t"
      "s_waitcnt lgkmcnt(12)\n\t"
      "v_mfma_f32_16x16x32_bf16 v[144:147], v[20:23], v[96:99], v[144:147]\n\t"
      "v_mfma_f32_16x16x32_bf16 v[148:151], v[20:23], v[100:103], v[148:151]\n\t"
      "v_mfma_f32_16x16x32_bf16 v[152:155], v[20:23], v[104:107], v[152:155]\n\t"
      "v_mfma_f32_16x16x32_bf16 v[156:159], v[20:23], v[108:111], v[156:159]\n\t"
      "s_waitcnt lgkmcnt(10)\n\t"
      "v_mfma_f32_16x16x32_bf16 v[160:163], v[24:27], v[96:99], v[160:163]\n\t"
      "v_mfma_f32_16x16x32_bf16 v[164:167], v[24:27], v[100:103], v[164:167]\n\t"
      "v_mfma_f32_16x16x32_bf16 v[168:171], v[24:27], v[104:107], v[168:171]\n\t"
      "v_mfma_f32_16x16x32_bf16 v[172:175], v[24:27], v[108:111], v[172:175]\n\t"
      "s_waitcnt lgkmcnt(8)\n\t"
      "v_mfma_f32_16x16x32_bf16 v[176:179], v[28:31], v[96:99], v[176:179]\n\t"
      "v_mfma_f32_16x16x32_bf16 v[180:183], v[28:31], v[100:103], v[180:183]\n\t"
      "v_mfma_f32_16x16x32_bf16 v[184:187], v[28:31], v[104:107], v[184:187]\n\t"
      "v_mfma_f32_16x16x32_bf16 v[188:191], v[28:31], v[108:111], v[188:191]\n\t"
      "s_waitcnt lgkmcnt(6)\n\t"
      "v_mfma_f32_16x16x32_bf16 v[192:195], v[32:35], v[96:99], v[192:195]\n\t"
      "v_mfma_f32_16x16x32_bf16 v[196:199], v[32:35], v[100:103], v[196:199]\n\t"
      "v_mfma_f32_16x16x32_bf16 v[200:203], v[32:35], v[104:107], v[200:203]\n\t"
      "v_mfma_f32_16x16x32_bf16 v[204:207], v[32:35], v[108:111], v[204:207]\n\t"
      "s_waitcnt lgkmcnt(4)\n\t"
      "v_mfma_f32_16x16x32_bf16 v[208:211], v[36:39], v[96:99], v[208:211]\n\t"
      "v_mfma_f32_16x16x32_bf16 v[212:215], v[36:39], v[100:103], v[212:215]\n\t"
      "v_mfma_f32_16x16x32_bf16 v[216:219], v[36:39], v[104:107], v[216:219]\n\t"
      "v_mfma_f32_16x16x32_bf16 v[220:223], v[36:39], v[108:111], v[220:223]\n\t"
      "s_waitcnt lgkmcnt(2)\n\t"
      "v_mfma_f32_16x16x32_bf16 v[224:227], v[40:43], v[96:99], v[224:227]\n\t"
      "v_mfma_f32_16x16x32_bf16 v[228:231], v[40:43], v[100:103], v[228:231]\n\t"
      "v_mfma_f32_16x16x32_bf16 v[232:235], v[40:43], v[104:107], v[232:235]\n\t"
      "v_mfma_f32_16x16x32_bf16 v[236:239], v[40:43], v[108:111], v[236:239]\n\t"
      "s_waitcnt lgkmcnt(0)\n\t"
      "v_mfma_f32_16x16x32_bf16 v[240:243], v[44:47], v[96:99], v[240:243]\n\t"
      "v_mfma_f32_16x16x32_bf16 v[244:247], v[44:47], v[100:103], v[244:247]\n\t"
      "v_mfma_f32_16x16x32_bf16 v[248:251], v[44:47], v[104:107], v[248:251]\n\t"
      "v_mfma_f32_16x16x32_bf16 v[252:255], v[44:47], v[108:111], v[252:255]\n\t"
      "s_nop 7\n\t"
      "s_nop 7\n\t"
      "v_pk_mul_f32 v[80:81], v[128:129], s[88:89]\n\t"
      "v_pk_mul_f32 v[82:83], v[130:131], s[88:89]\n\t"
      "v_pk_mul_f32 v[84:85], v[144:145], s[88:89]\n\t"
      "v_pk_mul_f32 v[86:87], v[146:147], s[88:89]\n\t"
      "v_pk_mul_f32 v[88:89], v[132:133], s[88:89]\n\t"
      "v_pk_mul_f32 v[90:91], v[134:135], s[88:89]\n\t"
      "v_pk_mul_f32 v[92:93], v[148:149], s[88:89]\n\t"
      "v_pk_mul_f32 v[94:95], v[150:151], s[88:89]\n\t"
      "v_exp_f32 v80, v80\n\t"
      "v_exp_f32 v81, v81\n\t"
      "v_exp_f32 v82, v82\n\t"
      "v_exp_f32 v83, v83\n\t"
      "v_exp_f32 v84, v84\n\t"
      "v_exp_f32 v85, v85\n\t"
      "v_exp_f32 v86, v86\n\t"
      "v_exp_f32 v87, v87\n\t"
      "v_exp_f32 v88, v88\n\t"
      "v_exp_f32 v89, v89\n\t"
      "v_exp_f32 v90, v90\n\t"
      "v_exp_f32 v91, v91\n\t"
      "v_exp_f32 v92, v92\n\t"
      "v_exp_f32 v93, v93\n\t"
      "v_exp_f32 v94, v94\n\t"
      "v_exp_f32 v95, v95\n\t"
      "v_pk_fma_f32 v[80:81], v[80:81], s[86:87], s[86:87]\n\t"
      "v_pk_fma_f32 v[82:83], v[82:83], s[86:87], s[86:87]\n\t"
      "v_pk_fma_f32 v[84:85], v[84:85], s[86:87], s[86:87]\n\t"
      "v_pk_fma_f32 v[86:87], v[86:87], s[86:87], s[86:87]\n\t"
      "v_pk_fma_f32 v[88:89], v[88:89], s[86:87], s[86:87]\n\t"
      "v_pk_fma_f32 v[90:91], v[90:91], s[86:87], s[86:87]\n\t"
      "v_pk_fma_f32 v[92:93], v[92:93], s[86:87], s[86:87]\n\t"
      "v_pk_fma_f32 v[94:95], v[94:95], s[86:87], s[86:87]\n\t"
      "v_rcp_f32 v80, v80\n\t"
      "v_rcp_f32 v81, v81\n\t"
      "v_rcp_f32 v82, v82\n\t"
      "v_rcp_f32 v83, v83\n\t"
      "v_rcp_f32 v84, v84\n\t"
      "v_rcp_f32 v85, v85\n\t"
      "v_rcp_f32 v86, v86\n\t"
      "v_rcp_f32 v87, v87\n\t"
      "v_rcp_f32 v88, v88\n\t"
      "v_rcp_f32 v89, v89\n\t"
      "v_rcp_f32 v90, v90\n\t"
      "v_rcp_f32 v91, v91\n\t"
      "v_rcp_f32 v92, v92\n\t"
      "v_rcp_f32 v93, v93\n\t"
      "v_rcp_f32 v94, v94\n\t"
      "v_rcp_f32 v95, v95\n\t"
      "v_pk_mul_f32 v[80:81], v[80:81], v[192:193]\n\t"
      "v_pk_mul_f32 v[82:83], v[82:83], v[194:195]\n\t"
      "v_pk_mul_f32 v[84:85], v[84:85], v[208:209]\n\t"
      "v_pk_mul_f32 v[86:87], v[86:87], v[210:211]\n\t"
      "v_pk_mul_f32 v[88:89], v[88:89], v[196:197]\n\t"
      "v_pk_mul_f32 v[90:91], v[90:91], v[198:199]\n\t"
      "v_pk_mul_f32 v[92:93], v[92:93], v[212:213]\n\t"
      "v_pk_mul_f32 v[94:95], v[94:95], v[214:215]\n\t"
      "v_cvt_pk_bf16_f32 v128, v80, v81\n\t"
      "v_cvt_pk_bf16_f32 v129, v82, v83\n\t"
      "v_cvt_pk_bf16_f32 v130, v84, v85\n\t"
      "v_cvt_pk_bf16_f32 v131, v86, v87\n\t"
      "v_cvt_pk_bf16_f32 v132, v88, v89\n\t"
      "v_cvt_pk_bf16_f32 v133, v90, v91\n\t"
      "v_cvt_pk_bf16_f32 v134, v92, v93\n\t"
      "v_cvt_pk_bf16_f32 v135, v94, v95\n\t"
      "v_pk_mul_f32 v[80:81], v[136:137], s[88:89]\n\t"
      "v_pk_mul_f32 v[82:83], v[138:139], s[88:89]\n\t"
      "v_pk_mul_f32 v[84:85], v[152:153], s[88:89]\n\t"
      "v_pk_mul_f32 v[86:87], v[154:155], s[88:89]\n\t"
      "v_pk_mul_f32 v[88:89], v[140:141], s[88:89]\n\t"
      "v_pk_mul_f32 v[90:91], v[142:143], s[88:89]\n\t"
      "v_pk_mul_f32 v[92:93], v[156:157], s[88:89]\n\t"
      "v_pk_mul_f32 v[94:95], v[158:159], s[88:89]\n\t"
      "v_exp_f32 v80, v80\n\t"
      "v_exp_f32 v81, v81\n\t"
      "v_exp_f32 v82, v82\n\t"
      "v_exp_f32 v83, v83\n\t"
      "v_exp_f32 v84, v84\n\t"
      "v_exp_f32 v85, v85\n\t"
      "v_exp_f32 v86, v86\n\t"
      "v_exp_f32 v87, v87\n\t"
      "v_exp_f32 v88, v88\n\t"
      "v_exp_f32 v89, v89\n\t"
      "v_exp_f32 v90, v90\n\t"
      "v_exp_f32 v91, v91\n\t"
      "v_exp_f32 v92, v92\n\t"
      "v_exp_f32 v93, v93\n\t"
      "v_exp_f32 v94, v94\n\t"
      "v_exp_f32 v95, v95\n\t"
      "v_pk_fma_f32 v[80:81], v[80:81], s[86:87], s[86:87]\n\t"
      "v_pk_fma_f32 v[82:83], v[82:83], s[86:87], s[86:87]\n\t"
      "v_pk_fma_f32 v[84:85], v[84:85], s[86:87], s[86:87]\n\t"
      "v_pk_fma_f32 v[86:87], v[86:87], s[86:87], s[86:87]\n\t"
      "v_pk_fma_f32 v[88:89], v[88:89], s[86:87], s[86:87]\n\t"
      "v_pk_fma_f32 v[90:91], v[90:91], s[86:87], s[86:87]\n\t"
      "v_pk_fma_f32 v[92:93], v[92:93], s[86:87], s[86:87]\n\t"
      "v_pk_fma_f32 v[94:95], v[94:95], s[86:87], s[86:87]\n\t"
      "v_rcp_f32 v80, v80\n\t"
      "v_rcp_f32 v81, v81\n\t"
      "v_rcp_f32 v82, v82\n\t"
      "v_rcp_f32 v83, v83\n\t"
      "v_rcp_f32 v84, v84\n\t"
      "v_rcp_f32 v85, v85\n\t"
      "v_rcp_f32 v86, v86\n\t"
      "v_rcp_f32 v87, v87\n\t"
      "v_rcp_f32 v88, v88\n\t"
      "v_rcp_f32 v89, v89\n\t"
      "v_rcp_f32 v90, v90\n\t"
      "v_rcp_f32 v91, v91\n\t"
      "v_rcp_f32 v92, v92\n\t"
      "v_rcp_f32 v93, v93\n\t"
      "v_rcp_f32 v94, v94\n\t"
      "v_rcp_f32 v95, v95\n\t"
      "v_pk_mul_f32 v[80:81], v[80:81], v[200:201]\n\t"
      "v_pk_mul_f32 v[82:83], v[82:83], v[202:203]\n\t"
      "v_pk_mul_f32 v[84:85], v[84:85], v[216:217]\n\t"
      "v_pk_mul_f32 v[86:87], v[86:87], v[218:219]\n\t"
      "v_pk_mul_f32 v[88:89], v[88:89], v[204:205]\n\t"
      "v_pk_mul_f32 v[90:91], v[90:91], v[206:207]\n\t"
      "v_pk_mul_f32 v[92:93], v[92:93], v[220:221]\n\t"
      "v_pk_mul_f32 v[94:95], v[94:95], v[222:223]\n\t"
      "v_cvt_pk_bf16_f32 v136, v80, v81\n\t"
      "v_cvt_pk_bf16_f32 v137, v82, v83\n\t"
      "v_cvt_pk_bf16_f32 v138, v84, v85\n\t"
      "v_cvt_pk_bf16_f32 v139, v86, v87\n\t"
      "v_cvt_pk_bf16_f32 v140, v88, v89\n\t"
      "v_cvt_pk_bf16_f32 v141, v90, v91\n\t"
      "v_cvt_pk_bf16_f32 v142, v92, v93\n\t"
      "v_cvt_pk_bf16_f32 v143, v94, v95\n\t"
      "v_pk_mul_f32 v[80:81], v[160:161], s[88:89]\n\t"
      "v_pk_mul_f32 v[82:83], v[162:163], s[88:89]\n\t"
      "v_pk_mul_f32 v[84:85], v[176:177], s[88:89]\n\t"
      "v_pk_mul_f32 v[86:87], v[178:179], s[88:89]\n\t"
      "v_pk_mul_f32 v[88:89], v[164:165], s[88:89]\n\t"
      "v_pk_mul_f32 v[90:91], v[166:167], s[88:89]\n\t"
      "v_pk_mul_f32 v[92:93], v[180:181], s[88:89]\n\t"
      "v_pk_mul_f32 v[94:95], v[182:183], s[88:89]\n\t"
      "v_exp_f32 v80, v80\n\t"
      "v_exp_f32 v81, v81\n\t"
      "v_exp_f32 v82, v82\n\t"
      "v_exp_f32 v83, v83\n\t"
      "v_exp_f32 v84, v84\n\t"
      "v_exp_f32 v85, v85\n\t"
      "v_exp_f32 v86, v86\n\t"
      "v_exp_f32 v87, v87\n\t"
      "v_exp_f32 v88, v88\n\t"
      "v_exp_f32 v89, v89\n\t"
      "v_exp_f32 v90, v90\n\t"
      "v_exp_f32 v91, v91\n\t"
      "v_exp_f32 v92, v92\n\t"
      "v_exp_f32 v93, v93\n\t"
      "v_exp_f32 v94, v94\n\t"
      "v_exp_f32 v95, v95\n\t"
      "v_pk_fma_f32 v[80:81], v[80:81], s[86:87], s[86:87]\n\t"
      "v_pk_fma_f32 v[82:83], v[82:83], s[86:87], s[86:87]\n\t"
      "v_pk_fma_f32 v[84:85], v[84:85], s[86:87], s[86:87]\n\t"
      "v_pk_fma_f32 v[86:87], v[86:87], s[86:87], s[86:87]\n\t"
      "v_pk_fma_f32 v[88:89], v[88:89], s[86:87], s[86:87]\n\t"
      "v_pk_fma_f32 v[90:91], v[90:91], s[86:87], s[86:87]\n\t"
      "v_pk_fma_f32 v[92:93], v[92:93], s[86:87], s[86:87]\n\t"
      "v_pk_fma_f32 v[94:95], v[94:95], s[86:87], s[86:87]\n\t"
      "v_rcp_f32 v80, v80\n\t"
      "v_rcp_f32 v81, v81\n\t"
      "v_rcp_f32 v82, v82\n\t"
      "v_rcp_f32 v83, v83\n\t"
      "v_rcp_f32 v84, v84\n\t"
      "v_rcp_f32 v85, v85\n\t"
      "v_rcp_f32 v86, v86\n\t"
      "v_rcp_f32 v87, v87\n\t"
      "v_rcp_f32 v88, v88\n\t"
      "v_rcp_f32 v89, v89\n\t"
      "v_rcp_f32 v90, v90\n\t"
      "v_rcp_f32 v91, v91\n\t"
      "v_rcp_f32 v92, v92\n\t"
      "v_rcp_f32 v93, v93\n\t"
      "v_rcp_f32 v94, v94\n\t"
      "v_rcp_f32 v95, v95\n\t"
      "v_pk_mul_f32 v[80:81], v[80:81], v[224:225]\n\t"
      "v_pk_mul_f32 v[82:83], v[82:83], v[226:227]\n\t"
      "v_pk_mul_f32 v[84:85], v[84:85], v[240:241]\n\t"
      "v_pk_mul_f32 v[86:87], v[86:87], v[242:243]\n\t"
      "v_pk_mul_f32 v[88:89], v[88:89], v[228:229]\n\t"
      "v_pk_mul_f32 v[90:91], v[90:91], v[230:231]\n\t"
      "v_pk_mul_f32 v[92:93], v[92:93], v[244:245]\n\t"
      "v_pk_mul_f32 v[94:95], v[94:95], v[246:247]\n\t"
      "v_cvt_pk_bf16_f32 v160, v80, v81\n\t"
      "v_cvt_pk_bf16_f32 v161, v82, v83\n\t"
      "v_cvt_pk_bf16_f32 v162, v84, v85\n\t"
      "v_cvt_pk_bf16_f32 v163, v86, v87\n\t"
      "v_cvt_pk_bf16_f32 v164, v88, v89\n\t"
      "v_cvt_pk_bf16_f32 v165, v90, v91\n\t"
      "v_cvt_pk_bf16_f32 v166, v92, v93\n\t"
      "v_cvt_pk_bf16_f32 v167, v94, v95\n\t"
      "v_pk_mul_f32 v[80:81], v[168:169], s[88:89]\n\t"
      "v_pk_mul_f32 v[82:83], v[170:171], s[88:89]\n\t"
      "v_pk_mul_f32 v[84:85], v[184:185], s[88:89]\n\t"
      "v_pk_mul_f32 v[86:87], v[186:187], s[88:89]\n\t"
      "v_pk_mul_f32 v[88:89], v[172:173], s[88:89]\n\t"
      "v_pk_mul_f32 v[90:91], v[174:175], s[88:89]\n\t"
      "v_pk_mul_f32 v[92:93], v[188:189], s[88:89]\n\t"
      "v_pk_mul_f32 v[94:95], v[190:191], s[88:89]\n\t"
      "v_exp_f32 v80, v80\n\t"
      "v_exp_f32 v81, v81\n\t"
      "v_exp_f32 v82, v82\n\t"
      "v_exp_f32 v83, v83\n\t"
      "v_exp_f32 v84, v84\n\t"
      "v_exp_f32 v85, v85\n\t"
      "v_exp_f32 v86, v86\n\t"
      "v_exp_f32 v87, v87\n\t"
      "v_exp_f32 v88, v88\n\t"
      "v_exp_f32 v89, v89\n\t"
      "v_exp_f32 v90, v90\n\t"
      "v_exp_f32 v91, v91\n\t"
      "v_exp_f32 v92, v92\n\t"
      "v_exp_f32 v93, v93\n\t"
      "v_exp_f32 v94, v94\n\t"
      "v_exp_f32 v95, v95\n\t"
      "v_pk_fma_f32 v[80:81], v[80:81], s[86:87], s[86:87]\n\t"
      "v_pk_fma_f32 v[82:83], v[82:83], s[86:87], s[86:87]\n\t"
      "v_pk_fma_f32 v[84:85], v[84:85], s[86:87], s[86:87]\n\t"
      "v_pk_fma_f32 v[86:87], v[86:87], s[86:87], s[86:87]\n\t"
      "v_pk_fma_f32 v[88:89], v[88:89], s[86:87], s[86:87]\n\t"
      "v_pk_fma_f32 v[90:91], v[90:91], s[86:87], s[86:87]\n\t"
      "v_pk_fma_f32 v[92:93], v[92:93], s[86:87], s[86:87]\n\t"
      "v_pk_fma_f32 v[94:95], v[94:95], s[86:87], s[86:87]\n\t"
      "v_rcp_f32 v80, v80\n\t"
      "v_rcp_f32 v81, v81\n\t"
      "v_rcp_f32 v82, v82\n\t"
      "v_rcp_f32 v83, v83\n\t"
      "v_rcp_f32 v84, v84\n\t"
      "v_rcp_f32 v85, v85\n\t"
      "v_rcp_f32 v86, v86\n\t"
      "v_rcp_f32 v87, v87\n\t"
      "v_rcp_f32 v88, v88\n\t"
      "v_rcp_f32 v89, v89\n\t"
      "v_rcp_f32 v90, v90\n\t"
      "v_rcp_f32 v91, v91\n\t"
      "v_rcp_f32 v92, v92\n\t"
      "v_rcp_f32 v93, v93\n\t"
      "v_rcp_f32 v94, v94\n\t"
      "v_rcp_f32 v95, v95\n\t"
      "v_pk_mul_f32 v[80:81], v[80:81], v[232:233]\n\t"
      "v_pk_mul_f32 v[82:83], v[82:83], v[234:235]\n\t"
      "v_pk_mul_f32 v[84:85], v[84:85], v[248:249]\n\t"
      "v_pk_mul_f32 v[86:87], v[86:87], v[250:251]\n\t"
      "v_pk_mul_f32 v[88:89], v[88:89], v[236:237]\n\t"
      "v_pk_mul_f32 v[90:91], v[90:91], v[238:239]\n\t"
      "v_pk_mul_f32 v[92:93], v[92:93], v[252:253]\n\t"
      "v_pk_mul_f32 v[94:95], v[94:95], v[254:255]\n\t"
      "v_cvt_pk_bf16_f32 v168, v80, v81\n\t"
      "v_cvt_pk_bf16_f32 v169, v82, v83\n\t"
      "v_cvt_pk_bf16_f32 v170, v84, v85\n\t"
      "v_cvt_pk_bf16_f32 v171, v86, v87\n\t"
      "v_cvt_pk_bf16_f32 v172, v88, v89\n\t"
      "v_cvt_pk_bf16_f32 v173, v90, v91\n\t"
      "v_cvt_pk_bf16_f32 v174, v92, v93\n\t"
      "v_cvt_pk_bf16_f32 v175, v94, v95\n\t"
      "s_waitcnt vmcnt(8)\n\t"
      "s_add_u32 s93, s55, 0\n\t"
      "buffer_store_dwordx4 v[128:131], v0, s[56:59], s93 offen sc1\n\t"
      "buffer_store_dwordx4 v[132:135], v0, s[56:59], s93 offen offset:1024 sc1\n\t"
      "buffer_store_dwordx4 v[136:139], v0, s[56:59], s93 offen offset:2048 sc1\n\t"
      "buffer_store_dwordx4 v[140:143], v0, s[56:59], s93 offen offset:3072 sc1\n\t"
      "s_add_u32 s93, s55, 0x1000\n\t"
      "buffer_store_dwordx4 v[160:163], v0, s[56:59], s93 offen sc1\n\t"
      "buffer_store_dwordx4 v[164:167], v0, s[56:59], s93 offen offset:1024 sc1\n\t"
      "buffer_store_dwordx4 v[168:171], v0, s[56:59], s93 offen offset:2048 sc1\n\t"
      "buffer_store_dwordx4 v[172:175], v0, s[56:59], s93 offen offset:3072 sc1\n\t"
      "s_branch K1common\n\t"
      "K1skip:\n\t"
      "s_mov_b32 m0, s83\n\t"
      "s_nop 0\n\t"
      "buffer_load_dwordx4 v4, s[60:63], s81 offen lds\n\t"
      "s_add_u32 m0, s83, 0x380\n\t"
      "s_nop 0\n\t"
      "buffer_load_dwordx4 v4, s[60:63], s81 offen offset:128 lds\n\t"
      "s_add_u32 m0, s83, 0x1000\n\t"
      "s_nop 0\n\t"
      "buffer_load_dwordx4 v5, s[60:63], s81 offen lds\n\t"
      "s_add_u32 m0, s83, 0x1380\n\t"
      "s_nop 0\n\t"
      "buffer_load_dwordx4 v5, s[60:63], s81 offen offset:128 lds\n\t"
      "s_add_u32 s81, s81, 0x80000\n\t"
      "s_add_u32 s82, s82, 0x36000\n\t"
      "s_add_u32 s83, s83, 0x8000\n\t"
      "s_mov_b32 m0, s83\n\t"
      "s_nop 0\n\t"
      "buffer_load_dwordx4 v4, s[60:63], s81 offen lds\n\t"
      "s_add_u32 m0, s83, 0x380\n\t"
      "s_nop 0\n\t"
      "buffer_load_dwordx4 v4, s[60:63], s81 offen offset:128 lds\n\t"
      "s_add_u32 m0, s83, 0x1000\n\t"
      "s_nop 0\n\t"
      "buffer_load_dwordx4 v5, s[60:63], s81 offen lds\n\t"
      "s_add_u32 m0, s83, 0x1380\n\t"
      "s_nop 0\n\t"
      "buffer_load_dwordx4 v5, s[60:63], s81 offen offset:128 lds\n\t"
      "v_lshrrev_b32 v14, 2, v0\n\t"
      "v_and_b32 v14, 0x7c, v14\n\t"
      "s_mov_b32 s46, 0\n\t"
      "K1plQ:\n\t"
      "buffer_load_dword v15, v14, s[56:59], s50 offen sc1\n\t"
      "s_waitcnt vmcnt(0)\n\t"
      "v_cmp_ne_u32 vcc, s52, v15\n\t"
      "s_nop 1\n\t"
      "s_cbranch_vccz K1plQd\n\t"
      "s_sleep 2\n\t"
      "s_add_u32 s46, s46, 1\n\t"
      "s_cmp_lt_u32 s46, 0x40000\n\t"
      "s_cbranch_scc1 K1plQ\n\t"
      "K1plQd:\n\t"
      "buffer_load_dwordx4 v[48:51], v0, s[56:59], s80 offen sc1\n\t"
      "buffer_load_dwordx4 v[52:55], v0, s[56:59], s80 offen offset:1024 sc1\n\t"
      "buffer_load_dwordx4 v[56:59], v0, s[56:59], s80 offen offset:2048 sc1\n\t"
      "buffer_load_dwordx4 v[60:63], v0, s[56:59], s80 offen offset:3072 sc1\n\t"
      "s_add_u32 s80, s80, 0x1000\n\t"
      "buffer_load_dwordx4 v[64:67], v0, s[56:59], s80 offen sc1\n\t"
      "buffer_load_dwordx4 v[68:71], v0, s[56:59], s80 offen offset:1024 sc1\n\t"
      "buffer_load_dwordx4 v[72:75], v0, s[56:59], s80 offen offset:2048 sc1\n\t"
      "buffer_load_dwordx4 v[76:79], v0, s[56:59], s80 offen offset:3072 sc1\n\t"
      "s_add_u32 s80, s80, 0x1000\n\t"
      "buffer_load_dwordx4 v[80:83], v0, s[56:59], s80 offen sc1\n\t"
      "buffer_load_dwordx4 v[84:87], v0, s[56:59], s80 offen offset:1024 sc1\n\t"
      "buffer_load_dwordx4 v[88:91], v0, s[56:59], s80 offen offset:2048 sc1\n\t"
      "buffer_load_dwordx4 v[92:95], v0, s[56:59], s80 offen offset:3072 sc1\n\t"
      "v_accvgpr_write_b32 a0, 0\n\t"
      "v_accvgpr_write_b32 a1, 0\n\t"
      "v_accvgpr_write_b32 a2, 0\n\t"
      "v_accvgpr_write_b32 a3, 0\n\t"
      "v_accvgpr_write_b32 a4, 0\n\t"
      "v_accvgpr_write_b32 a5, 0\n\t"
      "v_accvgpr_write_b32 a6, 0\n\t"
      "v_accvgpr_write_b32 a7, 0\n\t"
      "v_accvgpr_write_b32 a8, 0\n\t"
      "v_accvgpr_write_b32 a9, 0\n\t"
      "v_accvgpr_write_b32 a10, 0\n\t"
      "v_accvgpr_write_b32 a11, 0\n\t"
      "v_accvgpr_write_b32 a12, 0\n\t"
      "v_accvgpr_write_b32 a13, 0\n\t"
      "v_accvgpr_write_b32 a14, 0\n\t"
      "v_accvgpr_write_b32 a15, 0\n\t"
      "v_accvgpr_write_b32 a16, 0\n\t"
      "v_accvgpr_write_b32 a17, 0\n\t"
      "v_accvgpr_write_b32 a18, 0\n\t"
      "v_accvgpr_write_b32 a19, 0\n\t"
      "v_accvgpr_write_b32 a20, 0\n\t"
      "v_accvgpr_write_b32 a21, 0\n\t"
      "v_accvgpr_write_b32 a22, 0\n\t"
      "v_accvgpr_write_b32 a23, 0\n\t"
      "v_accvgpr_write_b32 a24, 0\n\t"
      "v_accvgpr_write_b32 a25, 0\n\t"
      "v_accvgpr_write_b32 a26, 0\n\t"
      "v_accvgpr_write_b32 a27, 0\n\t"
      "v_accvgpr_write_b32 a28, 0\n\t"
      "v_accvgpr_write_b32 a29, 0\n\t"
      "v_accvgpr_write_b32 a30, 0\n\t"
      "v_accvgpr_write_b32 a31, 0\n\t"
      "v_accvgpr_write_b32 a32, 0\n\t"
      "v_accvgpr_write_b32 a33, 0\n\t"
      "v_accvgpr_write_b32 a34, 0\n\t"
      "v_accvgpr_write_b32 a35, 0\n\t"
      "v_accvgpr_write_b32 a36, 0\n\t"
      "v_accvgpr_write_b32 a37, 0\n\t"
      "v_accvgpr_write_b32 a38, 0\n\t"
      "v_accvgpr_write_b32 a39, 0\n\t"
      "v_accvgpr_write_b32 a40, 0\n\t"
      "v_accvgpr_write_b32 a41, 0\n\t"
      "v_accvgpr_write_b32 a42, 0\n\t"
      "v_accvgpr_write_b32 a43, 0\n\t"
      "v_accvgpr_write_b32 a44, 0\n\t"
      "v_accvgpr_write_b32 a45, 0\n\t"
      "v_accvgpr_write_b32 a46, 0\n\t"
      "v_accvgpr_write_b32 a47, 0\n\t"
      "v_accvgpr_write_b32 a48, 0\n\t"
      "v_accvgpr_write_b32 a49, 0\n\t"
      "v_accvgpr_write_b32 a50, 0\n\t"
      "v_accvgpr_write_b32 a51, 0\n\t"
      "v_accvgpr_write_b32 a52, 0\n\t"
      "v_accvgpr_write_b32 a53, 0\n\t"
      "v_accvgpr_write_b32 a54, 0\n\t"
      "v_accvgpr_write_b32 a55, 0\n\t"
      "v_accvgpr_write_b32 a56, 0\n\t"
      "v_accvgpr_write_b32 a57, 0\n\t"
      "v_accvgpr_write_b32 a58, 0\n\t"
      "v_accvgpr_write_b32 a59, 0\n\t"
      "v_accvgpr_write_b32 a60, 0\n\t"
      "v_accvgpr_write_b32 a61, 0\n\t"
      "v_accvgpr_write_b32 a62, 0\n\t"
      "v_accvgpr_write_b32 a63, 0\n\t"
      "v_accvgpr_write_b32 a64, 0\n\t"
      "v_accvgpr_write_b32 a65, 0\n\t"
      "v_accvgpr_write_b32 a66, 0\n\t"
      "v_accvgpr_write_b32 a67, 0\n\t"
      "v_accvgpr_write_b32 a68, 0\n\t"
      "v_accvgpr_write_b32 a69, 0\n\t"
      "v_accvgpr_write_b32 a70, 0\n\t"
      "v_accvgpr_write_b32 a71, 0\n\t"
      "v_accvgpr_write_b32 a72, 0\n\t"
      "v_accvgpr_write_b32 a73, 0\n\t"
      "v_accvgpr_write_b32 a74, 0\n\t"
      "v_accvgpr_write_b32 a75, 0\n\t"
      "v_accvgpr_write_b32 a76, 0\n\t"
      "v_accvgpr_write_b32 a77, 0\n\t"
      "v_accvgpr_write_b32 a78, 0\n\t"
      "v_accvgpr_write_b32 a79, 0\n\t"
      "v_accvgpr_write_b32 a80, 0\n\t"
      "v_accvgpr_write_b32 a81, 0\n\t"
      "v_accvgpr_write_b32 a82, 0\n\t"
      "v_accvgpr_write_b32 a83, 0\n\t"
      "v_accvgpr_write_b32 a84, 0\n\t"
      "v_accvgpr_write_b32 a85, 0\n\t"
      "v_accvgpr_write_b32 a86, 0\n\t"
      "v_accvgpr_write_b32 a87, 0\n\t"
      "v_accvgpr_write_b32 a88, 0\n\t"
      "v_accvgpr_write_b32 a89, 0\n\t"
      "v_accvgpr_write_b32 a90, 0\n\t"
      "v_accvgpr_write_b32 a91, 0\n\t"
      "v_accvgpr_write_b32 a92, 0\n\t"
      "v_accvgpr_write_b32 a93, 0\n\t"
      "v_accvgpr_write_b32 a94, 0\n\t"
      "v_accvgpr_write_b32 a95, 0\n\t"
      "v_accvgpr_write_b32 a96, 0\n\t"
      "v_accvgpr_write_b32 a97, 0\n\t"
      "v_accvgpr_write_b32 a98, 0\n\t"
      "v_accvgpr_write_b32 a99, 0\n\t"
      "v_accvgpr_write_b32 a100, 0\n\t"
      "v_accvgpr_write_b32 a101, 0\n\t"
      "v_accvgpr_write_b32 a102, 0\n\t"
      "v_accvgpr_write_b32 a103, 0\n\t"
      "v_accvgpr_write_b32 a104, 0\n\t"
      "v_accvgpr_write_b32 a105, 0\n\t"
      "v_accvgpr_write_b32 a106, 0\n\t"
      "v_accvgpr_write_b32 a107, 0\n\t"
      "v_accvgpr_write_b32 a108, 0\n\t"
      "v_accvgpr_write_b32 a109, 0\n\t"
      "v_accvgpr_write_b32 a110, 0\n\t"
      "v_accvgpr_write_b32 a111, 0\n\t"
      "v_accvgpr_write_b32 a112, 0\n\t"
      "v_accvgpr_write_b32 a113, 0\n\t"
      "v_accvgpr_write_b32 a114, 0\n\t"
      "v_accvgpr_write_b32 a115, 0\n\t"
      "v_accvgpr_write_b32 a116, 0\n\t"
      "v_accvgpr_write_b32 a117, 0\n\t"
      "v_accvgpr_write_b32 a118, 0\n\t"
      "v_accvgpr_write_b32 a119, 0\n\t"
      "v_accvgpr_write_b32 a120, 0\n\t"
      "v_accvgpr_write_b32 a121, 0\n\t"
      "v_accvgpr_write_b32 a122, 0\n\t"
      "v_accvgpr_write_b32 a123, 0\n\t"
      "v_accvgpr_write_b32 a124, 0\n\t"
      "v_accvgpr_write_b32 a125, 0\n\t"
      "v_accvgpr_write_b32 a126, 0\n\t"
      "v_accvgpr_write_b32 a127, 0\n\t"
      "s_waitcnt vmcnt(16)\n\t"
      "s_barrier\n\t"
      "ds_read_b64_tr_b16 v[16:17], v1\n\t"
      "ds_read_b64_tr_b16 v[18:19], v1 offset:256\n\t"
      "ds_read_b64_tr_b16 v[20:21], v1 offset:8\n\t"
      "ds_read_b64_tr_b16 v[22:23], v1 offset:264\n\t"
      "ds_read_b64_tr_b16 v[24:25], v1 offset:512\n\t"
      "ds_read_b64_tr_b16 v[26:27], v1 offset:768\n\t"
      "ds_read_b64_tr_b16 v[28:29], v1 offset:520\n\t"
      "ds_read_b64_tr_b16 v[30:31], v1 offset:776\n\t"
      "s_mov_b32 s92, 4\n\t"
      "K1projq:\n\t"
      "s_waitcnt vmcnt(8) lgkmcnt(6)\n\t"
      "v_mfma_f32_16x16x32_bf16 a[0:3], v[16:19], v[48:51], a[0:3]\n\t"
      "ds_read_b64_tr_b16 v[32:33], v1 offset:1024\n\t"
      "v_mfma_f32_16x16x32_bf16 a[4:7], v[16:19], v[52:55], a[4:7]\n\t"
      "ds_read_b64_tr_b16 v[34:35], v1 offset:1280\n\t"
      "s_add_u32 s80, s80, 0x1000\n\t"
      "v_mfma_f32_16x16x32_bf16 a[8:11], v[16:19], v[56:59], a[8:11]\n\t"
      "buffer_load_dwordx4 v[96:99], v0, s[56:59], s80 offen sc1\n\t"
      "v_mfma_f32_16x16x32_bf16 a[12:15], v[16:19], v[60:63], a[12:15]\n\t"
      "s_waitcnt lgkmcnt(6)\n\t"
      "v_mfma_f32_16x16x32_bf16 a[16:19], v[20:23], v[48:51], a[16:19]\n\t"
      "ds_read_b64_tr_b16 v[36:37], v1 offset:1032\n\t"
      "v_mfma_f32_16x16x32_bf16 a[20:23], v[20:23], v[52:55], a[20:23]\n\t"
      "ds_read_b64_tr_b16 v[38:39], v1 offset:1288\n\t"
      "buffer_load_dwordx4 v[100:103], v0, s[56:59], s80 offen offset:1024 sc1\n\t"
      "v_mfma_f32_16x16x32_bf16 a[24:27], v[20:23], v[56:59], a[24:27]\n\t"
      "v_mfma_f32_16x16x32_bf16 a[28:31], v[20:23], v[60:63], a[28:31]\n\t"
      "s_waitcnt lgkmcnt(6)\n\t"
      "v_mfma_f32_16x16x32_bf16 a[32:35], v[24:27], v[48:51], a[32:35]\n\t"
      "ds_read_b64_tr_b16 v[40:41], v1 offset:1536\n\t"
      "buffer_load_dwordx4 v[104:107], v0, s[56:59], s80 offen offset:2048 sc1\n\t"
      "v_mfma_f32_16x16x32_bf16 a[36:39], v[24:27], v[52:55], a[36:39]\n\t"
      "ds_read_b64_tr_b16 v[42:43], v1 offset:1792\n\t"
      "v_mfma_f32_16x16x32_bf16 a[40:43], v[24:27], v[56:59], a[40:43]\n\t"
      "v_mfma_f32_16x16x32_bf16 a[44:47], v[24:27], v[60:63], a[44:47]\n\t"
      "buffer_load_dwordx4 v[108:111], v0, s[56:59], s80 offen offset:3072 sc1\n\t"
      "s_waitcnt lgkmcnt(6)\n\t"
      "v_mfma_f32_16x16x32_bf16 a[48:51], v[28:31], v[48:51], a[48:51]\n\t"
      "ds_read_b64_tr_b16 v[44:45], v1 offset:1544\n\t"
      "v_mfma_f32_16x16x32_bf16 a[52:55], v[28:31], v[52:55], a[52:55]\n\t"
      "ds_read_b64_tr_b16 v[46:47], v1 offset:1800\n\t"
      "s_add_u32 s81, s81, 0x80000\n\t"
      "s_add_u32 s83, s83, 0x8000\n\t"
      "s_cmp_ge_u32 s83, s94\n\t"
      "s_cselect_b32 s93, 0x18000, 0\n\t"
      "s_sub_u32 s83, s83, s93\n\t"
      "s_mov_b32 m0, s83\n\t"
      "v_mfma_f32_16x16x32_bf16 a[56:59], v[28:31], v[56:59], a[56:59]\n\t"
      "buffer_load_dwordx4 v4, s[60:63], s81 offen lds\n\t"
      "v_mfma_f32_16x16x32_bf16 a[60:63], v[28:31], v[60:63], a[60:63]\n\t"
      "s_waitcnt lgkmcnt(6)\n\t"
      "v_mfma_f32_16x16x32_bf16 a[64:67], v[32:35], v[48:51], a[64:67]\n\t"
      "ds_read_b64_tr_b16 v[16:17], v1 offset:16384\n\t"
      "s_add_u32 m0, s83, 0x380\n\t"
      "v_mfma_f32_16x16x32_bf16 a[68:71], v[32:35], v[52:55], a[68:71]\n\t"
      "ds_read_b64_tr_b16 v[18:19], v1 offset:16640\n\t"
      "buffer_load_dwordx4 v4, s[60:63], s81 offen offset:128 lds\n\t"
      "v_mfma_f32_16x16x32_bf16 a[72:75], v[32:35], v[56:59], a[72:75]\n\t"
      "v_mfma_f32_16x16x32_bf16 a[76:79], v[32:35], v[60:63], a[76:79]\n\t"
      "s_waitcnt lgkmcnt(6)\n\t"
      "v_mfma_f32_16x16x32_bf16 a[80:83], v[36:39], v[48:51], a[80:83]\n\t"
      "ds_read_b64_tr_b16 v[20:21], v1 offset:16392\n\t"
      "v_mfma_f32_16x16x32_bf16 a[84:87], v[36:39], v[52:55], a[84:87]\n\t"
      "ds_read_b64_tr_b16 v[22:23], v1 offset:16648\n\t"
      "v_mfma_f32_16x16x32_bf16 a[88:91], v[36:39], v[56:59], a[88:91]\n\t"
      "v_mfma_f32_16x16x32_bf16 a[92:95], v[36:39], v[60:63], a[92:95]\n\t"
      "s_waitcnt lgkmcnt(6)\n\t"
      "v_mfma_f32_16x16x32_bf16 a[96:99], v[40:43], v[48:51], a[96:99]\n\t"
      "ds_read_b64_tr_b16 v[24:25], v1 offset:16896\n\t"
      "v_mfma_f32_16x16x32_bf16 a[100:103], v[40:43], v[52:55], a[100:103]\n\t"
      "ds_read_b64_tr_b16 v[26:27], v1 offset:17152\n\t"
      "v_mfma_f32_16x16x32_bf16 a[104:107], v[40:43], v[56:59], a[104:107]\n\t"
      "v_mfma_f32_16x16x32_bf16 a[108:111], v[40:43], v[60:63], a[108:111]\n\t"
      "s_waitcnt lgkmcnt(6)\n\t"
      "v_mfma_f32_16x16x32_bf16 a[112:115], v[44:47], v[48:51], a[112:115]\n\t"
      "ds_read_b64_tr_b16 v[28:29], v1 offset:16904\n\t"
      "v_mfma_f32_16x16x32_bf16 a[116:119], v[44:47], v[52:55], a[116:119]\n\t"
      "ds_read_b64_tr_b16 v[30:31], v1 offset:17160\n\t"
      "v_mfma_f32_16x16x32_bf16 a[120:123], v[44:47], v[56:59], a[120:123]\n\t"
      "v_mfma_f32_16x16x32_bf16 a[124:127], v[44:47], v[60:63], a[124:127]\n\t"
      "s_waitcnt vmcnt(10) lgkmcnt(6)\n\t"
      "v_mfma_f32_16x16x32_bf16 a[0:3], v[16:19], v[64:67], a[0:3]\n\t"
      "ds_read_b64_tr_b16 v[32:33], v1 offset:17408\n\t"
      "v_mfma_f32_16x16x32_bf16 a[4:7], v[16:19], v[68:71], a[4:7]\n\t"
      "ds_read_b64_tr_b16 v[34:35], v1 offset:17664\n\t"
      "s_add_u32 s80, s80, 0x1000\n\t"
      "v_mfma_f32_16x16x32_bf16 a[8:11], v[16:19], v[72:75], a[8:11]\n\t"
      "buffer_load_dwordx4 v[48:51], v0, s[56:59], s80 offen sc1\n\t"
      "v_mfma_f32_16x16x32_bf16 a[12:15], v[16:19], v[76:79], a[12:15]\n\t"
      "s_waitcnt lgkmcnt(6)\n\t"
      "v_mfma_f32_16x16x32_bf16 a[16:19], v[20:23], v[64:67], a[16:19]\n\t"
      "ds_read_b64_tr_b16 v[36:37], v1 offset:17416\n\t"
      "v_mfma_f32_16x16x32_bf16 a[20:23], v[20:23], v[68:71], a[20:23]\n\t"
      "ds_read_b64_tr_b16 v[38:39], v1 offset:17672\n\t"
      "buffer_load_dwordx4 v[52:55], v0, s[56:59], s80 offen offset:1024 sc1\n\t"
      "v_mfma_f32_16x16x32_bf16 a[24:27], v[20:23], v[72:75], a[24:27]\n\t"
      "v_mfma_f32_16x16x32_bf16 a[28:31], v[20:23], v[76:79], a[28:31]\n\t"
      "s_waitcnt lgkmcnt(6)\n\t"
      "v_mfma_f32_16x16x32_bf16 a[32:35], v[24:27], v[64:67], a[32:35]\n\t"
      "ds_read_b64_tr_b16 v[40:41], v1 offset:17920\n\t"
      "buffer_load_dwordx4 v[56:59], v0, s[56:59], s80 offen offset:2048 sc1\n\t"
      "v_mfma_f32_16x16x32_bf16 a[36:39], v[24:27], v[68:71], a[36:39]\n\t"
      "ds_read_b64_tr_b16 v[42:43], v1 offset:18176\n\t"
      "v_mfma_f32_16x16x32_bf16 a[40:43], v[24:27], v[72:75], a[40:43]\n\t"
      "v_mfma_f32_16x16x32_bf16 a[44:47], v[24:27], v[76:79], a[44:47]\n\t"
      "buffer_load_dwordx4 v[60:63], v0, s[56:59], s80 offen offset:3072 sc1\n\t"
      "s_waitcnt lgkmcnt(6)\n\t"
      "v_mfma_f32_16x16x32_bf16 a[48:51], v[28:31], v[64:67], a[48:51]\n\t"
      "ds_read_b64_tr_b16 v[44:45], v1 offset:17928\n\t"
      "v_mfma_f32_16x16x32_bf16 a[52:55], v[28:31], v[68:71], a[52:55]\n\t"
      "ds_read_b64_tr_b16 v[46:47], v1 offset:18184\n\t"
      "s_add_u32 m0, s83, 0x1000\n\t"
      "v_mfma_f32_16x16x32_bf16 a[56:59], v[28:31], v[72:75], a[56:59]\n\t"
      "buffer_load_dwordx4 v5, s[60:63], s81 offen lds\n\t"
      "v_mfma_f32_16x16x32_bf16 a[60:63], v[28:31], v[76:79], a[60:63]\n\t"
      "s_waitcnt lgkmcnt(6)\n\t"
      "v_mfma_f32_16x16x32_bf16 a[64:67], v[32:35], v[64:67], a[64:67]\n\t"
      "s_add_u32 m0, s83, 0x1380\n\t"
      "v_mfma_f32_16x16x32_bf16 a[68:71], v[32:35], v[68:71], a[68:71]\n\t"
      "buffer_load_dwordx4 v5, s[60:63], s81 offen offset:128 lds\n\t"
      "v_mfma_f32_16x16x32_bf16 a[72:75], v[32:35], v[72:75], a[72:75]\n\t"
      "v_mfma_f32_16x16x32_bf16 a[76:79], v[32:35], v[76:79], a[76:79]\n\t"
      "s_waitcnt vmcnt(12) lgkmcnt(0)\n\t"
      "s_barrier\n\t"
      "s_add_u32 s98, s98, 0x8000\n\t"
      "s_cmp_ge_u32 s98, 0x18000\n\t"
      "s_cselect_b32 s97, 0xffff0000, s44\n\t"
      "s_cselect_b32 s98, 0, s98\n\t"
      "v_add_u32 v1, s97, v1\n\t"
      "v_mfma_f32_16x16x32_bf16 a[80:83], v[36:39], v[64:67], a[80:83]\n\t"
      "ds_read_b64_tr_b16 v[16:17], v1\n\t"
      "v_mfma_f32_16x16x32_bf16 a[84:87], v[36:39], v[68:71], a[84:87]\n\t"
      "ds_read_b64_tr_b16 v[18:19], v1 offset:256\n\t"
      "v_mfma_f32_16x16x32_bf16 a[88:91], v[36:39], v[72:75], a[88:91]\n\t"
      "v_mfma_f32_16x16x32_bf16 a[92:95], v[36:39], v[76:79], a[92:95]\n\t"
      "v_mfma_f32_16x16x32_bf16 a[96:99], v[40:43], v[64:67], a[96:99]\n\t"
      "ds_read_b64_tr_b16 v[20:21], v1 offset:8\n\t"
      "v_mfma_f32_16x16x32_bf16 a[100:103], v[40:43], v[68:71], a[100:103]\n\t"
      "ds_read_b64_tr_b16 v[22:23], v1 offset:264\n\t"
      "v_mfma_f32_16x16x32_bf16 a[104:107], v[40:43], v[72:75], a[104:107]\n\t"
      "ds_read_b64_tr_b16 v[24:25], v1 offset:512\n\t"
      "v_mfma_f32_16x16x32_bf16 a[108:111], v[40:43], v[76:79], a[108:111]\n\t"
      "ds_read_b64_tr_b16 v[26:27], v1 offset:768\n\t"
      "v_mfma_f32_16x16x32_bf16 a[112:115], v[44:47], v[64:67], a[112:115]\n\t"
      "ds_read_b64_tr_b16 v[28:29], v1 offset:520\n\t"
      "v_mfma_f32_16x16x32_bf16 a[116:119], v[44:47], v[68:71], a[116:119]\n\t"
      "ds_read_b64_tr_b16 v[30:31], v1 offset:776\n\t"
      "v_mfma_f32_16x16x32_bf16 a[120:123], v[44:47], v[72:75], a[120:123]\n\t"
      "v_mfma_f32_16x16x32_bf16 a[124:127], v[44:47], v[76:79], a[124:127]\n\t"
      "s_waitcnt vmcnt(12) lgkmcnt(6)\n\t"
      "v_mfma_f32_16x16x32_bf16 a[0:3], v[16:19], v[80:83], a[0:3]\n\t"
      "ds_read_b64_tr_b16 v[32:33], v1 offset:1024\n\t"
      "v_mfma_f32_16x16x32_bf16 a[4:7], v[16:19], v[84:87], a[4:7]\n\t"
      "ds_read_b64_tr_b16 v[34:35], v1 offset:1280\n\t"
      "s_add_u32 s80, s80, 0x1000\n\t"
      "v_mfma_f32_16x16x32_bf16 a[8:11], v[16:19], v[88:91], a[8:11]\n\t"
      "buffer_load_dwordx4 v[64:67], v0, s[56:59], s80 offen sc1\n\t"
      "v_mfma_f32_16x16x32_bf16 a[12:15], v[16:19], v[92:95], a[12:15]\n\t"
      "s_waitcnt lgkmcnt(6)\n\t"
      "v_mfma_f32_16x16x32_bf16 a[16:19], v[20:23], v[80:83], a[16:19]\n\t"
      "ds_read_b64_tr_b16 v[36:37], v1 offset:1032\n\t"
      "v_mfma_f32_16x16x32_bf16 a[20:23], v[20:23], v[84:87], a[20:23]\n\t"
      "ds_read_b64_tr_b16 v[38:39], v1 offset:1288\n\t"
      "buffer_load_dwordx4 v[68:71], v0, s[56:59], s80 offen offset:1024 sc1\n\t"
      "v_mfma_f32_16x16x32_bf16 a[24:27], v[20:23], v[88:91], a[24:27]\n\t"
      "v_mfma_f32_16x16x32_bf16 a[28:31], v[20:23], v[92:95], a[28:31]\n\t"
      "s_waitcnt lgkmcnt(6)\n\t"
      "v_mfma_f32_16x16x32_bf16 a[32:35], v[24:27], v[80:83], a[32:35]\n\t"
      "ds_read_b64_tr_b16 v[40:41], v1 offset:1536\n\t"
      "buffer_load_dwordx4 v[72:75], v0, s[56:59], s80 offen offset:2048 sc1\n\t"
      "v_mfma_f32_16x16x32_bf16 a[36:39], v[24:27], v[84:87], a[36:39]\n\t"
      "ds_read_b64_tr_b16 v[42:43], v1 offset:1792\n\t"
      "v_mfma_f32_16x16x32_bf16 a[40:43], v[24:27], v[88:91], a[40:43]\n\t"
      "v_mfma_f32_16x16x32_bf16 a[44:47], v[24:27], v[92:95], a[44:47]\n\t"
      "buffer_load_dwordx4 v[76:79], v0, s[56:59], s80 offen offset:3072 sc1\n\t"
      "s_waitcnt lgkmcnt(6)\n\t"
      "v_mfma_f32_16x16x32_bf16 a[48:51], v[28:31], v[80:83], a[48:51]\n\t"
      "ds_read_b64_tr_b16 v[44:45], v1 offset:1544\n\t"
      "v_mfma_f32_16x16x32_bf16 a[52:55], v[28:31], v[84:87], a[52:55]\n\t"
      "ds_read_b64_tr_b16 v[46:47], v1 offset:1800\n\t"
      "s_add_u32 s81, s81, 0x80000\n\t"
      "s_add_u32 s83, s83, 0x8000\n\t"
      "s_cmp_ge_u32 s83, s94\n\t"
      "s_cselect_b32 s93, 0x18000, 0\n\t"
      "s_sub_u32 s83, s83, s93\n\t"
      "s_mov_b32 m0, s83\n\t"
      "v_mfma_f32_16x16x32_bf16 a[56:59], v[28:31], v[88:91], a[56:59]\n\t"
      "buffer_load_dwordx4 v4, s[60:63], s81 offen lds\n\t"
      "v_mfma_f32_16x16x32_bf16 a[60:63], v[28:31], v[92:95], a[60:63]\n\t"
      "s_waitcnt lgkmcnt(6)\n\t"
      "v_mfma_f32_16x16x32_bf16 a[64:67], v[32:35], v[80:83], a[64:67]\n\t"
      "ds_read_b64_tr_b16 v[16:17], v1 offset:16384\n\t"
      "s_add_u32 m0, s83, 0x380\n\t"
      "v_mfma_f32_16x16x32_bf16 a[68:71], v[32:35], v[84:87], a[68:71]\n\t"
      "ds_read_b64_tr_b16 v[18:19], v1 offset:16640\n\t"
      "buffer_load_dwordx4 v4, s[60:63], s81 offen offset:128 lds\n\t"
      "v_mfma_f32_16x16x32_bf16 a[72:75], v[32:35], v[88:91], a[72:75]\n\t"
      "v_mfma_f32_16x16x32_bf16 a[76:79], v[32:35], v[92:95], a[76:79]\n\t"
      "s_waitcnt lgkmcnt(6)\n\t"
      "v_mfma_f32_16x16x32_bf16 a[80:83], v[36:39], v[80:83], a[80:83]\n\t"
      "ds_read_b64_tr_b16 v[20:21], v1 offset:16392\n\t"
      "v_mfma_f32_16x16x32_bf16 a[84:87], v[36:39], v[84:87], a[84:87]\n\t"
      "ds_read_b64_tr_b16 v[22:23], v1 offset:16648\n\t"
      "v_mfma_f32_16x16x32_bf16 a[88:91], v[36:39], v[88:91], a[88:91]\n\t"
      "v_mfma_f32_16x16x32_bf16 a[92:95], v[36:39], v[92:95], a[92:95]\n\t"
      "s_waitcnt lgkmcnt(6)\n\t"
      "v_mfma_f32_16x16x32_bf16 a[96:99], v[40:43], v[80:83], a[96:99]\n\t"
      "ds_read_b64_tr_b16 v[24:25], v1 offset:16896\n\t"
      "v_mfma_f32_16x16x32_bf16 a[100:103], v[40:43], v[84:87], a[100:103]\n\t"
      "ds_read_b64_tr_b16 v[26:27], v1 offset:17152\n\t"
      "v_mfma_f32_16x16x32_bf16 a[104:107], v[40:43], v[88:91], a[104:107]\n\t"
      "v_mfma_f32_16x16x32_bf16 a[108:111], v[40:43], v[92:95], a[108:111]\n\t"
      "s_waitcnt lgkmcnt(6)\n\t"
      "v_mfma_f32_16x16x32_bf16 a[112:115], v[44:47], v[80:83], a[112:115]\n\t"
      "ds_read_b64_tr_b16 v[28:29], v1 offset:16904\n\t"
      "v_mfma_f32_16x16x32_bf16 a[116:119], v[44:47], v[84:87], a[116:119]\n\t"
      "ds_read_b64_tr_b16 v[30:31], v1 offset:17160\n\t"
      "v_mfma_f32_16x16x32_bf16 a[120:123], v[44:47], v[88:91], a[120:123]\n\t"
      "v_mfma_f32_16x16x32_bf16 a[124:127], v[44:47], v[92:95], a[124:127]\n\t"
      "s_waitcnt vmcnt(14) lgkmcnt(6)\n\t"
      "v_mfma_f32_16x16x32_bf16 a[0:3], v[16:19], v[96:99], a[0:3]\n\t"
      "ds_read_b64_tr_b16 v[32:33], v1 offset:17408\n\t"
      "v_mfma_f32_16x16x32_bf16 a[4:7], v[16:19], v[100:103], a[4:7]\n\t"
      "ds_read_b64_tr_b16 v[34:35], v1 offset:17664\n\t"
      "s_add_u32 s80, s80, 0x1000\n\t"
      "v_mfma_f32_16x16x32_bf16 a[8:11], v[16:19], v[104:107], a[8:11]\n\t"
      "buffer_load_dwordx4 v[80:83], v0, s[56:59], s80 offen sc1\n\t"
      "v_mfma_f32_16x16x32_bf16 a[12:15], v[16:19], v[108:111], a[12:15]\n\t"
      "s_waitcnt lgkmcnt(6)\n\t"
      "v_mfma_f32_16x16x32_bf16 a[16:19], v[20:23], v[96:99], a[16:19]\n\t"
      "ds_read_b64_tr_b16 v[36:37], v1 offset:17416\n\t"
      "v_mfma_f32_16x16x32_bf16 a[20:23], v[20:23], v[100:103], a[20:23]\n\t"
      "ds_read_b64_tr_b16 v[38:39], v1 offset:17672\n\t"
      "buffer_load_dwordx4 v[84:87], v0, s[56:59], s80 offen offset:1024 sc1\n\t"
      "v_mfma_f32_16x16x32_bf16 a[24:27], v[20:23], v[104:107], a[24:27]\n\t"
      "v_mfma_f32_16x16x32_bf16 a[28:31], v[20:23], v[108:111], a[28:31]\n\t"
      "s_waitcnt lgkmcnt(6)\n\t"
      "v_mfma_f32_16x16x32_bf16 a[32:35], v[24:27], v[96:99], a[32:35]\n\t"
      "ds_read_b64_tr_b16 v[40:41], v1 offset:17920\n\t"
      "buffer_load_dwordx4 v[88:91], v0, s[56:59], s80 offen offset:2048 sc1\n\t"
      "v_mfma_f32_16x16x32_bf16 a[36:39], v[24:27], v[100:103], a[36:39]\n\t"
      "ds_read_b64_tr_b16 v[42:43], v1 offset:18176\n\t"
      "v_mfma_f32_16x16x32_bf16 a[40:43], v[24:27], v[104:107], a[40:43]\n\t"
      "v_mfma_f32_16x16x32_bf16 a[44:47], v[24:27], v[108:111], a[44:47]\n\t"
      "buffer_load_dwordx4 v[92:95], v0, s[56:59], s80 offen offset:3072 sc1\n\t"
      "s_waitcnt lgkmcnt(6)\n\t"
      "v_mfma_f32_16x16x32_bf16 a[48:51], v[28:31], v[96:99], a[48:51]\n\t"
      "ds_read_b64_tr_b16 v[44:45], v1 offset:17928\n\t"
      "v_mfma_f32_16x16x32_bf16 a[52:55], v[28:31], v[100:103], a[52:55]\n\t"
      "ds_read_b64_tr_b16 v[46:47], v1 offset:18184\n\t"
      "s_add_u32 m0, s83, 0x1000\n\t"
      "v_mfma_f32_16x16x32_bf16 a[56:59], v[28:31], v[104:107], a[56:59]\n\t"
      "buffer_load_dwordx4 v5, s[60:63], s81 offen lds\n\t"
      "v_mfma_f32_16x16x32_bf16 a[60:63], v[28:31], v[108:111], a[60:63]\n\t"
      "s_waitcnt lgkmcnt(6)\n\t"
      "v_mfma_f32_16x16x32_bf16 a[64:67], v[32:35], v[96:99], a[64:67]\n\t"
      "s_add_u32 m0, s83, 0x1380\n\t"
      "v_mfma_f32_16x16x32_bf16 a[68:71], v[32:35], v[100:103], a[68:71]\n\t"
      "buffer_load_dwordx4 v5, s[60:63], s81 offen offset:128 lds\n\t"
      "v_mfma_f32_16x16x32_bf16 a[72:75], v[32:35], v[104:107], a[72:75]\n\t"
      "v_mfma_f32_16x16x32_bf16 a[76:79], v[32:35], v[108:111], a[76:79]\n\t"
      "s_waitcnt vmcnt(12) lgkmcnt(0)\n\t"
      "s_barrier\n\t"
      "s_add_u32 s98, s98, 0x8000\n\t"
      "s_cmp_ge_u32 s98, 0x18000\n\t"
      "s_cselect_b32 s97, 0xffff0000, s44\n\t"
      "s_cselect_b32 s98, 0, s98\n\t"
      "v_add_u32 v1, s97, v1\n\t"
      "v_mfma_f32_16x16x32_bf16 a[80:83], v[36:39], v[96:99], a[80:83]\n\t"
      "ds_read_b64_tr_b16 v[16:17], v1\n\t"
      "v_mfma_f32_16x16x32_bf16 a[84:87], v[36:39], v[100:103], a[84:87]\n\t"
      "ds_read_b64_tr_b16 v[18:19], v1 offset:256\n\t"
      "v_mfma_f32_16x16x32_bf16 a[88:91], v[36:39], v[104:107], a[88:91]\n\t"
      "v_mfma_f32_16x16x32_bf16 a[92:95], v[36:39], v[108:111], a[92:95]\n\t"
      "v_mfma_f32_16x16x32_bf16 a[96:99], v[40:43], v[96:99], a[96:99]\n\t"
      "ds_read_b64_tr_b16 v[20:21], v1 offset:8\n\t"
      "v_mfma_f32_16x16x32_bf16 a[100:103], v[40:43], v[100:103], a[100:103]\n\t"
      "ds_read_b64_tr_b16 v[22:23], v1 offset:264\n\t"
      "v_mfma_f32_16x16x32_bf16 a[104:107], v[40:43], v[104:107], a[104:107]\n\t"
      "ds_read_b64_tr_b16 v[24:25], v1 offset:512\n\t"
      "v_mfma_f32_16x16x32_bf16 a[108:111], v[40:43], v[108:111], a[108:111]\n\t"
      "ds_read_b64_tr_b16 v[26:27], v1 offset:768\n\t"
      "v_mfma_f32_16x16x32_bf16 a[112:115], v[44:47], v[96:99], a[112:115]\n\t"
      "ds_read_b64_tr_b16 v[28:29], v1 offset:520\n\t"
      "v_mfma_f32_16x16x32_bf16 a[116:119], v[44:47], v[100:103], a[116:119]\n\t"
      "ds_read_b64_tr_b16 v[30:31], v1 offset:776\n\t"
      "v_mfma_f32_16x16x32_bf16 a[120:123], v[44:47], v[104:107], a[120:123]\n\t"
      "v_mfma_f32_16x16x32_bf16 a[124:127], v[44:47], v[108:111], a[124:127]\n\t"
      "s_sub_u32 s92, s92, 1\n\t"
      "s_cmp_lg_u32 s92, 0\n\t"
      "s_cbranch_scc1 K1projq\n\t"
      "s_getpc_b64 s[90:91]\n\t"
      "91:\n\t"
      "s_lshl_b32 s93, s99, 13\n\t"
      "s_add_u32 s93, s93, K1common-91b-8192\n\t"
      "s_cmp_eq_u32 s99, 0\n\t"
      "s_cselect_b32 s93, 0, s93\n\t"
      "v_lshl_add_u32 v14, v0, 3, s93\n\t"
      "v_min_u32 v14, K9end-91b-4, v14\n\t"
      "global_load_dword v15, v14, s[90:91]\n\t"
      "s_waitcnt lgkmcnt(6)\n\t"
      "v_mfma_f32_16x16x32_bf16 a[0:3], v[16:19], v[48:51], a[0:3]\n\t"
      "ds_read_b64_tr_b16 v[32:33], v1 offset:1024\n\t"
      "v_mfma_f32_16x16x32_bf16 a[4:7], v[16:19], v[52:55], a[4:7]\n\t"
      "ds_read_b64_tr_b16 v[34:35], v1 offset:1280\n\t"
      "s_add_u32 s80, s80, 0x1000\n\t"
      "v_mfma_f32_16x16x32_bf16 a[8:11], v[16:19], v[56:59], a[8:11]\n\t"
      "buffer_load_dwordx4 v[96:99], v0, s[56:59], s80 offen sc1\n\t"
      "v_mfma_f32_16x16x32_bf16 a[12:15], v[16:19], v[60:63], a[12:15]\n\t"
      "s_waitcnt lgkmcnt(6)\n\t"
      "v_mfma_f32_16x16x32_bf16 a[16:19], v[20:23], v[48:51], a[16:19]\n\t"
      "ds_read_b64_tr_b16 v[36:37], v1 offset:1032\n\t"
      "v_mfma_f32_16x16x32_bf16 a[20:23], v[20:23], v[52:55], a[20:23]\n\t"
      "ds_read_b64_tr_b16 v[38:39], v1 offset:1288\n\t"
      "buffer_load_dwordx4 v[100:103], v0, s[56:59], s80 offen offset:1024 sc1\n\t"
      "v_mfma_f32_16x16x32_bf16 a[24:27], v[20:23], v[56:59], a[24:27]\n\t"
      "v_mfma_f32_16x16x32_bf16 a[28:31], v[20:23], v[60:63], a[28:31]\n\t"
      "s_waitcnt lgkmcnt(6)\n\t"
      "v_mfma_f32_16x16x32_bf16 a[32:35], v[24:27], v[48:51], a[32:35]\n\t"
      "ds_read_b64_tr_b16 v[40:41], v1 offset:1536\n\t"
      "buffer_load_dwordx4 v[104:107], v0, s[56:59], s80 offen offset:2048 sc1\n\t"
      "v_mfma_f32_16x16x32_bf16 a[36:39], v[24:27], v[52:55], a[36:39]\n\t"
      "ds_read_b64_tr_b16 v[42:43], v1 offset:1792\n\t"
      "v_mfma_f32_16x16x32_bf16 a[40:43], v[24:27], v[56:59], a[40:43]\n\t"
      "v_mfma_f32_16x16x32_bf16 a[44:47], v[24:27], v[60:63], a[44:47]\n\t"
      "buffer_load_dwordx4 v[108:111], v0, s[56:59], s80 offen offset:3072 sc1\n\t"
      "s_waitcnt lgkmcnt(6)\n\t"
      "v_mfma_f32_16x16x32_bf16 a[48:51], v[28:31], v[48:51], a[48:51]\n\t"
      "ds_read_b64_tr_b16 v[44:45], v1 offset:1544\n\t"
      "v_mfma_f32_16x16x32_bf16 a[52:55], v[28:31], v[52:55], a[52:55]\n\t"
      "ds_read_b64_tr_b16 v[46:47], v1 offset:1800\n\t"
      "s_mov_b32 m0, s96\n\t"
      "v_mfma_f32_16x16x32_bf16 a[56:59], v[28:31], v[56:59], a[56:59]\n\t"
      "buffer_load_dwordx4 v8, s[72:75], s95 offen lds\n\t"
      "v_mfma_f32_16x16x32_bf16 a[60:63], v[28:31], v[60:63], a[60:63]\n\t"
      "s_waitcnt lgkmcnt(6)\n\t"
      "v_mfma_f32_16x16x32_bf16 a[64:67], v[32:35], v[48:51], a[64:67]\n\t"
      "ds_read_b64_tr_b16 v[16:17], v1 offset:16384\n\t"
      "v_mfma_f32_16x16x32_bf16 a[68:71], v[32:35], v[52:55], a[68:71]\n\t"
      "ds_read_b64_tr_b16 v[18:19], v1 offset:16640\n\t"
      "buffer_load_dwordx4 v9, s[72:75], s95 offen offset:1024 lds\n\t"
      "v_mfma_f32_16x16x32_bf16 a[72:75], v[32:35], v[56:59], a[72:75]\n\t"
      "v_mfma_f32_16x16x32_bf16 a[76:79], v[32:35], v[60:63], a[76:79]\n\t"
      "s_waitcnt lgkmcnt(6)\n\t"
      "v_mfma_f32_16x16x32_bf16 a[80:83], v[36:39], v[48:51], a[80:83]\n\t"
      "ds_read_b64_tr_b16 v[20:21], v1 offset:16392\n\t"
      "buffer_load_dwordx4 v10, s[72:75], s95 offen offset:2048 lds\n\t"
      "v_mfma_f32_16x16x32_bf16 a[84:87], v[36:39], v[52:55], a[84:87]\n\t"
      "ds_read_b64_tr_b16 v[22:23], v1 offset:16648\n\t"
      "v_mfma_f32_16x16x32_bf16 a[88:91], v[36:39], v[56:59], a[88:91]\n\t"
      "v_mfma_f32_16x16x32_bf16 a[92:95], v[36:39], v[60:63], a[92:95]\n\t"
      "buffer_load_dwordx4 v11, s[72:75], s95 offen offset:3072 lds\n\t"
      "s_waitcnt lgkmcnt(6)\n\t"
      "v_mfma_f32_16x16x32_bf16 a[96:99], v[40:43], v[48:51], a[96:99]\n\t"
      "ds_read_b64_tr_b16 v[24:25], v1 offset:16896\n\t"
      "v_mfma_f32_16x16x32_bf16 a[100:103], v[40:43], v[52:55], a[100:103]\n\t"
      "ds_read_b64_tr_b16 v[26:27], v1 offset:17152\n\t"
      "s_add_u32 m0, s96, 0x4000\n\t"
      "v_mfma_f32_16x16x32_bf16 a[104:107], v[40:43], v[56:59], a[104:107]\n\t"
      "buffer_load_dwordx4 v12, s[76:79], s95 offen lds\n\t"
      "v_mfma_f32_16x16x32_bf16 a[108:111], v[40:43], v[60:63], a[108:111]\n\t"
      "s_waitcnt lgkmcnt(6)\n\t"
      "v_mfma_f32_16x16x32_bf16 a[112:115], v[44:47], v[48:51], a[112:115]\n\t"
      "ds_read_b64_tr_b16 v[28:29], v1 offset:16904\n\t"
      "v_mfma_f32_16x16x32_bf16 a[116:119], v[44:47], v[52:55], a[116:119]\n\t"
      "ds_read_b64_tr_b16 v[30:31], v1 offset:17160\n\t"
      "buffer_load_dwordx4 v12, s[76:79], s95 offen offset:2048 lds\n\t"
      "v_mfma_f32_16x16x32_bf16 a[120:123], v[44:47], v[56:59], a[120:123]\n\t"
      "v_mfma_f32_16x16x32_bf16 a[124:127], v[44:47], v[60:63], a[124:127]\n\t"
      "s_waitcnt vmcnt(19) lgkmcnt(6)\n\t"
      "v_mfma_f32_16x16x32_bf16 a[0:3], v[16:19], v[64:67], a[0:3]\n\t"
      "ds_read_b64_tr_b16 v[32:33], v1 offset:17408\n\t"
      "v_mfma_f32_16x16x32_bf16 a[4:7], v[16:19], v[68:71], a[4:7]\n\t"
      "ds_read_b64_tr_b16 v[34:35], v1 offset:17664\n\t"
      "s_add_u32 m0, s96, 0x4380\n\t"
      "v_mfma_f32_16x16x32_bf16 a[8:11], v[16:19], v[72:75], a[8:11]\n\t"
      "buffer_load_dwordx4 v12, s[76:79], s95 offen offset:128 lds\n\t"
      "v_mfma_f32_16x16x32_bf16 a[12:15], v[16:19], v[76:79], a[12:15]\n\t"
      "s_waitcnt lgkmcnt(6)\n\t"
      "v_mfma_f32_16x16x32_bf16 a[16:19], v[20:23], v[64:67], a[16:19]\n\t"
      "ds_read_b64_tr_b16 v[36:37], v1 offset:17416\n\t"
      "v_mfma_f32_16x16x32_bf16 a[20:23], v[20:23], v[68:71], a[20:23]\n\t"
      "ds_read_b64_tr_b16 v[38:39], v1 offset:17672\n\t"
      "buffer_load_dwordx4 v12, s[76:79], s95 offen offset:2176 lds\n\t"
      "v_mfma_f32_16x16x32_bf16 a[24:27], v[20:23], v[72:75], a[24:27]\n\t"
      "v_mfma_f32_16x16x32_bf16 a[28:31], v[20:23], v[76:79], a[28:31]\n\t"
      "s_add_u32 s96, s96, 0x8000\n\t"
      "s_cmp_ge_u32 s96, 0x28000\n\t"
      "s_cselect_b32 s93, 0x28000, 0\n\t"
      "s_sub_u32 s96, s96, s93\n\t"
      "s_add_u32 s95, s95, 0x4000\n\t"
      "s_mov_b32 m0, s96\n\t"
      "s_waitcnt lgkmcnt(6)\n\t"
      "v_mfma_f32_16x16x32_bf16 a[32:35], v[24:27], v[64:67], a[32:35]\n\t"
      "ds_read_b64_tr_b16 v[40:41], v1 offset:17920\n\t"
      "buffer_load_dwordx4 v8, s[72:75], s95 offen lds\n\t"
      "v_mfma_f32_16x16x32_bf16 a[36:39], v[24:27], v[68:71], a[36:39]\n\t"
      "ds_read_b64_tr_b16 v[42:43], v1 offset:18176\n\t"
      "v_mfma_f32_16x16x32_bf16 a[40:43], v[24:27], v[72:75], a[40:43]\n\t"
      "v_mfma_f32_16x16x32_bf16 a[44:47], v[24:27], v[76:79], a[44:47]\n\t"
      "buffer_load_dwordx4 v9, s[72:75], s95 offen offset:1024 lds\n\t"
      "s_waitcnt lgkmcnt(6)\n\t"
      "v_mfma_f32_16x16x32_bf16 a[48:51], v[28:31], v[64:67], a[48:51]\n\t"
      "ds_read_b64_tr_b16 v[44:45], v1 offset:17928\n\t"
      "v_mfma_f32_16x16x32_bf16 a[52:55], v[28:31], v[68:71], a[52:55]\n\t"
      "ds_read_b64_tr_b16 v[46:47], v1 offset:18184\n\t"
      "v_mfma_f32_16x16x32_bf16 a[56:59], v[28:31], v[72:75], a[56:59]\n\t"
      "buffer_load_dwordx4 v10, s[72:75], s95 offen offset:2048 lds\n\t"
      "v_mfma_f32_16x16x32_bf16 a[60:63], v[28:31], v[76:79], a[60:63]\n\t"
      "s_waitcnt lgkmcnt(6)\n\t"
      "v_mfma_f32_16x16x32_bf16 a[64:67], v[32:35], v[64:67], a[64:67]\n\t"
      "v_mfma_f32_16x16x32_bf16 a[68:71], v[32:35], v[68:71], a[68:71]\n\t"
      "buffer_load_dwordx4 v11, s[72:75], s95 offen offset:3072 lds\n\t"
      "v_mfma_f32_16x16x32_bf16 a[72:75], v[32:35], v[72:75], a[72:75]\n\t"
      "v_mfma_f32_16x16x32_bf16 a[76:79], v[32:35], v[76:79], a[76:79]\n\t"
      "s_waitcnt vmcnt(17) lgkmcnt(0)\n\t"
      "s_barrier\n\t"
      "s_add_u32 s98, s98, 0x8000\n\t"
      "s_cmp_ge_u32 s98, 0x18000\n\t"
      "s_cselect_b32 s97, 0xffff0000, s44\n\t"
      "s_cselect_b32 s98, 0, s98\n\t"
      "v_add_u32 v1, s97, v1\n\t"
      "v_mfma_f32_16x16x32_bf16 a[80:83], v[36:39], v[64:67], a[80:83]\n\t"
      "ds_read_b64_tr_b16 v[16:17], v1\n\t"
      "v_mfma_f32_16x16x32_bf16 a[84:87], v[36:39], v[68:71], a[84:87]\n\t"
      "ds_read_b64_tr_b16 v[18:19], v1 offset:256\n\t"
      "v_mfma_f32_16x16x32_bf16 a[88:91], v[36:39], v[72:75], a[88:91]\n\t"
      "v_mfma_f32_16x16x32_bf16 a[92:95], v[36:39], v[76:79], a[92:95]\n\t"
      "v_mfma_f32_16x16x32_bf16 a[96:99], v[40:43], v[64:67], a[96:99]\n\t"
      "ds_read_b64_tr_b16 v[20:21], v1 offset:8\n\t"
      "v_mfma_f32_16x16x32_bf16 a[100:103], v[40:43], v[68:71], a[100:103]\n\t"
      "ds_read_b64_tr_b16 v[22:23], v1 offset:264\n\t"
      "v_mfma_f32_16x16x32_bf16 a[104:107], v[40:43], v[72:75], a[104:107]\n\t"
      "ds_read_b64_tr_b16 v[24:25], v1 offset:512\n\t"
      "v_mfma_f32_16x16x32_bf16 a[108:111], v[40:43], v[76:79], a[108:111]\n\t"
      "ds_read_b64_tr_b16 v[26:27], v1 offset:768\n\t"
      "v_mfma_f32_16x16x32_bf16 a[112:115], v[44:47], v[64:67], a[112:115]\n\t"
      "ds_read_b64_tr_b16 v[28:29], v1 offset:520\n\t"
      "v_mfma_f32_16x16x32_bf16 a[116:119], v[44:47], v[68:71], a[116:119]\n\t"
      "ds_read_b64_tr_b16 v[30:31], v1 offset:776\n\t"
      "v_mfma_f32_16x16x32_bf16 a[120:123], v[44:47], v[72:75], a[120:123]\n\t"
      "v_mfma_f32_16x16x32_bf16 a[124:127], v[44:47], v[76:79], a[124:127]\n\t"
      "s_waitcnt lgkmcnt(6)\n\t"
      "v_mfma_f32_16x16x32_bf16 a[0:3], v[16:19], v[80:83], a[0:3]\n\t"
      "ds_read_b64_tr_b16 v[32:33], v1 offset:1024\n\t"
      "v_mfma_f32_16x16x32_bf16 a[4:7], v[16:19], v[84:87], a[4:7]\n\t"
      "ds_read_b64_tr_b16 v[34:35], v1 offset:1280\n\t"
      "s_add_u32 m0, s96, 0x4000\n\t"
      "v_mfma_f32_16x16x32_bf16 a[8:11], v[16:19], v[88:91], a[8:11]\n\t"
      "buffer_load_dwordx4 v12, s[76:79], s95 offen lds\n\t"
      "v_mfma_f32_16x16x32_bf16 a[12:15], v[16:19], v[92:95], a[12:15]\n\t"
      "s_waitcnt lgkmcnt(6)\n\t"
      "v_mfma_f32_16x16x32_bf16 a[16:19], v[20:23], v[80:83], a[16:19]\n\t"
      "ds_read_b64_tr_b16 v[36:37], v1 offset:1032\n\t"
      "v_mfma_f32_16x16x32_bf16 a[20:23], v[20:23], v[84:87], a[20:23]\n\t"
      "ds_read_b64_tr_b16 v[38:39], v1 offset:1288\n\t"
      "buffer_load_dwordx4 v12, s[76:79], s95 offen offset:2048 lds\n\t"
      "v_mfma_f32_16x16x32_bf16 a[24:27], v[20:23], v[88:91], a[24:27]\n\t"
      "v_mfma_f32_16x16x32_bf16 a[28:31], v[20:23], v[92:95], a[28:31]\n\t"
      "s_add_u32 m0, s96, 0x4380\n\t"
      "s_waitcnt lgkmcnt(6)\n\t"
      "v_mfma_f32_16x16x32_bf16 a[32:35], v[24:27], v[80:83], a[32:35]\n\t"
      "ds_read_b64_tr_b16 v[40:41], v1 offset:1536\n\t"
      "buffer_load_dwordx4 v12, s[76:79], s95 offen offset:128 lds\n\t"
      "v_mfma_f32_16x16x32_bf16 a[36:39], v[24:27], v[84:87], a[36:39]\n\t"
      "ds_read_b64_tr_b16 v[42:43], v1 offset:1792\n\t"
      "v_mfma_f32_16x16x32_bf16 a[40:43], v[24:27], v[88:91], a[40:43]\n\t"
      "v_mfma_f32_16x16x32_bf16 a[44:47], v[24:27], v[92:95], a[44:47]\n\t"
      "buffer_load_dwordx4 v12, s[76:79], s95 offen offset:2176 lds\n\t"
      "s_waitcnt lgkmcnt(6)\n\t"
      "v_mfma_f32_16x16x32_bf16 a[48:51], v[28:31], v[80:83], a[48:51]\n\t"
      "ds_read_b64_tr_b16 v[44:45], v1 offset:1544\n\t"
      "v_mfma_f32_16x16x32_bf16 a[52:55], v[28:31], v[84:87], a[52:55]\n\t"
      "ds_read_b64_tr_b16 v[46:47], v1 offset:1800\n\t"
      "s_add_u32 s96, s96, 0x8000\n\t"
      "s_cmp_ge_u32 s96, 0x28000\n\t"
      "s_cselect_b32 s93, 0x28000, 0\n\t"
      "s_sub_u32 s96, s96, s93\n\t"
      "s_add_u32 s95, s95, 0x4000\n\t"
      "s_mov_b32 m0, s96\n\t"
      "v_mfma_f32_16x16x32_bf16 a[56:59], v[28:31], v[88:91], a[56:59]\n\t"
      "buffer_load_dwordx4 v8, s[72:75], s95 offen lds\n\t"
      "v_mfma_f32_16x16x32_bf16 a[60:63], v[28:31], v[92:95], a[60:63]\n\t"
      "s_waitcnt lgkmcnt(6)\n\t"
      "v_mfma_f32_16x16x32_bf16 a[64:67], v[32:35], v[80:83], a[64:67]\n\t"
      "ds_read_b64_tr_b16 v[16:17], v1 offset:16384\n\t"
      "v_mfma_f32_16x16x32_bf16 a[68:71], v[32:35], v[84:87], a[68:71]\n\t"
      "ds_read_b64_tr_b16 v[18:19], v1 offset:16640\n\t"
      "buffer_load_dwordx4 v9, s[72:75], s95 offen offset:1024 lds\n\t"
      "v_mfma_f32_16x16x32_bf16 a[72:75], v[32:35], v[88:91], a[72:75]\n\t"
      "v_mfma_f32_16x16x32_bf16 a[76:79], v[32:35], v[92:95], a[76:79]\n\t"
      "s_waitcnt lgkmcnt(6)\n\t"
      "v_mfma_f32_16x16x32_bf16 a[80:83], v[36:39], v[80:83], a[80:83]\n\t"
      "ds_read_b64_tr_b16 v[20:21], v1 offset:16392\n\t"
      "v_mfma_f32_16x16x32_bf16 a[84:87], v[36:39], v[84:87], a[84:87]\n\t"
      "ds_read_b64_tr_b16 v[22:23], v1 offset:16648\n\t"
      "v_mfma_f32_16x16x32_bf16 a[88:91], v[36:39], v[88:91], a[88:91]\n\t"
      "v_mfma_f32_16x16x32_bf16 a[92:95], v[36:39], v[92:95], a[92:95]\n\t"
      "s_waitcnt lgkmcnt(6)\n\t"
      "v_mfma_f32_16x16x32_bf16 a[96:99], v[40:43], v[80:83], a[96:99]\n\t"
      "ds_read_b64_tr_b16 v[24:25], v1 offset:16896\n\t"
      "v_mfma_f32_16x16x32_bf16 a[100:103], v[40:43], v[84:87], a[100:103]\n\t"
      "ds_read_b64_tr_b16 v[26:27], v1 offset:17152\n\t"
      "v_mfma_f32_16x16x32_bf16 a[104:107], v[40:43], v[88:91], a[104:107]\n\t"
      "v_mfma_f32_16x16x32_bf16 a[108:111], v[40:43], v[92:95], a[108:111]\n\t"
      "s_waitcnt lgkmcnt(6)\n\t"
      "v_mfma_f32_16x16x32_bf16 a[112:115], v[44:47], v[80:83], a[112:115]\n\t"
      "ds_read_b64_tr_b16 v[28:29], v1 offset:16904\n\t"
      "v_mfma_f32_16x16x32_bf16 a[116:119], v[44:47], v[84:87], a[116:119]\n\t"
      "ds_read_b64_tr_b16 v[30:31], v1 offset:17160\n\t"
      "v_mfma_f32_16x16x32_bf16 a[120:123], v[44:47], v[88:91], a[120:123]\n\t"
      "v_mfma_f32_16x16x32_bf16 a[124:127], v[44:47], v[92:95], a[124:127]\n\t"
      "s_waitcnt vmcnt(18) lgkmcnt(6)\n\t"
      "v_mfma_f32_16x16x32_bf16 a[0:3], v[16:19], v[96:99], a[0:3]\n\t"
      "ds_read_b64_tr_b16 v[32:33], v1 offset:17408\n\t"
      "v_mfma_f32_16x16x32_bf16 a[4:7], v[16:19], v[100:103], a[4:7]\n\t"
      "ds_read_b64_tr_b16 v[34:35], v1 offset:17664\n\t"
      "v_mfma_f32_16x16x32_bf16 a[8:11], v[16:19], v[104:107], a[8:11]\n\t"
      "buffer_load_dwordx4 v10, s[72:75], s95 offen offset:2048 lds\n\t"
      "v_mfma_f32_16x16x32_bf16 a[12:15], v[16:19], v[108:111], a[12:15]\n\t"
      "s_waitcnt lgkmcnt(6)\n\t"
      "v_mfma_f32_16x16x32_bf16 a[16:19], v[20:23], v[96:99], a[16:19]\n\t"
      "ds_read_b64_tr_b16 v[36:37], v1 offset:17416\n\t"
      "v_mfma_f32_16x16x32_bf16 a[20:23], v[20:23], v[100:103], a[20:23]\n\t"
      "ds_read_b64_tr_b16 v[38:39], v1 offset:17672\n\t"
      "buffer_load_dwordx4 v11, s[72:75], s95 offen offset:3072 lds\n\t"
      "v_mfma_f32_16x16x32_bf16 a[24:27], v[20:23], v[104:107], a[24:27]\n\t"
      "v_mfma_f32_16x16x32_bf16 a[28:31], v[20:23], v[108:111], a[28:31]\n\t"
      "s_add_u32 m0, s96, 0x4000\n\t"
      "s_waitcnt lgkmcnt(6)\n\t"
      "v_mfma_f32_16x16x32_bf16 a[32:35], v[24:27], v[96:99], a[32:35]\n\t"
      "ds_read_b64_tr_b16 v[40:41], v1 offset:17920\n\t"
      "buffer_load_dwordx4 v12, s[76:79], s95 offen lds\n\t"
      "v_mfma_f32_16x16x32_bf16 a[36:39], v[24:27], v[100:103], a[36:39]\n\t"
      "ds_read_b64_tr_b16 v[42:43], v1 offset:18176\n\t"
      "v_mfma_f32_16x16x32_bf16 a[40:43], v[24:27], v[104:107], a[40:43]\n\t"
      "v_mfma_f32_16x16x32_bf16 a[44:47], v[24:27], v[108:111], a[44:47]\n\t"
      "buffer_load_dwordx4 v12, s[76:79], s95 offen offset:2048 lds\n\t"
      "s_waitcnt lgkmcnt(6)\n\t"
      "v_mfma_f32_16x16x32_bf16 a[48:51], v[28:31], v[96:99], a[48:51]\n\t"
      "ds_read_b64_tr_b16 v[44:45], v1 offset:17928\n\t"
      "v_mfma_f32_16x16x32_bf16 a[52:55], v[28:31], v[100:103], a[52:55]\n\t"
      "ds_read_b64_tr_b16 v[46:47], v1 offset:18184\n\t"
      "s_add_u32 m0, s96, 0x4380\n\t"
      "v_mfma_f32_16x16x32_bf16 a[56:59], v[28:31], v[104:107], a[56:59]\n\t"
      "buffer_load_dwordx4 v12, s[76:79], s95 offen offset:128 lds\n\t"
      "v_mfma_f32_16x16x32_bf16 a[60:63], v[28:31], v[108:111], a[60:63]\n\t"
      "s_waitcnt lgkmcnt(6)\n\t"
      "v_mfma_f32_16x16x32_bf16 a[64:67], v[32:35], v[96:99], a[64:67]\n\t"
      "v_mfma_f32_16x16x32_bf16 a[68:71], v[32:35], v[100:103], a[68:71]\n\t"
      "buffer_load_dwordx4 v12, s[76:79], s95 offen offset:2176 lds\n\t"
      "v_mfma_f32_16x16x32_bf16 a[72:75], v[32:35], v[104:107], a[72:75]\n\t"
      "v_mfma_f32_16x16x32_bf16 a[76:79], v[32:35], v[108:111], a[76:79]\n\t"
      "s_waitcnt lgkmcnt(4)\n\t"
      "v_mfma_f32_16x16x32_bf16 a[80:83], v[36:39], v[96:99], a[80:83]\n\t"
      "v_mfma_f32_16x16x32_bf16 a[84:87], v[36:39], v[100:103], a[84:87]\n\t"
      "v_mfma_f32_16x16x32_bf16 a[88:91], v[36:39], v[104:107], a[88:91]\n\t"
      "v_mfma_f32_16x16x32_bf16 a[92:95], v[36:39], v[108:111], a[92:95]\n\t"
      "s_waitcnt lgkmcnt(2)\n\t"
      "v_mfma_f32_16x16x32_bf16 a[96:99], v[40:43], v[96:99], a[96:99]\n\t"
      "v_mfma_f32_16x16x32_bf16 a[100:103], v[40:43], v[100:103], a[100:103]\n\t"
      "v_mfma_f32_16x16x32_bf16 a[104:107], v[40:43], v[104:107], a[104:107]\n\t"
      "v_mfma_f32_16x16x32_bf16 a[108:111], v[40:43], v[108:111], a[108:111]\n\t"
      "s_waitcnt lgkmcnt(0)\n\t"
      "v_mfma_f32_16x16x32_bf16 a[112:115], v[44:47], v[96:99], a[112:115]\n\t"
      "v_mfma_f32_16x16x32_bf16 a[116:119], v[44:47], v[100:103], a[116:119]\n\t"
      "v_mfma_f32_16x16x32_bf16 a[120:123], v[44:47], v[104:107], a[120:123]\n\t"
      "v_mfma_f32_16x16x32_bf16 a[124:127], v[44:47], v[108:111], a[124:127]\n\t"
      "s_nop 7\n\t"
      "s_nop 7\n\t"
      "K1common:\n\t"
      "s_waitcnt vmcnt(8)\n\t"
      "s_barrier\n\t"
      "ds_read_b128 a[144:147], v112\n\t"
      "ds_read_b128 a[148:151], v113\n\t"
      "ds_read_b128 a[152:155], v114\n\t"
      "ds_read_b128 a[156:159], v115\n\t"
      "ds_read_b128 a[160:163], v112 offset:4096\n\t"
      "ds_read_b128 a[164:167], v113 offset:4096\n\t"
      "ds_read_b128 a[168:171], v114 offset:4096\n\t"
      "ds_read_b128 a[172:175], v115 offset:4096\n\t"
      "v_accvgpr_read_b32 v80, a0\n\t"
      "v_accvgpr_read_b32 v81, a1\n\t"
      "v_accvgpr_read_b32 v82, a2\n\t"
      "v_accvgpr_read_b32 v83, a3\n\t"
      "v_accvgpr_read_b32 v84, a16\n\t"
      "v_accvgpr_read_b32 v85, a17\n\t"
      "v_accvgpr_read_b32 v86, a18\n\t"
      "v_accvgpr_read_b32 v87, a19\n\t"
      "v_cvt_pk_bf16_f32 v16, v80, v81\n\t"
      "v_cvt_pk_bf16_f32 v17, v82, v83\n\t"
      "v_cvt_pk_bf16_f32 v18, v84, v85\n\t"
      "v_cvt_pk_bf16_f32 v19, v86, v87\n\t"
      "v_accvgpr_read_b32 v88, a4\n\t"
      "v_accvgpr_read_b32 v89, a5\n\t"
      "v_accvgpr_read_b32 v90, a6\n\t"
      "v_accvgpr_read_b32 v91, a7\n\t"
      "v_accvgpr_read_b32 v92, a20\n\t"
      "v_accvgpr_read_b32 v93, a21\n\t"
      "v_accvgpr_read_b32 v94, a22\n\t"
      "v_accvgpr_read_b32 v95, a23\n\t"
      "v_cvt_pk_bf16_f32 v32, v88, v89\n\t"
      "v_cvt_pk_bf16_f32 v33, v90, v91\n\t"
      "v_cvt_pk_bf16_f32 v34, v92, v93\n\t"
      "v_cvt_pk_bf16_f32 v35, v94, v95\n\t"
      "v_accvgpr_read_b32 v80, a8\n\t"
      "v_accvgpr_read_b32 v81, a9\n\t"
      "v_accvgpr_read_b32 v82, a10\n\t"
      "v_accvgpr_read_b32 v83, a11\n\t"
      "v_accvgpr_read_b32 v84, a24\n\t"
      "v_accvgpr_read_b32 v85, a25\n\t"
      "v_accvgpr_read_b32 v86, a26\n\t"
      "v_accvgpr_read_b32 v87, a27\n\t"
      "v_cvt_pk_bf16_f32 v48, v80, v81\n\t"
      "v_cvt_pk_bf16_f32 v49, v82, v83\n\t"
      "v_cvt_pk_bf16_f32 v50, v84, v85\n\t"
      "v_cvt_pk_bf16_f32 v51, v86, v87\n\t"
      "v_accvgpr_read_b32 v88, a12\n\t"
      "v_accvgpr_read_b32 v89, a13\n\t"
      "v_accvgpr_read_b32 v90, a14\n\t"
      "v_accvgpr_read_b32 v91, a15\n\t"
      "v_accvgpr_read_b32 v92, a28\n\t"
      "v_accvgpr_read_b32 v93, a29\n\t"
      "v_accvgpr_read_b32 v94, a30\n\t"
      "v_accvgpr_read_b32 v95, a31\n\t"
      "v_cvt_pk_bf16_f32 v64, v88, v89\n\t"
      "v_cvt_pk_bf16_f32 v65, v90, v91\n\t"
      "v_cvt_pk_bf16_f32 v66, v92, v93\n\t"
      "v_cvt_pk_bf16_f32 v67, v94, v95\n\t"
      "v_accvgpr_read_b32 v80, a32\n\t"
      "v_accvgpr_read_b32 v81, a33\n\t"
      "v_accvgpr_read_b32 v82, a34\n\t"
      "v_accvgpr_read_b32 v83, a35\n\t"
      "v_accvgpr_read_b32 v84, a48\n\t"
      "v_accvgpr_read_b32 v85, a49\n\t"
      "v_accvgpr_read_b32 v86, a50\n\t"
      "v_accvgpr_read_b32 v87, a51\n\t"
      "v_cvt_pk_bf16_f32 v20, v80, v81\n\t"
      "v_cvt_pk_bf16_f32 v21, v82, v83\n\t"
      "v_cvt_pk_bf16_f32 v22, v84, v85\n\t"
      "v_cvt_pk_bf16_f32 v23, v86, v87\n\t"
      "v_accvgpr_read_b32 v88, a36\n\t"
      "v_accvgpr_read_b32 v89, a37\n\t"
      "v_accvgpr_read_b32 v90, a38\n\t"
      "v_accvgpr_read_b32 v91, a39\n\t"
      "v_accvgpr_read_b32 v92, a52\n\t"
      "v_accvgpr_read_b32 v93, a53\n\t"
      "v_accvgpr_read_b32 v94, a54\n\t"
      "v_accvgpr_read_b32 v95, a55\n\t"
      "v_cvt_pk_bf16_f32 v36, v88, v89\n\t"
      "v_cvt_pk_bf16_f32 v37, v90, v91\n\t"
      "v_cvt_pk_bf16_f32 v38, v92, v93\n\t"
      "v_cvt_pk_bf16_f32 v39, v94, v95\n\t"
      "v_accvgpr_read_b32 v80, a40\n\t"
      "v_accvgpr_read_b32 v81, a41\n\t"
      "v_accvgpr_read_b32 v82, a42\n\t"
      "v_accvgpr_read_b32 v83, a43\n\t"
      "v_accvgpr_read_b32 v84, a56\n\t"
      "v_accvgpr_read_b32 v85, a57\n\t"
      "v_accvgpr_read_b32 v86, a58\n\t"
      "v_accvgpr_read_b32 v87, a59\n\t"
      "v_cvt_pk_bf16_f32 v52, v80, v81\n\t"
      "v_cvt_pk_bf16_f32 v53, v82, v83\n\t"
      "v_cvt_pk_bf16_f32 v54, v84, v85\n\t"
      "v_cvt_pk_bf16_f32 v55, v86, v87\n\t"
      "v_accvgpr_read_b32 v88, a44\n\t"
      "v_accvgpr_read_b32 v89, a45\n\t"
      "v_accvgpr_read_b32 v90, a46\n\t"
      "v_accvgpr_read_b32 v91, a47\n\t"
      "v_accvgpr_read_b32 v92, a60\n\t"
      "v_accvgpr_read_b32 v93, a61\n\t"
      "v_accvgpr_read_b32 v94, a62\n\t"
      "v_accvgpr_read_b32 v95, a63\n\t"
      "v_cvt_pk_bf16_f32 v68, v88, v89\n\t"
      "v_cvt_pk_bf16_f32 v69, v90, v91\n\t"
      "v_cvt_pk_bf16_f32 v70, v92, v93\n\t"
      "v_cvt_pk_bf16_f32 v71, v94, v95\n\t"
      "v_accvgpr_read_b32 v80, a64\n\t"
      "v_accvgpr_read_b32 v81, a65\n\t"
      "v_accvgpr_read_b32 v82, a66\n\t"
      "v_accvgpr_read_b32 v83, a67\n\t"
      "v_accvgpr_read_b32 v84, a80\n\t"
      "v_accvgpr_read_b32 v85, a81\n\t"
      "v_accvgpr_read_b32 v86, a82\n\t"
      "v_accvgpr_read_b32 v87, a83\n\t"
      "v_cvt_pk_bf16_f32 v24, v80, v81\n\t"
      "v_cvt_pk_bf16_f32 v25, v82, v83\n\t"
      "v_cvt_pk_bf16_f32 v26, v84, v85\n\t"
      "v_cvt_pk_bf16_f32 v27, v86, v87\n\t"
      "v_accvgpr_read_b32 v88, a68\n\t"
      "v_accvgpr_read_b32 v89, a69\n\t"
      "v_accvgpr_read_b32 v90, a70\n\t"
      "v_accvgpr_read_b32 v91, a71\n\t"
      "v_accvgpr_read_b32 v92, a84\n\t"
      "v_accvgpr_read_b32 v93, a85\n\t"
      "v_accvgpr_read_b32 v94, a86\n\t"
      "v_accvgpr_read_b32 v95, a87\n\t"
      "v_cvt_pk_bf16_f32 v40, v88, v89\n\t"
      "v_cvt_pk_bf16_f32 v41, v90, v91\n\t"
      "v_cvt_pk_bf16_f32 v42, v92, v93\n\t"
      "v_cvt_pk_bf16_f32 v43, v94, v95\n\t"
      "v_accvgpr_read_b32 v80, a72\n\t"
      "v_accvgpr_read_b32 v81, a73\n\t"
      "v_accvgpr_read_b32 v82, a74\n\t"
      "v_accvgpr_read_b32 v83, a75\n\t"
      "v_accvgpr_read_b32 v84, a88\n\t"
      "v_accvgpr_read_b32 v85, a89\n\t"
      "v_accvgpr_read_b32 v86, a90\n\t"
      "v_accvgpr_read_b32 v87, a91\n\t"
      "v_cvt_pk_bf16_f32 v56, v80, v81\n\t"
      "v_cvt_pk_bf16_f32 v57, v82, v83\n\t"
      "v_cvt_pk_bf16_f32 v58, v84, v85\n\t"
      "v_cvt_pk_bf16_f32 v59, v86, v87\n\t"
      "v_accvgpr_read_b32 v88, a76\n\t"
      "v_accvgpr_read_b32 v89, a77\n\t"
      "v_accvgpr_read_b32 v90, a78\n\t"
      "v_accvgpr_read_b32 v91, a79\n\t"
      "v_accvgpr_read_b32 v92, a92\n\t"
      "v_accvgpr_read_b32 v93, a93\n\t"
      "v_accvgpr_read_b32 v94, a94\n\t"
      "v_accvgpr_read_b32 v95, a95\n\t"
      "v_cvt_pk_bf16_f32 v72, v88, v89\n\t"
      "v_cvt_pk_bf16_f32 v73, v90, v91\n\t"
      "v_cvt_pk_bf16_f32 v74, v92, v93\n\t"
      "v_cvt_pk_bf16_f32 v75, v94, v95\n\t"
      "v_accvgpr_read_b32 v80, a96\n\t"
      "v_accvgpr_read_b32 v81, a97\n\t"
      "v_accvgpr_read_b32 v82, a98\n\t"
      "v_accvgpr_read_b32 v83, a99\n\t"
      "v_accvgpr_read_b32 v84, a112\n\t"
      "v_accvgpr_read_b32 v85, a113\n\t"
      "v_accvgpr_read_b32 v86, a114\n\t"
      "v_accvgpr_read_b32 v87, a115\n\t"
      "v_cvt_pk_bf16_f32 v28, v80, v81\n\t"
      "v_cvt_pk_bf16_f32 v29, v82, v83\n\t"
      "v_cvt_pk_bf16_f32 v30, v84, v85\n\t"
      "v_cvt_pk_bf16_f32 v31, v86, v87\n\t"
      "v_accvgpr_read_b32 v88, a100\n\t"
      "v_accvgpr_read_b32 v89, a101\n\t"
      "v_accvgpr_read_b32 v90, a102\n\t"
      "v_accvgpr_read_b32 v91, a103\n\t"
      "v_accvgpr_read_b32 v92, a116\n\t"
      "v_accvgpr_read_b32 v93, a117\n\t"
      "v_accvgpr_read_b32 v94, a118\n\t"
      "v_accvgpr_read_b32 v95, a119\n\t"
      "v_cvt_pk_bf16_f32 v44, v88, v89\n\t"
      "v_cvt_pk_bf16_f32 v45, v90, v91\n\t"
      "v_cvt_pk_bf16_f32 v46, v92, v93\n\t"
      "v_cvt_pk_bf16_f32 v47, v94, v95\n\t"
      "v_accvgpr_read_b32 v80, a104\n\t"
      "v_accvgpr_read_b32 v81, a105\n\t"
      "v_accvgpr_read_b32 v82, a106\n\t"
      "v_accvgpr_read_b32 v83, a107\n\t"
      "v_accvgpr_read_b32 v84, a120\n\t"
      "v_accvgpr_read_b32 v85, a121\n\t"
      "v_accvgpr_read_b32 v86, a122\n\t"
      "v_accvgpr_read_b32 v87, a123\n\t"
      "v_cvt_pk_bf16_f32 v60, v80, v81\n\t"
      "v_cvt_pk_bf16_f32 v61, v82, v83\n\t"
      "v_cvt_pk_bf16_f32 v62, v84, v85\n\t"
      "v_cvt_pk_bf16_f32 v63, v86, v87\n\t"
      "v_accvgpr_read_b32 v88, a108\n\t"
      "v_accvgpr_read_b32 v89, a109\n\t"
      "v_accvgpr_read_b32 v90, a110\n\t"
      "v_accvgpr_read_b32 v91, a111\n\t"
      "v_accvgpr_read_b32 v92, a124\n\t"
      "v_accvgpr_read_b32 v93, a125\n\t"
      "v_accvgpr_read_b32 v94, a126\n\t"
      "v_accvgpr_read_b32 v95, a127\n\t"
      "v_cvt_pk_bf16_f32 v76, v88, v89\n\t"
      "v_cvt_pk_bf16_f32 v77, v90, v91\n\t"
      "v_cvt_pk_bf16_f32 v78, v92, v93\n\t"
      "v_cvt_pk_bf16_f32 v79, v94, v95\n\t"
      "v_mov_b32 v120, 0x3f803f80\n\t"
      "v_mov_b32 v121, 0x3f803f80\n\t"
      "v_mov_b32 v122, 0x3f803f80\n\t"
      "v_mov_b32 v123, 0x3f803f80\n\t"
      "s_mov_b32 s92, 0x18000\n\t"
      "s_mov_b32 s49, 0\n\t"
      "s_waitcnt lgkmcnt(7)\n\t"
      "v_mfma_f32_16x16x32_bf16 v[128:131], a[144:147], v[16:19], 0\n\t"
      "v_mfma_f32_16x16x32_bf16 v[132:135], a[144:147], v[32:35], 0\n\t"
      "v_mfma_f32_16x16x32_bf16 v[136:139], a[144:147], v[48:51], 0\n\t"
      "v_mfma_f32_16x16x32_bf16 v[140:143], a[144:147], v[64:67], 0\n\t"
      "s_waitcnt lgkmcnt(6)\n\t"
      "v_mfma_f32_16x16x32_bf16 v[128:131], a[148:151], v[20:23], v[128:131]\n\t"
      "v_mfma_f32_16x16x32_bf16 v[132:135], a[148:151], v[36:39], v[132:135]\n\t"
      "v_mfma_f32_16x16x32_bf16 v[136:139], a[148:151], v[52:55], v[136:139]\n\t"
      "v_mfma_f32_16x16x32_bf16 v[140:143], a[148:151], v[68:71], v[140:143]\n\t"
      "s_waitcnt lgkmcnt(5)\n\t"
      "v_mfma_f32_16x16x32_bf16 v[128:131], a[152:155], v[24:27], v[128:131]\n\t"
      "v_mfma_f32_16x16x32_bf16 v[132:135], a[152:155], v[40:43], v[132:135]\n\t"
      "v_mfma_f32_16x16x32_bf16 v[136:139], a[152:155], v[56:59], v[136:139]\n\t"
      "v_mfma_f32_16x16x32_bf16 v[140:143], a[152:155], v[72:75], v[140:143]\n\t"
      "s_waitcnt lgkmcnt(4)\n\t"
      "v_mfma_f32_16x16x32_bf16 v[128:131], a[156:159], v[28:31], v[128:131]\n\t"
      "v_mfma_f32_16x16x32_bf16 v[132:135], a[156:159], v[44:47], v[132:135]\n\t"
      "v_mfma_f32_16x16x32_bf16 v[136:139], a[156:159], v[60:63], v[136:139]\n\t"
      "v_mfma_f32_16x16x32_bf16 v[140:143], a[156:159], v[76:79], v[140:143]\n\t"
      "s_waitcnt lgkmcnt(3)\n\t"
      "v_mfma_f32_16x16x32_bf16 v[144:147], a[160:163], v[16:19], 0\n\t"
      "v_mfma_f32_16x16x32_bf16 v[148:151], a[160:163], v[32:35], 0\n\t"
      "v_mfma_f32_16x16x32_bf16 v[152:155], a[160:163], v[48:51], 0\n\t"
      "v_mfma_f32_16x16x32_bf16 v[156:159], a[160:163], v[64:67], 0\n\t"
      "s_waitcnt lgkmcnt(2)\n\t"
      "v_mfma_f32_16x16x32_bf16 v[144:147], a[164:167], v[20:23], v[144:147]\n\t"
      "v_mfma_f32_16x16x32_bf16 v[148:151], a[164:167], v[36:39], v[148:151]\n\t"
      "v_mfma_f32_16x16x32_bf16 v[152:155], a[164:167], v[52:55], v[152:155]\n\t"
      "v_mfma_f32_16x16x32_bf16 v[156:159], a[164:167], v[68:71], v[156:159]\n\t"
      "s_waitcnt lgkmcnt(1)\n\t"
      "v_mfma_f32_16x16x32_bf16 v[144:147], a[168:171], v[24:27], v[144:147]\n\t"
      "v_mfma_f32_16x16x32_bf16 v[148:151], a[168:171], v[40:43], v[148:151]\n\t"
      "v_mfma_f32_16x16x32_bf16 v[152:155], a[168:171], v[56:59], v[152:155]\n\t"
      "v_mfma_f32_16x16x32_bf16 v[156:159], a[168:171], v[72:75], v[156:159]\n\t"
      "s_waitcnt lgkmcnt(0)\n\t"
      "v_mfma_f32_16x16x32_bf16 v[144:147], a[172:175], v[28:31], v[144:147]\n\t"
      "v_mfma_f32_16x16x32_bf16 v[148:151], a[172:175], v[44:47], v[148:151]\n\t"
      "v_mfma_f32_16x16x32_bf16 v[152:155], a[172:175], v[60:63], v[152:155]\n\t"
      "v_mfma_f32_16x16x32_bf16 v[156:159], a[172:175], v[76:79], v[156:159]\n\t"
      "ds_read_b128 a[144:147], v112 offset:8192\n\t"
      "ds_read_b128 a[148:151], v113 offset:8192\n\t"
      "ds_read_b128 a[152:155], v114 offset:8192\n\t"
      "ds_read_b128 a[156:159], v115 offset:8192\n\t"
      "ds_read_b128 a[160:163], v112 offset:12288\n\t"
      "ds_read_b128 a[164:167], v113 offset:12288\n\t"
      "ds_read_b128 a[168:171], v114 offset:12288\n\t"
      "ds_read_b128 a[172:175], v115 offset:12288\n\t"
      "s_nop 7\n\t"
      "v_max3_f32 v240, v128, v129, v130\n\t"
      "v_max3_f32 v240, v240, v131, v144\n\t"
      "v_max3_f32 v240, v240, v145, v146\n\t"
      "v_max_f32 v240, v240, v147\n\t"
      "v_max3_f32 v241, v132, v133, v134\n\t"
      "v_max3_f32 v241, v241, v135, v148\n\t"
      "v_max3_f32 v241, v241, v149, v150\n\t"
      "v_max_f32 v241, v241, v151\n\t"
      "v_max3_f32 v242, v136, v137, v138\n\t"
      "v_max3_f32 v242, v242, v139, v152\n\t"
      "v_max3_f32 v242, v242, v153, v154\n\t"
      "v_max_f32 v242, v242, v155\n\t"
      "v_max3_f32 v243, v140, v141, v142\n\t"
      "v_max3_f32 v243, v243, v143, v156\n\t"
      "v_max3_f32 v243, v243, v157, v158\n\t"
      "v_max_f32 v243, v243, v159\n\t"
      "v_mov_b32 v244, v240\n\t"
      "v_mov_b32 v245, v241\n\t"
      "v_mov_b32 v246, v242\n\t"
      "v_mov_b32 v247, v243\n\t"
      "s_nop 1\n\t"
      "v_permlane32_swap_b32 v240, v244\n\t"
      "v_permlane32_swap_b32 v241, v245\n\t"
      "v_permlane32_swap_b32 v242, v246\n\t"
      "v_permlane32_swap_b32 v243, v247\n\t"
      "s_nop 1\n\t"
      "v_max_f32 v240, v240, v244\n\t"
      "v_max_f32 v241, v241, v245\n\t"
      "v_max_f32 v242, v242, v246\n\t"
      "v_max_f32 v243, v243, v247\n\t"
      "v_mov_b32 v244, v240\n\t"
      "v_mov_b32 v245, v241\n\t"
      "v_mov_b32 v246, v242\n\t"
      "v_mov_b32 v247, v243\n\t"
      "s_nop 1\n\t"
      "v_permlane16_swap_b32 v240, v244\n\t"
      "v_permlane16_swap_b32 v241, v245\n\t"
      "v_permlane16_swap_b32 v242, v246\n\t"
      "v_permlane16_swap_b32 v243, v247\n\t"
      "s_nop 1\n\t"
      "v_max_f32 v240, v240, v244\n\t"
      "v_max_f32 v241, v241, v245\n\t"
      "v_max_f32 v242, v242, v246\n\t"
      "v_max_f32 v243, v243, v247\n\t"
      "v_sub_f32 v224, 0, v240\n\t"
      "v_sub_f32 v225, 0, v240\n\t"
      "v_sub_f32 v226, 0, v240\n\t"
      "v_sub_f32 v227, 0, v240\n\t"
      "v_sub_f32 v228, 0, v241\n\t"
      "v_sub_f32 v229, 0, v241\n\t"
      "v_sub_f32 v230, 0, v241\n\t"
      "v_sub_f32 v231, 0, v241\n\t"
      "v_sub_f32 v232, 0, v242\n\t"
      "v_sub_f32 v233, 0, v242\n\t"
      "v_sub_f32 v234, 0, v242\n\t"
      "v_sub_f32 v235, 0, v242\n\t"
      "v_sub_f32 v236, 0, v243\n\t"
      "v_sub_f32 v237, 0, v243\n\t"
      "v_sub_f32 v238, 0, v243\n\t"
      "v_sub_f32 v239, 0, v243\n\t"
      "v_sub_f32 v128, v128, v240\n\t"
      "v_sub_f32 v129, v129, v240\n\t"
      "v_sub_f32 v130, v130, v240\n\t"
      "v_sub_f32 v131, v131, v240\n\t"
      "v_sub_f32 v144, v144, v240\n\t"
      "v_sub_f32 v145, v145, v240\n\t"
      "v_sub_f32 v146, v146, v240\n\t"
      "v_sub_f32 v147, v147, v240\n\t"
      "v_sub_f32 v132, v132, v241\n\t"
      "v_sub_f32 v133, v133, v241\n\t"
      "v_sub_f32 v134, v134, v241\n\t"
      "v_sub_f32 v135, v135, v241\n\t"
      "v_sub_f32 v148, v148, v241\n\t"
      "v_sub_f32 v149, v149, v241\n\t"
      "v_sub_f32 v150, v150, v241\n\t"
      "v_sub_f32 v151, v151, v241\n\t"
      "v_sub_f32 v136, v136, v242\n\t"
      "v_sub_f32 v137, v137, v242\n\t"
      "v_sub_f32 v138, v138, v242\n\t"
      "v_sub_f32 v139, v139, v242\n\t"
      "v_sub_f32 v152, v152, v242\n\t"
      "v_sub_f32 v153, v153, v242\n\t"
      "v_sub_f32 v154, v154, v242\n\t"
      "v_sub_f32 v155, v155, v242\n\t"
      "v_sub_f32 v140, v140, v243\n\t"
      "v_sub_f32 v141, v141, v243\n\t"
      "v_sub_f32 v142, v142, v243\n\t"
      "v_sub_f32 v143, v143, v243\n\t"
      "v_sub_f32 v156, v156, v243\n\t"
      "v_sub_f32 v157, v157, v243\n\t"
      "v_sub_f32 v158, v158, v243\n\t"
      "v_sub_f32 v159, v159, v243\n\t"
      "s_waitcnt lgkmcnt(0)\n\t"
      "v_mfma_f32_16x16x32_bf16 v[160:163], a[144:147], v[16:19], v[224:227]\n\t"
      "v_exp_f32 v128, v128\n\t"
      "v_mfma_f32_16x16x32_bf16 v[164:167], a[144:147], v[32:35], v[228:231]\n\t"
      "v_exp_f32 v129, v129\n\t"
      "v_mfma_f32_16x16x32_bf16 v[168:171], a[144:147], v[48:51], v[232:235]\n\t"
      "v_exp_f32 v130, v130\n\t"
      "v_mfma_f32_16x16x32_bf16 v[172:175], a[144:147], v[64:67], v[236:239]\n\t"
      "v_exp_f32 v131, v131\n\t"
      "v_mfma_f32_16x16x32_bf16 v[160:163], a[148:151], v[20:23], v[160:163]\n\t"
      "v_exp_f32 v144, v144\n\t"
      "v_mfma_f32_16x16x32_bf16 v[164:167], a[148:151], v[36:39], v[164:167]\n\t"
      "v_exp_f32 v145, v145\n\t"
      "v_mfma_f32_16x16x32_bf16 v[168:171], a[148:151], v[52:55], v[168:171]\n\t"
      "v_exp_f32 v146, v146\n\t"
      "v_mfma_f32_16x16x32_bf16 v[172:175], a[148:151], v[68:71], v[172:175]\n\t"
      "v_exp_f32 v147, v147\n\t"
      "v_mfma_f32_16x16x32_bf16 v[160:163], a[152:155], v[24:27], v[160:163]\n\t"
      "v_exp_f32 v132, v132\n\t"
      "v_mfma_f32_16x16x32_bf16 v[164:167], a[152:155], v[40:43], v[164:167]\n\t"
      "v_exp_f32 v133, v133\n\t"
      "v_mfma_f32_16x16x32_bf16 v[168:171], a[152:155], v[56:59], v[168:171]\n\t"
      "v_exp_f32 v134, v134\n\t"
      "v_mfma_f32_16x16x32_bf16 v[172:175], a[152:155], v[72:75], v[172:175]\n\t"
      "v_exp_f32 v135, v135\n\t"
      "v_mfma_f32_16x16x32_bf16 v[160:163], a[156:159], v[28:31], v[160:163]\n\t"
      "v_exp_f32 v148, v148\n\t"
      "v_mfma_f32_16x16x32_bf16 v[164:167], a[156:159], v[44:47], v[164:167]\n\t"
      "v_exp_f32 v149, v149\n\t"
      "v_mfma_f32_16x16x32_bf16 v[168:171], a[156:159], v[60:63], v[168:171]\n\t"
      "v_exp_f32 v150, v150\n\t"
      "v_mfma_f32_16x16x32_bf16 v[172:175], a[156:159], v[76:79], v[172:175]\n\t"
      "v_exp_f32 v151, v151\n\t"
      "v_mfma_f32_16x16x32_bf16 v[176:179], a[160:163], v[16:19], v[224:227]\n\t"
      "v_exp_f32 v136, v136\n\t"
      "v_mfma_f32_16x16x32_bf16 v[180:183], a[160:163], v[32:35], v[228:231]\n\t"
      "v_exp_f32 v137, v137\n\t"
      "v_mfma_f32_16x16x32_bf16 v[184:187], a[160:163], v[48:51], v[232:235]\n\t"
      "v_exp_f32 v138, v138\n\t"
      "v_mfma_f32_16x16x32_bf16 v[188:191], a[160:163], v[64:67], v[236:239]\n\t"
      "v_exp_f32 v139, v139\n\t"
      "v_mfma_f32_16x16x32_bf16 v[176:179], a[164:167], v[20:23], v[176:179]\n\t"
      "v_exp_f32 v152, v152\n\t"
      "v_mfma_f32_16x16x32_bf16 v[180:183], a[164:167], v[36:39], v[180:183]\n\t"
      "v_exp_f32 v153, v153\n\t"
      "v_mfma_f32_16x16x32_bf16 v[184:187], a[164:167], v[52:55], v[184:187]\n\t"
      "v_exp_f32 v154, v154\n\t"
      "v_mfma_f32_16x16x32_bf16 v[188:191], a[164:167], v[68:71], v[188:191]\n\t"
      "v_exp_f32 v155, v155\n\t"
      "v_mfma_f32_16x16x32_bf16 v[176:179], a[168:171], v[24:27], v[176:179]\n\t"
      "v_exp_f32 v140, v140\n\t"
      "v_mfma_f32_16x16x32_bf16 v[180:183], a[168:171], v[40:43], v[180:183]\n\t"
      "v_exp_f32 v141, v141\n\t"
      "v_mfma_f32_16x16x32_bf16 v[184:187], a[168:171], v[56:59], v[184:187]\n\t"
      "v_exp_f32 v142, v142\n\t"
      "v_mfma_f32_16x16x32_bf16 v[188:191], a[168:171], v[72:75], v[188:191]\n\t"
      "v_exp_f32 v143, v143\n\t"
      "v_mfma_f32_16x16x32_bf16 v[176:179], a[172:175], v[28:31], v[176:179]\n\t"
      "v_exp_f32 v156, v156\n\t"
      "v_mfma_f32_16x16x32_bf16 v[180:183], a[172:175], v[44:47], v[180:183]\n\t"
      "v_exp_f32 v157, v157\n\t"
      "v_mfma_f32_16x16x32_bf16 v[184:187], a[172:175], v[60:63], v[184:187]\n\t"
      "v_exp_f32 v158, v158\n\t"
      "v_mfma_f32_16x16x32_bf16 v[188:191], a[172:175], v[76:79], v[188:191]\n\t"
      "v_exp_f32 v159, v159\n\t"
      "s_add_u32 s92, s92, 0x8000\n\t"
      "s_cmp_ge_u32 s92, 0x28000\n\t"
      "s_cselect_b32 s97, 0xfffe0000, s44\n\t"
      "s_cselect_b32 s92, 0, s92\n\t"
      "s_add_u32 s96, s96, 0x8000\n\t"
      "s_cmp_ge_u32 s96, 0x28000\n\t"
      "s_cselect_b32 s93, 0x28000, 0\n\t"
      "s_sub_u32 s96, s96, s93\n\t"
      "s_add_u32 s95, s95, 0x4000\n\t"
      "v_cvt_pk_bf16_f32 v192, v128, v129\n\t"
      "v_cvt_pk_bf16_f32 v193, v130, v131\n\t"
      "v_cvt_pk_bf16_f32 v194, v144, v145\n\t"
      "v_cvt_pk_bf16_f32 v195, v146, v147\n\t"
      "v_cvt_pk_bf16_f32 v196, v132, v133\n\t"
      "v_cvt_pk_bf16_f32 v197, v134, v135\n\t"
      "v_cvt_pk_bf16_f32 v198, v148, v149\n\t"
      "v_cvt_pk_bf16_f32 v199, v150, v151\n\t"
      "v_cvt_pk_bf16_f32 v200, v136, v137\n\t"
      "v_cvt_pk_bf16_f32 v201, v138, v139\n\t"
      "v_cvt_pk_bf16_f32 v202, v152, v153\n\t"
      "v_cvt_pk_bf16_f32 v203, v154, v155\n\t"
      "v_cvt_pk_bf16_f32 v204, v140, v141\n\t"
      "v_cvt_pk_bf16_f32 v205, v142, v143\n\t"
      "v_cvt_pk_bf16_f32 v206, v156, v157\n\t"
      "v_cvt_pk_bf16_f32 v207, v158, v159\n\t"
      "s_waitcnt vmcnt(8)\n\t"
      "s_barrier\n\t"
      "v_add_u32 v112, s97, v112\n\t"
      "v_add_u32 v113, s97, v113\n\t"
      "v_add_u32 v114, s97, v114\n\t"
      "v_add_u32 v115, s97, v115\n\t"
      "ds_read_b128 a[144:147], v112\n\t"
      "ds_read_b128 a[148:151], v113\n\t"
      "ds_read_b128 a[152:155], v114\n\t"
      "ds_read_b128 a[156:159], v115\n\t"
      "ds_read_b128 a[160:163], v112 offset:4096\n\t"
      "ds_read_b128 a[164:167], v113 offset:4096\n\t"
      "ds_read_b128 a[168:171], v114 offset:4096\n\t"
      "ds_read_b128 a[172:175], v115 offset:4096\n\t"
      "s_mov_b32 s49, s97\n\t"
      "s_mov_b32 m0, s96\n\t"
      "s_nop 0\n\t"
      "buffer_load_dwordx4 v8, s[72:75], s95 offen lds\n\t"
      "buffer_load_dwordx4 v9, s[72:75], s95 offen offset:1024 lds\n\t"
      "buffer_load_dwordx4 v10, s[72:75], s95 offen offset:2048 lds\n\t"
      "buffer_load_dwordx4 v11, s[72:75], s95 offen offset:3072 lds\n\t"
      "s_waitcnt lgkmcnt(0)\n\t"
      "v_mfma_f32_16x16x32_bf16 v[128:131], a[144:147], v[16:19], v[224:227]\n\t"
      "v_exp_f32 v160, v160\n\t"
      "v_mfma_f32_16x16x32_bf16 v[132:135], a[144:147], v[32:35], v[228:231]\n\t"
      "ds_read_b64_tr_b16 v[80:81], v116 offset:16384\n\t"
      "ds_read_b64_tr_b16 v[82:83], v116 offset:20480\n\t"
      "v_mfma_f32_16x16x32_bf16 v[136:139], a[144:147], v[48:51], v[232:235]\n\t"
      "v_exp_f32 v161, v161\n\t"
      "v_mfma_f32_16x16x32_bf16 v[140:143], a[144:147], v[64:67], v[236:239]\n\t"
      "ds_read_b64_tr_b16 v[84:85], v117 offset:16384\n\t"
      "ds_read_b64_tr_b16 v[86:87], v117 offset:20480\n\t"
      "v_mfma_f32_16x16x32_bf16 v[128:131], a[148:151], v[20:23], v[128:131]\n\t"
      "v_exp_f32 v162, v162\n\t"
      "v_mfma_f32_16x16x32_bf16 v[132:135], a[148:151], v[36:39], v[132:135]\n\t"
      "ds_read_b64_tr_b16 v[88:89], v116 offset:16896\n\t"
      "ds_read_b64_tr_b16 v[90:91], v116 offset:20992\n\t"
      "v_mfma_f32_16x16x32_bf16 v[136:139], a[148:151], v[52:55], v[136:139]\n\t"
      "v_exp_f32 v163, v163\n\t"
      "v_mfma_f32_16x16x32_bf16 v[140:143], a[148:151], v[68:71], v[140:143]\n\t"
      "ds_read_b64_tr_b16 v[92:93], v117 offset:16896\n\t"
      "ds_read_b64_tr_b16 v[94:95], v117 offset:20992\n\t"
      "v_mfma_f32_16x16x32_bf16 v[128:131], a[152:155], v[24:27], v[128:131]\n\t"
      "v_exp_f32 v176, v176\n\t"
      "v_mfma_f32_16x16x32_bf16 v[132:135], a[152:155], v[40:43], v[132:135]\n\t"
      "ds_read_b64_tr_b16 v[96:97], v116 offset:17408\n\t"
      "ds_read_b64_tr_b16 v[98:99], v116 offset:21504\n\t"
      "v_mfma_f32_16x16x32_bf16 v[136:139], a[152:155], v[56:59], v[136:139]\n\t"
      "v_exp_f32 v177, v177\n\t"
      "v_mfma_f32_16x16x32_bf16 v[140:143], a[152:155], v[72:75], v[140:143]\n\t"
      "ds_read_b64_tr_b16 v[100:101], v117 offset:17408\n\t"
      "ds_read_b64_tr_b16 v[102:103], v117 offset:21504\n\t"
      "v_mfma_f32_16x16x32_bf16 v[128:131], a[156:159], v[28:31], v[128:131]\n\t"
      "v_exp_f32 v178, v178\n\t"
      "v_mfma_f32_16x16x32_bf16 v[132:135], a[156:159], v[44:47], v[132:135]\n\t"
      "ds_read_b64_tr_b16 v[104:105], v116 offset:17920\n\t"
      "ds_read_b64_tr_b16 v[106:107], v116 offset:22016\n\t"
      "v_mfma_f32_16x16x32_bf16 v[136:139], a[156:159], v[60:63], v[136:139]\n\t"
      "v_exp_f32 v179, v179\n\t"
      "v_mfma_f32_16x16x32_bf16 v[140:143], a[156:159], v[76:79], v[140:143]\n\t"
      "ds_read_b64_tr_b16 v[108:109], v117 offset:17920\n\t"
      "ds_read_b64_tr_b16 v[110:111], v117 offset:22016\n\t"
      "v_mfma_f32_16x16x32_bf16 v[144:147], a[160:163], v[16:19], v[224:227]\n\t"
      "v_exp_f32 v164, v164\n\t"
      "v_mfma_f32_16x16x32_bf16 v[148:151], a[160:163], v[32:35], v[228:231]\n\t"
      "v_cvt_pk_bf16_f32 v208, v160, v161\n\t"
      "v_cvt_pk_bf16_f32 v209, v162, v163\n\t"
      "v_mfma_f32_16x16x32_bf16 v[152:155], a[160:163], v[48:51], v[232:235]\n\t"
      "v_exp_f32 v165, v165\n\t"
      "v_mfma_f32_16x16x32_bf16 v[156:159], a[160:163], v[64:67], v[236:239]\n\t"
      "v_cvt_pk_bf16_f32 v210, v176, v177\n\t"
      "v_cvt_pk_bf16_f32 v211, v178, v179\n\t"
      "v_mfma_f32_16x16x32_bf16 v[144:147], a[164:167], v[20:23], v[144:147]\n\t"
      "v_exp_f32 v166, v166\n\t"
      "v_mfma_f32_16x16x32_bf16 v[148:151], a[164:167], v[36:39], v[148:151]\n\t"
      "v_cvt_pk_bf16_f32 v212, v164, v165\n\t"
      "v_mfma_f32_16x16x32_bf16 v[152:155], a[164:167], v[52:55], v[152:155]\n\t"
      "v_exp_f32 v167, v167\n\t"
      "v_mfma_f32_16x16x32_bf16 v[156:159], a[164:167], v[68:71], v[156:159]\n\t"
      "v_cvt_pk_bf16_f32 v213, v166, v167\n\t"
      "v_mfma_f32_16x16x32_bf16 v[144:147], a[168:171], v[24:27], v[144:147]\n\t"
      "v_exp_f32 v180, v180\n\t"
      "v_mfma_f32_16x16x32_bf16 v[148:151], a[168:171], v[40:43], v[148:151]\n\t"
      "v_exp_f32 v181, v181\n\t"
      "v_mfma_f32_16x16x32_bf16 v[152:155], a[168:171], v[56:59], v[152:155]\n\t"
      "v_exp_f32 v182, v182\n\t"
      "v_mfma_f32_16x16x32_bf16 v[156:159], a[168:171], v[72:75], v[156:159]\n\t"
      "v_cvt_pk_bf16_f32 v214, v180, v181\n\t"
      "v_mfma_f32_16x16x32_bf16 v[144:147], a[172:175], v[28:31], v[144:147]\n\t"
      "v_exp_f32 v183, v183\n\t"
      "v_mfma_f32_16x16x32_bf16 v[148:151], a[172:175], v[44:47], v[148:151]\n\t"
      "v_cvt_pk_bf16_f32 v215, v182, v183\n\t"
      "v_mfma_f32_16x16x32_bf16 v[152:155], a[172:175], v[60:63], v[152:155]\n\t"
      "v_exp_f32 v168, v168\n\t"
      "v_mfma_f32_16x16x32_bf16 v[156:159], a[172:175], v[76:79], v[156:159]\n\t"
      "v_exp_f32 v169, v169\n\t"
      "s_waitcnt lgkmcnt(0)\n\t"
      "v_mfma_f32_16x16x32_bf16 a[0:3], v[80:83], v[192:195], 0\n\t"
      "v_exp_f32 v170, v170\n\t"
      "v_mfma_f32_16x16x32_bf16 a[4:7], v[80:83], v[196:199], 0\n\t"
      "ds_read_b128 a[144:147], v112 offset:8192\n\t"
      "ds_read_b128 a[148:151], v113 offset:8192\n\t"
      "v_mfma_f32_16x16x32_bf16 a[8:11], v[80:83], v[200:203], 0\n\t"
      "v_exp_f32 v171, v171\n\t"
      "v_mfma_f32_16x16x32_bf16 a[12:15], v[80:83], v[204:207], 0\n\t"
      "ds_read_b128 a[152:155], v114 offset:8192\n\t"
      "ds_read_b128 a[156:159], v115 offset:8192\n\t"
      "v_mfma_f32_16x16x32_bf16 a[16:19], v[84:87], v[192:195], 0\n\t"
      "v_exp_f32 v184, v184\n\t"
      "v_mfma_f32_16x16x32_bf16 a[20:23], v[84:87], v[196:199], 0\n\t"
      "ds_read_b128 a[160:163], v112 offset:12288\n\t"
      "ds_read_b128 a[164:167], v113 offset:12288\n\t"
      "v_mfma_f32_16x16x32_bf16 a[24:27], v[84:87], v[200:203], 0\n\t"
      "v_exp_f32 v185, v185\n\t"
      "v_mfma_f32_16x16x32_bf16 a[28:31], v[84:87], v[204:207], 0\n\t"
      "ds_read_b128 a[168:171], v114 offset:12288\n\t"
      "ds_read_b128 a[172:175], v115 offset:12288\n\t"
      "v_mfma_f32_16x16x32_bf16 a[32:35], v[88:91], v[192:195], 0\n\t"
      "v_exp_f32 v186, v186\n\t"
      "v_mfma_f32_16x16x32_bf16 a[36:39], v[88:91], v[196:199], 0\n\t"
      "s_add_u32 m0, s96, 0x4000\n\t"
      "s_nop 0\n\t"
      "buffer_load_dwordx4 v12, s[76:79], s95 offen lds\n\t"
      "v_mfma_f32_16x16x32_bf16 a[40:43], v[88:91], v[200:203], 0\n\t"
      "v_exp_f32 v187, v187\n\t"
      "v_mfma_f32_16x16x32_bf16 a[44:47], v[88:91], v[204:207], 0\n\t"
      "v_cvt_pk_bf16_f32 v216, v168, v169\n\t"
      "v_cvt_pk_bf16_f32 v217, v170, v171\n\t"
      "v_mfma_f32_16x16x32_bf16 a[48:51], v[92:95], v[192:195], 0\n\t"
      "v_exp_f32 v172, v172\n\t"
      "v_mfma_f32_16x16x32_bf16 a[52:55], v[92:95], v[196:199], 0\n\t"
      "buffer_load_dwordx4 v12, s[76:79], s95 offen offset:2048 lds\n\t"
      "v_cvt_pk_bf16_f32 v218, v184, v185\n\t"
      "v_mfma_f32_16x16x32_bf16 a[56:59], v[92:95], v[200:203], 0\n\t"
      "v_exp_f32 v173, v173\n\t"
      "v_mfma_f32_16x16x32_bf16 a[60:63], v[92:95], v[204:207], 0\n\t"
      "v_cvt_pk_bf16_f32 v219, v186, v187\n\t"
      "v_cvt_pk_bf16_f32 v220, v172, v173\n\t"
      "v_mfma_f32_16x16x32_bf16 a[64:67], v[96:99], v[192:195], 0\n\t"
      "v_exp_f32 v174, v174\n\t"
      "v_mfma_f32_16x16x32_bf16 a[68:71], v[96:99], v[196:199], 0\n\t"
      "v_exp_f32 v175, v175\n\t"
      "v_mfma_f32_16x16x32_bf16 a[72:75], v[96:99], v[200:203], 0\n\t"
      "v_exp_f32 v188, v188\n\t"
      "v_mfma_f32_16x16x32_bf16 a[76:79], v[96:99], v[204:207], 0\n\t"
      "s_add_u32 m0, s96, 0x4380\n\t"
      "v_cvt_pk_bf16_f32 v221, v174, v175\n\t"
      "v_mfma_f32_16x16x32_bf16 a[80:83], v[100:103], v[192:195], 0\n\t"
      "v_exp_f32 v189, v189\n\t"
      "v_mfma_f32_16x16x32_bf16 a[84:87], v[100:103], v[196:199], 0\n\t"
      "buffer_load_dwordx4 v12, s[76:79], s95 offen offset:128 lds\n\t"
      "v_cvt_pk_bf16_f32 v222, v188, v189\n\t"
      "v_mfma_f32_16x16x32_bf16 a[88:91], v[100:103], v[200:203], 0\n\t"
      "v_exp_f32 v190, v190\n\t"
      "v_mfma_f32_16x16x32_bf16 a[92:95], v[100:103], v[204:207], 0\n\t"
      "v_exp_f32 v191, v191\n\t"
      "v_mfma_f32_16x16x32_bf16 a[96:99], v[104:107], v[192:195], 0\n\t"
      "v_cvt_pk_bf16_f32 v223, v190, v191\n\t"
      "v_mfma_f32_16x16x32_bf16 a[100:103], v[104:107], v[196:199], 0\n\t"
      "v_mfma_f32_16x16x32_bf16 a[104:107], v[104:107], v[200:203], 0\n\t"
      "v_mfma_f32_16x16x32_bf16 a[108:111], v[104:107], v[204:207], 0\n\t"
      "buffer_load_dwordx4 v12, s[76:79], s95 offen offset:2176 lds\n\t"
      "v_mfma_f32_16x16x32_bf16 a[112:115], v[108:111], v[192:195], 0\n\t"
      "v_mfma_f32_16x16x32_bf16 a[116:119], v[108:111], v[196:199], 0\n\t"
      "v_mfma_f32_16x16x32_bf16 a[120:123], v[108:111], v[200:203], 0\n\t"
      "v_mfma_f32_16x16x32_bf16 a[124:127], v[108:111], v[204:207], 0\n\t"
      "v_mfma_f32_16x16x32_bf16 a[128:131], v[120:123], v[192:195], 0\n\t"
      "v_mfma_f32_16x16x32_bf16 a[132:135], v[120:123], v[196:199], 0\n\t"
      "v_mfma_f32_16x16x32_bf16 a[136:139], v[120:123], v[200:203], 0\n\t"
      "v_mfma_f32_16x16x32_bf16 a[140:143], v[120:123], v[204:207], 0\n\t"
      "s_mov_b32 s45, 4\n\t"
      "K1att:\n\t"
      "s_waitcnt lgkmcnt(0)\n\t"
      "v_mfma_f32_16x16x32_bf16 v[160:163], a[144:147], v[16:19], v[224:227]\n\t"
      "v_exp_f32 v128, v128\n\t"
      "v_mfma_f32_16x16x32_bf16 v[164:167], a[144:147], v[32:35], v[228:231]\n\t"
      "ds_read_b64_tr_b16 v[80:81], v116 offset:24576\n\t"
      "ds_read_b64_tr_b16 v[82:83], v116 offset:28672\n\t"
      "v_mfma_f32_16x16x32_bf16 v[168:171], a[144:147], v[48:51], v[232:235]\n\t"
      "v_exp_f32 v129, v129\n\t"
      "v_mfma_f32_16x16x32_bf16 v[172:175], a[144:147], v[64:67], v[236:239]\n\t"
      "ds_read_b64_tr_b16 v[84:85], v117 offset:24576\n\t"
      "ds_read_b64_tr_b16 v[86:87], v117 offset:28672\n\t"
      "v_mfma_f32_16x16x32_bf16 v[160:163], a[148:151], v[20:23], v[160:163]\n\t"
      "v_exp_f32 v130, v130\n\t"
      "v_mfma_f32_16x16x32_bf16 v[164:167], a[148:151], v[36:39], v[164:167]\n\t"
      "ds_read_b64_tr_b16 v[88:89], v116 offset:25088\n\t"
      "ds_read_b64_tr_b16 v[90:91], v116 offset:29184\n\t"
      "v_mfma_f32_16x16x32_bf16 v[168:171], a[148:151], v[52:55], v[168:171]\n\t"
      "v_exp_f32 v131, v131\n\t"
      "v_mfma_f32_16x16x32_bf16 v[172:175], a[148:151], v[68:71], v[172:175]\n\t"
      "ds_read_b64_tr_b16 v[92:93], v117 offset:25088\n\t"
      "ds_read_b64_tr_b16 v[94:95], v117 offset:29184\n\t"
      "v_mfma_f32_16x16x32_bf16 v[160:163], a[152:155], v[24:27], v[160:163]\n\t"
      "v_exp_f32 v144, v144\n\t"
      "v_mfma_f32_16x16x32_bf16 v[164:167], a[152:155], v[40:43], v[164:167]\n\t"
      "ds_read_b64_tr_b16 v[96:97], v116 offset:25600\n\t"
      "ds_read_b64_tr_b16 v[98:99], v116 offset:29696\n\t"
      "v_mfma_f32_16x16x32_bf16 v[168:171], a[152:155], v[56:59], v[168:171]\n\t"
      "v_exp_f32 v145, v145\n\t"
      "v_mfma_f32_16x16x32_bf16 v[172:175], a[152:155], v[72:75], v[172:175]\n\t"
      "ds_read_b64_tr_b16 v[100:101], v117 offset:25600\n\t"
      "ds_read_b64_tr_b16 v[102:103], v117 offset:29696\n\t"
      "v_mfma_f32_16x16x32_bf16 v[160:163], a[156:159], v[28:31], v[160:163]\n\t"
      "v_exp_f32 v146, v146\n\t"
      "v_mfma_f32_16x16x32_bf16 v[164:167], a[156:159], v[44:47], v[164:167]\n\t"
      "ds_read_b64_tr_b16 v[104:105], v116 offset:26112\n\t"
      "ds_read_b64_tr_b16 v[106:107], v116 offset:30208\n\t"
      "v_mfma_f32_16x16x32_bf16 v[168:171], a[156:159], v[60:63], v[168:171]\n\t"
      "v_exp_f32 v147, v147\n\t"
      "v_mfma_f32_16x16x32_bf16 v[172:175], a[156:159], v[76:79], v[172:175]\n\t"
      "ds_read_b64_tr_b16 v[108:109], v117 offset:26112\n\t"
      "ds_read_b64_tr_b16 v[110:111], v117 offset:30208\n\t"
      "v_mfma_f32_16x16x32_bf16 v[176:179], a[160:163], v[16:19], v[224:227]\n\t"
      "v_exp_f32 v132, v132\n\t"
      "v_mfma_f32_16x16x32_bf16 v[180:183], a[160:163], v[32:35], v[228:231]\n\t"
      "s_add_u32 s92, s92, 0x8000\n\t"
      "s_cmp_ge_u32 s92, 0x28000\n\t"
      "v_mfma_f32_16x16x32_bf16 v[184:187], a[160:163], v[48:51], v[232:235]\n\t"
      "v_exp_f32 v133, v133\n\t"
      "v_mfma_f32_16x16x32_bf16 v[188:191], a[160:163], v[64:67], v[236:239]\n\t"
      "s_cselect_b32 s97, 0xfffe0000, s44\n\t"
      "s_cselect_b32 s92, 0, s92\n\t"
      "v_mfma_f32_16x16x32_bf16 v[176:179], a[164:167], v[20:23], v[176:179]\n\t"
      "v_exp_f32 v134, v134\n\t"
      "v_mfma_f32_16x16x32_bf16 v[180:183], a[164:167], v[36:39], v[180:183]\n\t"
      "s_add_u32 s96, s96, 0x8000\n\t"
      "s_cmp_ge_u32 s96, 0x28000\n\t"
      "v_mfma_f32_16x16x32_bf16 v[184:187], a[164:167], v[52:55], v[184:187]\n\t"
      "v_exp_f32 v135, v135\n\t"
      "v_mfma_f32_16x16x32_bf16 v[188:191], a[164:167], v[68:71], v[188:191]\n\t"
      "s_cselect_b32 s93, 0x28000, 0\n\t"
      "s_sub_u32 s96, s96, s93\n\t"
      "v_mfma_f32_16x16x32_bf16 v[176:179], a[168:171], v[24:27], v[176:179]\n\t"
      "v_exp_f32 v148, v148\n\t"
      "v_mfma_f32_16x16x32_bf16 v[180:183], a[168:171], v[40:43], v[180:183]\n\t"
      "s_add_u32 s95, s95, 0x4000\n\t"
      "v_cvt_pk_bf16_f32 v192, v128, v129\n\t"
      "v_mfma_f32_16x16x32_bf16 v[184:187], a[168:171], v[56:59], v[184:187]\n\t"
      "v_exp_f32 v149, v149\n\t"
      "v_mfma_f32_16x16x32_bf16 v[188:191], a[168:171], v[72:75], v[188:191]\n\t"
      "v_cvt_pk_bf16_f32 v193, v130, v131\n\t"
      "v_cvt_pk_bf16_f32 v194, v144, v145\n\t"
      "v_mfma_f32_16x16x32_bf16 v[176:179], a[172:175], v[28:31], v[176:179]\n\t"
      "v_exp_f32 v150, v150\n\t"
      "v_mfma_f32_16x16x32_bf16 v[180:183], a[172:175], v[44:47], v[180:183]\n\t"
      "v_cvt_pk_bf16_f32 v195, v146, v147\n\t"
      "v_cvt_pk_bf16_f32 v196, v132, v133\n\t"
      "v_mfma_f32_16x16x32_bf16 v[184:187], a[172:175], v[60:63], v[184:187]\n\t"
      "v_exp_f32 v151, v151\n\t"
      "v_mfma_f32_16x16x32_bf16 v[188:191], a[172:175], v[76:79], v[188:191]\n\t"
      "v_cvt_pk_bf16_f32 v197, v134, v135\n\t"
      "v_cvt_pk_bf16_f32 v198, v148, v149\n\t"
      "s_waitcnt vmcnt(8) lgkmcnt(0)\n\t"
      "s_barrier\n\t"
      "v_mfma_f32_16x16x32_bf16 a[0:3], v[80:83], v[208:211], a[0:3]\n\t"
      "v_exp_f32 v136, v136\n\t"
      "v_mfma_f32_16x16x32_bf16 a[4:7], v[80:83], v[212:215], a[4:7]\n\t"
      "v_add_u32 v112, s97, v112\n\t"
      "v_add_u32 v113, s97, v113\n\t"
      "v_mfma_f32_16x16x32_bf16 a[8:11], v[80:83], v[216:219], a[8:11]\n\t"
      "v_exp_f32 v137, v137\n\t"
      "v_mfma_f32_16x16x32_bf16 a[12:15], v[80:83], v[220:223], a[12:15]\n\t"
      "v_add_u32 v114, s97, v114\n\t"
      "v_add_u32 v115, s97, v115\n\t"
      "v_mfma_f32_16x16x32_bf16 a[16:19], v[84:87], v[208:211], a[16:19]\n\t"
      "v_exp_f32 v138, v138\n\t"
      "v_mfma_f32_16x16x32_bf16 a[20:23], v[84:87], v[212:215], a[20:23]\n\t"
      "ds_read_b128 a[144:147], v112\n\t"
      "ds_read_b128 a[148:151], v113\n\t"
      "v_mfma_f32_16x16x32_bf16 a[24:27], v[84:87], v[216:219], a[24:27]\n\t"
      "v_exp_f32 v139, v139\n\t"
      "v_mfma_f32_16x16x32_bf16 a[28:31], v[84:87], v[220:223], a[28:31]\n\t"
      "ds_read_b128 a[152:155], v114\n\t"
      "ds_read_b128 a[156:159], v115\n\t"
      "v_mfma_f32_16x16x32_bf16 a[32:35], v[88:91], v[208:211], a[32:35]\n\t"
      "v_exp_f32 v152, v152\n\t"
      "v_mfma_f32_16x16x32_bf16 a[36:39], v[88:91], v[212:215], a[36:39]\n\t"
      "ds_read_b128 a[160:163], v112 offset:4096\n\t"
      "ds_read_b128 a[164:167], v113 offset:4096\n\t"
      "v_mfma_f32_16x16x32_bf16 a[40:43], v[88:91], v[216:219], a[40:43]\n\t"
      "v_exp_f32 v153, v153\n\t"
      "v_mfma_f32_16x16x32_bf16 a[44:47], v[88:91], v[220:223], a[44:47]\n\t"
      "ds_read_b128 a[168:171], v114 offset:4096\n\t"
      "ds_read_b128 a[172:175], v115 offset:4096\n\t"
      "v_mfma_f32_16x16x32_bf16 a[48:51], v[92:95], v[208:211], a[48:51]\n\t"
      "v_exp_f32 v154, v154\n\t"
      "v_mfma_f32_16x16x32_bf16 a[52:55], v[92:95], v[212:215], a[52:55]\n\t"
      "v_add_u32 v116, s49, v116\n\t"
      "v_add_u32 v117, s49, v117\n\t"
      "v_mfma_f32_16x16x32_bf16 a[56:59], v[92:95], v[216:219], a[56:59]\n\t"
      "v_exp_f32 v155, v155\n\t"
      "v_mfma_f32_16x16x32_bf16 a[60:63], v[92:95], v[220:223], a[60:63]\n\t"
      "s_mov_b32 s49, s97\n\t"
      "s_mov_b32 m0, s96\n\t"
      "v_mfma_f32_16x16x32_bf16 a[64:67], v[96:99], v[208:211], a[64:67]\n\t"
      "v_exp_f32 v140, v140\n\t"
      "v_mfma_f32_16x16x32_bf16 a[68:71], v[96:99], v[212:215], a[68:71]\n\t"
      "buffer_load_dwordx4 v8, s[72:75], s95 offen lds\n\t"
      "buffer_load_dwordx4 v9, s[72:75], s95 offen offset:1024 lds\n\t"
      "v_mfma_f32_16x16x32_bf16 a[72:75], v[96:99], v[216:219], a[72:75]\n\t"
      "v_exp_f32 v141, v141\n\t"
      "v_mfma_f32_16x16x32_bf16 a[76:79], v[96:99], v[220:223], a[76:79]\n\t"
      "v_cvt_pk_bf16_f32 v199, v150, v151\n\t"
      "v_cvt_pk_bf16_f32 v200, v136, v137\n\t"
      "v_mfma_f32_16x16x32_bf16 a[80:83], v[100:103], v[208:211], a[80:83]\n\t"
      "v_exp_f32 v142, v142\n\t"
      "v_mfma_f32_16x16x32_bf16 a[84:87], v[100:103], v[212:215], a[84:87]\n\t"
      "buffer_load_dwordx4 v10, s[72:75], s95 offen offset:2048 lds\n\t"
      "v_cvt_pk_bf16_f32 v201, v138, v139\n\t"
      "v_mfma_f32_16x16x32_bf16 a[88:91], v[100:103], v[216:219], a[88:91]\n\t"
      "v_exp_f32 v143, v143\n\t"
      "v_mfma_f32_16x16x32_bf16 a[92:95], v[100:103], v[220:223], a[92:95]\n\t"
      "v_cvt_pk_bf16_f32 v202, v152, v153\n\t"
      "v_cvt_pk_bf16_f32 v203, v154, v155\n\t"
      "v_mfma_f32_16x16x32_bf16 a[96:99], v[104:107], v[208:211], a[96:99]\n\t"
      "v_exp_f32 v156, v156\n\t"
      "v_mfma_f32_16x16x32_bf16 a[100:103], v[104:107], v[212:215], a[100:103]\n\t"
      "v_cvt_pk_bf16_f32 v204, v140, v141\n\t"
      "v_cvt_pk_bf16_f32 v205, v142, v143\n\t"
      "v_mfma_f32_16x16x32_bf16 a[104:107], v[104:107], v[216:219], a[104:107]\n\t"
      "v_exp_f32 v157, v157\n\t"
      "v_mfma_f32_16x16x32_bf16 a[108:111], v[104:107], v[220:223], a[108:111]\n\t"
      "buffer_load_dwordx4 v11, s[72:75], s95 offen offset:3072 lds\n\t"
      "v_cvt_pk_bf16_f32 v206, v156, v157\n\t"
      "v_mfma_f32_16x16x32_bf16 a[112:115], v[108:111], v[208:211], a[112:115]\n\t"
      "v_exp_f32 v158, v158\n\t"
      "v_mfma_f32_16x16x32_bf16 a[116:119], v[108:111], v[212:215], a[116:119]\n\t"
      "v_exp_f32 v159, v159\n\t"
      "v_mfma_f32_16x16x32_bf16 a[120:123], v[108:111], v[216:219], a[120:123]\n\t"
      "v_cvt_pk_bf16_f32 v207, v158, v159\n\t"
      "v_mfma_f32_16x16x32_bf16 a[124:127], v[108:111], v[220:223], a[124:127]\n\t"
      "v_mfma_f32_16x16x32_bf16 a[128:131], v[120:123], v[208:211], a[128:131]\n\t"
      "v_mfma_f32_16x16x32_bf16 a[132:135], v[120:123], v[212:215], a[132:135]\n\t"
      "v_mfma_f32_16x16x32_bf16 a[136:139], v[120:123], v[216:219], a[136:139]\n\t"
      "v_mfma_f32_16x16x32_bf16 a[140:143], v[120:123], v[220:223], a[140:143]\n\t"
      "s_waitcnt lgkmcnt(0)\n\t"
      "v_mfma_f32_16x16x32_bf16 v[128:131], a[144:147], v[16:19], v[224:227]\n\t"
      "v_exp_f32 v160, v160\n\t"
      "v_mfma_f32_16x16x32_bf16 v[132:135], a[144:147], v[32:35], v[228:231]\n\t"
      "ds_read_b64_tr_b16 v[80:81], v116 offset:16384\n\t"
      "ds_read_b64_tr_b16 v[82:83], v116 offset:20480\n\t"
      "v_mfma_f32_16x16x32_bf16 v[136:139], a[144:147], v[48:51], v[232:235]\n\t"
      "v_exp_f32 v161, v161\n\t"
      "v_mfma_f32_16x16x32_bf16 v[140:143], a[144:147], v[64:67], v[236:239]\n\t"
      "ds_read_b64_tr_b16 v[84:85], v117 offset:16384\n\t"
      "ds_read_b64_tr_b16 v[86:87], v117 offset:20480\n\t"
      "v_mfma_f32_16x16x32_bf16 v[128:131], a[148:151], v[20:23], v[128:131]\n\t"
      "v_exp_f32 v162, v162\n\t"
      "v_mfma_f32_16x16x32_bf16 v[132:135], a[148:151], v[36:39], v[132:135]\n\t"
      "ds_read_b64_tr_b16 v[88:89], v116 offset:16896\n\t"
      "ds_read_b64_tr_b16 v[90:91], v116 offset:20992\n\t"
      "v_mfma_f32_16x16x32_bf16 v[136:139], a[148:151], v[52:55], v[136:139]\n\t"
      "v_exp_f32 v163, v163\n\t"
      "v_mfma_f32_16x16x32_bf16 v[140:143], a[148:151], v[68:71], v[140:143]\n\t"
      "ds_read_b64_tr_b16 v[92:93], v117 offset:16896\n\t"
      "ds_read_b64_tr_b16 v[94:95], v117 offset:20992\n\t"
      "v_mfma_f32_16x16x32_bf16 v[128:131], a[152:155], v[24:27], v[128:131]\n\t"
      "v_exp_f32 v176, v176\n\t"
      "v_mfma_f32_16x16x32_bf16 v[132:135], a[152:155], v[40:43], v[132:135]\n\t"
      "ds_read_b64_tr_b16 v[96:97], v116 offset:17408\n\t"
      "ds_read_b64_tr_b16 v[98:99], v116 offset:21504\n\t"
      "v_mfma_f32_16x16x32_bf16 v[136:139], a[152:155], v[56:59], v[136:139]\n\t"
      "v_exp_f32 v177, v177\n\t"
      "v_mfma_f32_16x16x32_bf16 v[140:143], a[152:155], v[72:75], v[140:143]\n\t"
      "ds_read_b64_tr_b16 v[100:101], v117 offset:17408\n\t"
      "ds_read_b64_tr_b16 v[102:103], v117 offset:21504\n\t"
      "v_mfma_f32_16x16x32_bf16 v[128:131], a[156:159], v[28:31], v[128:131]\n\t"
      "v_exp_f32 v178, v178\n\t"
      "v_mfma_f32_16x16x32_bf16 v[132:135], a[156:159], v[44:47], v[132:135]\n\t"
      "ds_read_b64_tr_b16 v[104:105], v116 offset:17920\n\t"
      "ds_read_b64_tr_b16 v[106:107], v116 offset:22016\n\t"
      "v_mfma_f32_16x16x32_bf16 v[136:139], a[156:159], v[60:63], v[136:139]\n\t"
      "v_exp_f32 v179, v179\n\t"
      "v_mfma_f32_16x16x32_bf16 v[140:143], a[156:159], v[76:79], v[140:143]\n\t"
      "ds_read_b64_tr_b16 v[108:109], v117 offset:17920\n\t"
      "ds_read_b64_tr_b16 v[110:111], v117 offset:22016\n\t"
      "v_mfma_f32_16x16x32_bf16 v[144:147], a[160:163], v[16:19], v[224:227]\n\t"
      "v_exp_f32 v164, v164\n\t"
      "v_mfma_f32_16x16x32_bf16 v[148:151], a[160:163], v[32:35], v[228:231]\n\t"
      "v_cvt_pk_bf16_f32 v208, v160, v161\n\t"
      "v_cvt_pk_bf16_f32 v209, v162, v163\n\t"
      "v_mfma_f32_16x16x32_bf16 v[152:155], a[160:163], v[48:51], v[232:235]\n\t"
      "v_exp_f32 v165, v165\n\t"
      "v_mfma_f32_16x16x32_bf16 v[156:159], a[160:163], v[64:67], v[236:239]\n\t"
      "v_cvt_pk_bf16_f32 v210, v176, v177\n\t"
      "v_cvt_pk_bf16_f32 v211, v178, v179\n\t"
      "v_mfma_f32_16x16x32_bf16 v[144:147], a[164:167], v[20:23], v[144:147]\n\t"
      "v_exp_f32 v166, v166\n\t"
      "v_mfma_f32_16x16x32_bf16 v[148:151], a[164:167], v[36:39], v[148:151]\n\t"
      "v_cvt_pk_bf16_f32 v212, v164, v165\n\t"
      "v_mfma_f32_16x16x32_bf16 v[152:155], a[164:167], v[52:55], v[152:155]\n\t"
      "v_exp_f32 v167, v167\n\t"
      "v_mfma_f32_16x16x32_bf16 v[156:159], a[164:167], v[68:71], v[156:159]\n\t"
      "v_cvt_pk_bf16_f32 v213, v166, v167\n\t"
      "v_mfma_f32_16x16x32_bf16 v[144:147], a[168:171], v[24:27], v[144:147]\n\t"
      "v_exp_f32 v180, v180\n\t"
      "v_mfma_f32_16x16x32_bf16 v[148:151], a[168:171], v[40:43], v[148:151]\n\t"
      "v_exp_f32 v181, v181\n\t"
      "v_mfma_f32_16x16x32_bf16 v[152:155], a[168:171], v[56:59], v[152:155]\n\t"
      "v_exp_f32 v182, v182\n\t"
      "v_mfma_f32_16x16x32_bf16 v[156:159], a[168:171], v[72:75], v[156:159]\n\t"
      "v_cvt_pk_bf16_f32 v214, v180, v181\n\t"
      "v_mfma_f32_16x16x32_bf16 v[144:147], a[172:175], v[28:31], v[144:147]\n\t"
      "v_exp_f32 v183, v183\n\t"
      "v_mfma_f32_16x16x32_bf16 v[148:151], a[172:175], v[44:47], v[148:151]\n\t"
      "v_cvt_pk_bf16_f32 v215, v182, v183\n\t"
      "v_mfma_f32_16x16x32_bf16 v[152:155], a[172:175], v[60:63], v[152:155]\n\t"
      "v_exp_f32 v168, v168\n\t"
      "v_mfma_f32_16x16x32_bf16 v[156:159], a[172:175], v[76:79], v[156:159]\n\t"
      "v_exp_f32 v169, v169\n\t"
      "s_waitcnt lgkmcnt(0)\n\t"
      "v_mfma_f32_16x16x32_bf16 a[0:3], v[80:83], v[192:195], a[0:3]\n\t"
      "v_exp_f32 v170, v170\n\t"
      "v_mfma_f32_16x16x32_bf16 a[4:7], v[80:83], v[196:199], a[4:7]\n\t"
      "ds_read_b128 a[144:147], v112 offset:8192\n\t"
      "ds_read_b128 a[148:151], v113 offset:8192\n\t"
      "v_mfma_f32_16x16x32_bf16 a[8:11], v[80:83], v[200:203], a[8:11]\n\t"
      "v_exp_f32 v171, v171\n\t"
      "v_mfma_f32_16x16x32_bf16 a[12:15], v[80:83], v[204:207], a[12:15]\n\t"
      "ds_read_b128 a[152:155], v114 offset:8192\n\t"
      "ds_read_b128 a[156:159], v115 offset:8192\n\t"
      "v_mfma_f32_16x16x32_bf16 a[16:19], v[84:87], v[192:195], a[16:19]\n\t"
      "v_exp_f32 v184, v184\n\t"
      "v_mfma_f32_16x16x32_bf16 a[20:23], v[84:87], v[196:199], a[20:23]\n\t"
      "ds_read_b128 a[160:163], v112 offset:12288\n\t"
      "ds_read_b128 a[164:167], v113 offset:12288\n\t"
      "v_mfma_f32_16x16x32_bf16 a[24:27], v[84:87], v[200:203], a[24:27]\n\t"
      "v_exp_f32 v185, v185\n\t"
      "v_mfma_f32_16x16x32_bf16 a[28:31], v[84:87], v[204:207], a[28:31]\n\t"
      "ds_read_b128 a[168:171], v114 offset:12288\n\t"
      "ds_read_b128 a[172:175], v115 offset:12288\n\t"
      "v_mfma_f32_16x16x32_bf16 a[32:35], v[88:91], v[192:195], a[32:35]\n\t"
      "v_exp_f32 v186, v186\n\t"
      "v_mfma_f32_16x16x32_bf16 a[36:39], v[88:91], v[196:199], a[36:39]\n\t"
      "s_add_u32 m0, s96, 0x4000\n\t"
      "s_nop 0\n\t"
      "buffer_load_dwordx4 v12, s[76:79], s95 offen lds\n\t"
      "v_mfma_f32_16x16x32_bf16 a[40:43], v[88:91], v[200:203], a[40:43]\n\t"
      "v_exp_f32 v187, v187\n\t"
      "v_mfma_f32_16x16x32_bf16 a[44:47], v[88:91], v[204:207], a[44:47]\n\t"
      "v_cvt_pk_bf16_f32 v216, v168, v169\n\t"
      "v_cvt_pk_bf16_f32 v217, v170, v171\n\t"
      "v_mfma_f32_16x16x32_bf16 a[48:51], v[92:95], v[192:195], a[48:51]\n\t"
      "v_exp_f32 v172, v172\n\t"
      "v_mfma_f32_16x16x32_bf16 a[52:55], v[92:95], v[196:199], a[52:55]\n\t"
      "buffer_load_dwordx4 v12, s[76:79], s95 offen offset:2048 lds\n\t"
      "v_cvt_pk_bf16_f32 v218, v184, v185\n\t"
      "v_mfma_f32_16x16x32_bf16 a[56:59], v[92:95], v[200:203], a[56:59]\n\t"
      "v_exp_f32 v173, v173\n\t"
      "v_mfma_f32_16x16x32_bf16 a[60:63], v[92:95], v[204:207], a[60:63]\n\t"
      "v_cvt_pk_bf16_f32 v219, v186, v187\n\t"
      "v_cvt_pk_bf16_f32 v220, v172, v173\n\t"
      "v_mfma_f32_16x16x32_bf16 a[64:67], v[96:99], v[192:195], a[64:67]\n\t"
      "v_exp_f32 v174, v174\n\t"
      "v_mfma_f32_16x16x32_bf16 a[68:71], v[96:99], v[196:199], a[68:71]\n\t"
      "v_exp_f32 v175, v175\n\t"
      "v_mfma_f32_16x16x32_bf16 a[72:75], v[96:99], v[200:203], a[72:75]\n\t"
      "v_exp_f32 v188, v188\n\t"
      "v_mfma_f32_16x16x32_bf16 a[76:79], v[96:99], v[204:207], a[76:79]\n\t"
      "s_add_u32 m0, s96, 0x4380\n\t"
      "v_cvt_pk_bf16_f32 v221, v174, v175\n\t"
      "v_mfma_f32_16x16x32_bf16 a[80:83], v[100:103], v[192:195], a[80:83]\n\t"
      "v_exp_f32 v189, v189\n\t"
      "v_mfma_f32_16x16x32_bf16 a[84:87], v[100:103], v[196:199], a[84:87]\n\t"
      "buffer_load_dwordx4 v12, s[76:79], s95 offen offset:128 lds\n\t"
      "v_cvt_pk_bf16_f32 v222, v188, v189\n\t"
      "v_mfma_f32_16x16x32_bf16 a[88:91], v[100:103], v[200:203], a[88:91]\n\t"
      "v_exp_f32 v190, v190\n\t"
      "v_mfma_f32_16x16x32_bf16 a[92:95], v[100:103], v[204:207], a[92:95]\n\t"
      "v_exp_f32 v191, v191\n\t"
      "v_mfma_f32_16x16x32_bf16 a[96:99], v[104:107], v[192:195], a[96:99]\n\t"
      "v_cvt_pk_bf16_f32 v223, v190, v191\n\t"
      "v_mfma_f32_16x16x32_bf16 a[100:103], v[104:107], v[196:199], a[100:103]\n\t"
      "v_mfma_f32_16x16x32_bf16 a[104:107], v[104:107], v[200:203], a[104:107]\n\t"
      "v_mfma_f32_16x16x32_bf16 a[108:111], v[104:107], v[204:207], a[108:111]\n\t"
      "buffer_load_dwordx4 v12, s[76:79], s95 offen offset:2176 lds\n\t"
      "v_mfma_f32_16x16x32_bf16 a[112:115], v[108:111], v[192:195], a[112:115]\n\t"
      "v_mfma_f32_16x16x32_bf16 a[116:119], v[108:111], v[196:199], a[116:119]\n\t"
      "v_mfma_f32_16x16x32_bf16 a[120:123], v[108:111], v[200:203], a[120:123]\n\t"
      "v_mfma_f32_16x16x32_bf16 a[124:127], v[108:111], v[204:207], a[124:127]\n\t"
      "v_mfma_f32_16x16x32_bf16 a[128:131], v[120:123], v[192:195], a[128:131]\n\t"
      "v_mfma_f32_16x16x32_bf16 a[132:135], v[120:123], v[196:199], a[132:135]\n\t"
      "v_mfma_f32_16x16x32_bf16 a[136:139], v[120:123], v[200:203], a[136:139]\n\t"
      "v_mfma_f32_16x16x32_bf16 a[140:143], v[120:123], v[204:207], a[140:143]\n\t"
      "s_sub_u32 s45, s45, 1\n\t"
      "s_cmp_lg_u32 s45, 0\n\t"
      "s_cbranch_scc1 K1att\n\t"
      "s_cmp_lg_u32 s99, 0\n\t"
      "s_cbranch_scc1 K1hf\n\t"
      "v_mov_b32 v14, s52\n\t"
      "v_mov_b32 v15, 0\n\t"
      "s_add_u32 s93, s47, 1024\n\t"
      "s_mov_b64 exec, 1\n\t"
      "s_nop 1\n\t"
      "buffer_store_dword v14, v15, s[56:59], s93 offen sc1\n\t"
      "s_mov_b64 exec, -1\n\t"
      "K1hf:\n\t"
      "s_mov_b32 s45, 2\n\t"
      "K1atb:\n\t"
      "s_waitcnt lgkmcnt(0)\n\t"
      "v_mfma_f32_16x16x32_bf16 v[160:163], a[144:147], v[16:19], v[224:227]\n\t"
      "v_exp_f32 v128, v128\n\t"
      "v_mfma_f32_16x16x32_bf16 v[164:167], a[144:147], v[32:35], v[228:231]\n\t"
      "ds_read_b64_tr_b16 v[80:81], v116 offset:24576\n\t"
      "ds_read_b64_tr_b16 v[82:83], v116 offset:28672\n\t"
      "v_mfma_f32_16x16x32_bf16 v[168:171], a[144:147], v[48:51], v[232:235]\n\t"
      "v_exp_f32 v129, v129\n\t"
      "v_mfma_f32_16x16x32_bf16 v[172:175], a[144:147], v[64:67], v[236:239]\n\t"
      "ds_read_b64_tr_b16 v[84:85], v117 offset:24576\n\t"
      "ds_read_b64_tr_b16 v[86:87], v117 offset:28672\n\t"
      "v_mfma_f32_16x16x32_bf16 v[160:163], a[148:151], v[20:23], v[160:163]\n\t"
      "v_exp_f32 v130, v130\n\t"
      "v_mfma_f32_16x16x32_bf16 v[164:167], a[148:151], v[36:39], v[164:167]\n\t"
      "ds_read_b64_tr_b16 v[88:89], v116 offset:25088\n\t"
      "ds_read_b64_tr_b16 v[90:91], v116 offset:29184\n\t"
      "v_mfma_f32_16x16x32_bf16 v[168:171], a[148:151], v[52:55], v[168:171]\n\t"
      "v_exp_f32 v131, v131\n\t"
      "v_mfma_f32_16x16x32_bf16 v[172:175], a[148:151], v[68:71], v[172:175]\n\t"
      "ds_read_b64_tr_b16 v[92:93], v117 offset:25088\n\t"
      "ds_read_b64_tr_b16 v[94:95], v117 offset:29184\n\t"
      "v_mfma_f32_16x16x32_bf16 v[160:163], a[152:155], v[24:27], v[160:163]\n\t"
      "v_exp_f32 v144, v144\n\t"
      "v_mfma_f32_16x16x32_bf16 v[164:167], a[152:155], v[40:43], v[164:167]\n\t"
      "ds_read_b64_tr_b16 v[96:97], v116 offset:25600\n\t"
      "ds_read_b64_tr_b16 v[98:99], v116 offset:29696\n\t"
      "v_mfma_f32_16x16x32_bf16 v[168:171], a[152:155], v[56:59], v[168:171]\n\t"
      "v_exp_f32 v145, v145\n\t"
      "v_mfma_f32_16x16x32_bf16 v[172:175], a[152:155], v[72:75], v[172:175]\n\t"
      "ds_read_b64_tr_b16 v[100:101], v117 offset:25600\n\t"
      "ds_read_b64_tr_b16 v[102:103], v117 offset:29696\n\t"
      "v_mfma_f32_16x16x32_bf16 v[160:163], a[156:159], v[28:31], v[160:163]\n\t"
      "v_exp_f32 v146, v146\n\t"
      "v_mfma_f32_16x16x32_bf16 v[164:167], a[156:159], v[44:47], v[164:167]\n\t"
      "ds_read_b64_tr_b16 v[104:105], v116 offset:26112\n\t"
      "ds_read_b64_tr_b16 v[106:107], v116 offset:30208\n\t"
      "v_mfma_f32_16x16x32_bf16 v[168:171], a[156:159], v[60:63], v[168:171]\n\t"
      "v_exp_f32 v147, v147\n\t"
      "v_mfma_f32_16x16x32_bf16 v[172:175], a[156:159], v[76:79], v[172:175]\n\t"
      "ds_read_b64_tr_b16 v[108:109], v117 offset:26112\n\t"
      "ds_read_b64_tr_b16 v[110:111], v117 offset:30208\n\t"
      "v_mfma_f32_16x16x32_bf16 v[176:179], a[160:163], v[16:19], v[224:227]\n\t"
      "v_exp_f32 v132, v132\n\t"
      "v_mfma_f32_16x16x32_bf16 v[180:183], a[160:163], v[32:35], v[228:231]\n\t"
      "s_add_u32 s92, s92, 0x8000\n\t"
      "s_cmp_ge_u32 s92, 0x28000\n\t"
      "v_mfma_f32_16x16x32_bf16 v[184:187], a[160:163], v[48:51], v[232:235]\n\t"
      "v_exp_f32 v133, v133\n\t"
      "v_mfma_f32_16x16x32_bf16 v[188:191], a[160:163], v[64:67], v[236:239]\n\t"
      "s_cselect_b32 s97, 0xfffe0000, s44\n\t"
      "s_cselect_b32 s92, 0, s92\n\t"
      "v_mfma_f32_16x16x32_bf16 v[176:179], a[164:167], v[20:23], v[176:179]\n\t"
      "v_exp_f32 v134, v134\n\t"
      "v_mfma_f32_16x16x32_bf16 v[180:183], a[164:167], v[36:39], v[180:183]\n\t"
      "v_cvt_pk_bf16_f32 v192, v128, v129\n\t"
      "v_cvt_pk_bf16_f32 v193, v130, v131\n\t"
      "v_mfma_f32_16x16x32_bf16 v[184:187], a[164:167], v[52:55], v[184:187]\n\t"
      "v_exp_f32 v135, v135\n\t"
      "v_mfma_f32_16x16x32_bf16 v[188:191], a[164:167], v[68:71], v[188:191]\n\t"
      "v_cvt_pk_bf16_f32 v194, v144, v145\n\t"
      "v_cvt_pk_bf16_f32 v195, v146, v147\n\t"
      "v_mfma_f32_16x16x32_bf16 v[176:179], a[168:171], v[24:27], v[176:179]\n\t"
      "v_exp_f32 v148, v148\n\t"
      "v_mfma_f32_16x16x32_bf16 v[180:183], a[168:171], v[40:43], v[180:183]\n\t"
      "v_cvt_pk_bf16_f32 v196, v132, v133\n\t"
      "v_cvt_pk_bf16_f32 v197, v134, v135\n\t"
      "v_mfma_f32_16x16x32_bf16 v[184:187], a[168:171], v[56:59], v[184:187]\n\t"
      "v_exp_f32 v149, v149\n\t"
      "v_mfma_f32_16x16x32_bf16 v[188:191], a[168:171], v[72:75], v[188:191]\n\t"
      "v_cvt_pk_bf16_f32 v198, v148, v149\n\t"
      "v_mfma_f32_16x16x32_bf16 v[176:179], a[172:175], v[28:31], v[176:179]\n\t"
      "v_exp_f32 v150, v150\n\t"
      "v_mfma_f32_16x16x32_bf16 v[180:183], a[172:175], v[44:47], v[180:183]\n\t"
      "v_exp_f32 v151, v151\n\t"
      "v_mfma_f32_16x16x32_bf16 v[184:187], a[172:175], v[60:63], v[184:187]\n\t"
      "v_exp_f32 v136, v136\n\t"
      "v_mfma_f32_16x16x32_bf16 v[188:191], a[172:175], v[76:79], v[188:191]\n\t"
      "v_cvt_pk_bf16_f32 v199, v150, v151\n\t"
      "s_waitcnt vmcnt(0) lgkmcnt(0)\n\t"
      "s_barrier\n\t"
      "v_mfma_f32_16x16x32_bf16 a[0:3], v[80:83], v[208:211], a[0:3]\n\t"
      "v_exp_f32 v137, v137\n\t"
      "v_mfma_f32_16x16x32_bf16 a[4:7], v[80:83], v[212:215], a[4:7]\n\t"
      "v_add_u32 v112, s97, v112\n\t"
      "v_add_u32 v113, s97, v113\n\t"
      "v_mfma_f32_16x16x32_bf16 a[8:11], v[80:83], v[216:219], a[8:11]\n\t"
      "v_exp_f32 v138, v138\n\t"
      "v_mfma_f32_16x16x32_bf16 a[12:15], v[80:83], v[220:223], a[12:15]\n\t"
      "v_add_u32 v114, s97, v114\n\t"
      "v_add_u32 v115, s97, v115\n\t"
      "v_mfma_f32_16x16x32_bf16 a[16:19], v[84:87], v[208:211], a[16:19]\n\t"
      "v_exp_f32 v139, v139\n\t"
      "v_mfma_f32_16x16x32_bf16 a[20:23], v[84:87], v[212:215], a[20:23]\n\t"
      "ds_read_b128 a[144:147], v112\n\t"
      "ds_read_b128 a[148:151], v113\n\t"
      "v_mfma_f32_16x16x32_bf16 a[24:27], v[84:87], v[216:219], a[24:27]\n\t"
      "v_exp_f32 v152, v152\n\t"
      "v_mfma_f32_16x16x32_bf16 a[28:31], v[84:87], v[220:223], a[28:31]\n\t"
      "ds_read_b128 a[152:155], v114\n\t"
      "ds_read_b128 a[156:159], v115\n\t"
      "v_mfma_f32_16x16x32_bf16 a[32:35], v[88:91], v[208:211], a[32:35]\n\t"
      "v_exp_f32 v153, v153\n\t"
      "v_mfma_f32_16x16x32_bf16 a[36:39], v[88:91], v[212:215], a[36:39]\n\t"
      "ds_read_b128 a[160:163], v112 offset:4096\n\t"
      "ds_read_b128 a[164:167], v113 offset:4096\n\t"
      "v_mfma_f32_16x16x32_bf16 a[40:43], v[88:91], v[216:219], a[40:43]\n\t"
      "v_exp_f32 v154, v154\n\t"
      "v_mfma_f32_16x16x32_bf16 a[44:47], v[88:91], v[220:223], a[44:47]\n\t"
      "ds_read_b128 a[168:171], v114 offset:4096\n\t"
      "ds_read_b128 a[172:175], v115 offset:4096\n\t"
      "v_mfma_f32_16x16x32_bf16 a[48:51], v[92:95], v[208:211], a[48:51]\n\t"
      "v_exp_f32 v155, v155\n\t"
      "v_mfma_f32_16x16x32_bf16 a[52:55], v[92:95], v[212:215], a[52:55]\n\t"
      "v_add_u32 v116, s49, v116\n\t"
      "v_add_u32 v117, s49, v117\n\t"
      "v_mfma_f32_16x16x32_bf16 a[56:59], v[92:95], v[216:219], a[56:59]\n\t"
      "v_exp_f32 v140, v140\n\t"
      "v_mfma_f32_16x16x32_bf16 a[60:63], v[92:95], v[220:223], a[60:63]\n\t"
      "s_mov_b32 s49, s97\n\t"
      "v_cvt_pk_bf16_f32 v200, v136, v137\n\t"
      "v_mfma_f32_16x16x32_bf16 a[64:67], v[96:99], v[208:211], a[64:67]\n\t"
      "v_exp_f32 v141, v141\n\t"
      "v_mfma_f32_16x16x32_bf16 a[68:71], v[96:99], v[212:215], a[68:71]\n\t"
      "v_cvt_pk_bf16_f32 v201, v138, v139\n\t"
      "v_cvt_pk_bf16_f32 v202, v152, v153\n\t"
      "v_mfma_f32_16x16x32_bf16 a[72:75], v[96:99], v[216:219], a[72:75]\n\t"
      "v_exp_f32 v142, v142\n\t"
      "v_mfma_f32_16x16x32_bf16 a[76:79], v[96:99], v[220:223], a[76:79]\n\t"
      "v_cvt_pk_bf16_f32 v203, v154, v155\n\t"
      "v_cvt_pk_bf16_f32 v204, v140, v141\n\t"
      "v_mfma_f32_16x16x32_bf16 a[80:83], v[100:103], v[208:211], a[80:83]\n\t"
      "v_exp_f32 v143, v143\n\t"
      "v_mfma_f32_16x16x32_bf16 a[84:87], v[100:103], v[212:215], a[84:87]\n\t"
      "v_cvt_pk_bf16_f32 v205, v142, v143\n\t"
      "v_mfma_f32_16x16x32_bf16 a[88:91], v[100:103], v[216:219], a[88:91]\n\t"
      "v_exp_f32 v156, v156\n\t"
      "v_mfma_f32_16x16x32_bf16 a[92:95], v[100:103], v[220:223], a[92:95]\n\t"
      "v_exp_f32 v157, v157\n\t"
      "v_mfma_f32_16x16x32_bf16 a[96:99], v[104:107], v[208:211], a[96:99]\n\t"
      "v_exp_f32 v158, v158\n\t"
      "v_mfma_f32_16x16x32_bf16 a[100:103], v[104:107], v[212:215], a[100:103]\n\t"
      "v_cvt_pk_bf16_f32 v206, v156, v157\n\t"
      "v_mfma_f32_16x16x32_bf16 a[104:107], v[104:107], v[216:219], a[104:107]\n\t"
      "v_exp_f32 v159, v159\n\t"
      "v_mfma_f32_16x16x32_bf16 a[108:111], v[104:107], v[220:223], a[108:111]\n\t"
      "v_cvt_pk_bf16_f32 v207, v158, v159\n\t"
      "v_mfma_f32_16x16x32_bf16 a[112:115], v[108:111], v[208:211], a[112:115]\n\t"
      "v_mfma_f32_16x16x32_bf16 a[116:119], v[108:111], v[212:215], a[116:119]\n\t"
      "v_mfma_f32_16x16x32_bf16 a[120:123], v[108:111], v[216:219], a[120:123]\n\t"
      "v_mfma_f32_16x16x32_bf16 a[124:127], v[108:111], v[220:223], a[124:127]\n\t"
      "v_mfma_f32_16x16x32_bf16 a[128:131], v[120:123], v[208:211], a[128:131]\n\t"
      "v_mfma_f32_16x16x32_bf16 a[132:135], v[120:123], v[212:215], a[132:135]\n\t"
      "v_mfma_f32_16x16x32_bf16 a[136:139], v[120:123], v[216:219], a[136:139]\n\t"
      "v_mfma_f32_16x16x32_bf16 a[140:143], v[120:123], v[220:223], a[140:143]\n\t"
      "s_waitcnt lgkmcnt(0)\n\t"
      "v_mfma_f32_16x16x32_bf16 v[128:131], a[144:147], v[16:19], v[224:227]\n\t"
      "v_exp_f32 v160, v160\n\t"
      "v_mfma_f32_16x16x32_bf16 v[132:135], a[144:147], v[32:35], v[228:231]\n\t"
      "ds_read_b64_tr_b16 v[80:81], v116 offset:16384\n\t"
      "ds_read_b64_tr_b16 v[82:83], v116 offset:20480\n\t"
      "v_mfma_f32_16x16x32_bf16 v[136:139], a[144:147], v[48:51], v[232:235]\n\t"
      "v_exp_f32 v161, v161\n\t"
      "v_mfma_f32_16x16x32_bf16 v[140:143], a[144:147], v[64:67], v[236:239]\n\t"
      "ds_read_b64_tr_b16 v[84:85], v117 offset:16384\n\t"
      "ds_read_b64_tr_b16 v[86:87], v117 offset:20480\n\t"
      "v_mfma_f32_16x16x32_bf16 v[128:131], a[148:151], v[20:23], v[128:131]\n\t"
      "v_exp_f32 v162, v162\n\t"
      "v_mfma_f32_16x16x32_bf16 v[132:135], a[148:151], v[36:39], v[132:135]\n\t"
      "ds_read_b64_tr_b16 v[88:89], v116 offset:16896\n\t"
      "ds_read_b64_tr_b16 v[90:91], v116 offset:20992\n\t"
      "v_mfma_f32_16x16x32_bf16 v[136:139], a[148:151], v[52:55], v[136:139]\n\t"
      "v_exp_f32 v163, v163\n\t"
      "v_mfma_f32_16x16x32_bf16 v[140:143], a[148:151], v[68:71], v[140:143]\n\t"
      "ds_read_b64_tr_b16 v[92:93], v117 offset:16896\n\t"
      "ds_read_b64_tr_b16 v[94:95], v117 offset:20992\n\t"
      "v_mfma_f32_16x16x32_bf16 v[128:131], a[152:155], v[24:27], v[128:131]\n\t"
      "v_exp_f32 v176, v176\n\t"
      "v_mfma_f32_16x16x32_bf16 v[132:135], a[152:155], v[40:43], v[132:135]\n\t"
      "ds_read_b64_tr_b16 v[96:97], v116 offset:17408\n\t"
      "ds_read_b64_tr_b16 v[98:99], v116 offset:21504\n\t"
      "v_mfma_f32_16x16x32_bf16 v[136:139], a[152:155], v[56:59], v[136:139]\n\t"
      "v_exp_f32 v177, v177\n\t"
      "v_mfma_f32_16x16x32_bf16 v[140:143], a[152:155], v[72:75], v[140:143]\n\t"
      "ds_read_b64_tr_b16 v[100:101], v117 offset:17408\n\t"
      "ds_read_b64_tr_b16 v[102:103], v117 offset:21504\n\t"
      "v_mfma_f32_16x16x32_bf16 v[128:131], a[156:159], v[28:31], v[128:131]\n\t"
      "v_exp_f32 v178, v178\n\t"
      "v_mfma_f32_16x16x32_bf16 v[132:135], a[156:159], v[44:47], v[132:135]\n\t"
      "ds_read_b64_tr_b16 v[104:105], v116 offset:17920\n\t"
      "ds_read_b64_tr_b16 v[106:107], v116 offset:22016\n\t"
      "v_mfma_f32_16x16x32_bf16 v[136:139], a[156:159], v[60:63], v[136:139]\n\t"
      "v_exp_f32 v179, v179\n\t"
      "v_mfma_f32_16x16x32_bf16 v[140:143], a[156:159], v[76:79], v[140:143]\n\t"
      "ds_read_b64_tr_b16 v[108:109], v117 offset:17920\n\t"
      "ds_read_b64_tr_b16 v[110:111], v117 offset:22016\n\t"
      "v_mfma_f32_16x16x32_bf16 v[144:147], a[160:163], v[16:19], v[224:227]\n\t"
      "v_exp_f32 v164, v164\n\t"
      "v_mfma_f32_16x16x32_bf16 v[148:151], a[160:163], v[32:35], v[228:231]\n\t"
      "v_cvt_pk_bf16_f32 v208, v160, v161\n\t"
      "v_cvt_pk_bf16_f32 v209, v162, v163\n\t"
      "v_mfma_f32_16x16x32_bf16 v[152:155], a[160:163], v[48:51], v[232:235]\n\t"
      "v_exp_f32 v165, v165\n\t"
      "v_mfma_f32_16x16x32_bf16 v[156:159], a[160:163], v[64:67], v[236:239]\n\t"
      "v_cvt_pk_bf16_f32 v210, v176, v177\n\t"
      "v_cvt_pk_bf16_f32 v211, v178, v179\n\t"
      "v_mfma_f32_16x16x32_bf16 v[144:147], a[164:167], v[20:23], v[144:147]\n\t"
      "v_exp_f32 v166, v166\n\t"
      "v_mfma_f32_16x16x32_bf16 v[148:151], a[164:167], v[36:39], v[148:151]\n\t"
      "v_cvt_pk_bf16_f32 v212, v164, v165\n\t"
      "v_mfma_f32_16x16x32_bf16 v[152:155], a[164:167], v[52:55], v[152:155]\n\t"
      "v_exp_f32 v167, v167\n\t"
      "v_mfma_f32_16x16x32_bf16 v[156:159], a[164:167], v[68:71], v[156:159]\n\t"
      "v_cvt_pk_bf16_f32 v213, v166, v167\n\t"
      "v_mfma_f32_16x16x32_bf16 v[144:147], a[168:171], v[24:27], v[144:147]\n\t"
      "v_exp_f32 v180, v180\n\t"
      "v_mfma_f32_16x16x32_bf16 v[148:151], a[168:171], v[40:43], v[148:151]\n\t"
      "v_exp_f32 v181, v181\n\t"
      "v_mfma_f32_16x16x32_bf16 v[152:155], a[168:171], v[56:59], v[152:155]\n\t"
      "v_exp_f32 v182, v182\n\t"
      "v_mfma_f32_16x16x32_bf16 v[156:159], a[168:171], v[72:75], v[156:159]\n\t"
      "v_cvt_pk_bf16_f32 v214, v180, v181\n\t"
      "v_mfma_f32_16x16x32_bf16 v[144:147], a[172:175], v[28:31], v[144:147]\n\t"
      "v_exp_f32 v183, v183\n\t"
      "v_mfma_f32_16x16x32_bf16 v[148:151], a[172:175], v[44:47], v[148:151]\n\t"
      "v_cvt_pk_bf16_f32 v215, v182, v183\n\t"
      "v_mfma_f32_16x16x32_bf16 v[152:155], a[172:175], v[60:63], v[152:155]\n\t"
      "v_exp_f32 v168, v168\n\t"
      "v_mfma_f32_16x16x32_bf16 v[156:159], a[172:175], v[76:79], v[156:159]\n\t"
      "v_exp_f32 v169, v169\n\t"
      "s_waitcnt lgkmcnt(0)\n\t"
      "v_mfma_f32_16x16x32_bf16 a[0:3], v[80:83], v[192:195], a[0:3]\n\t"
      "v_exp_f32 v170, v170\n\t"
      "v_mfma_f32_16x16x32_bf16 a[4:7], v[80:83], v[196:199], a[4:7]\n\t"
      "ds_read_b128 a[144:147], v112 offset:8192\n\t"
      "ds_read_b128 a[148:151], v113 offset:8192\n\t"
      "v_mfma_f32_16x16x32_bf16 a[8:11], v[80:83], v[200:203], a[8:11]\n\t"
      "v_exp_f32 v171, v171\n\t"
      "v_mfma_f32_16x16x32_bf16 a[12:15], v[80:83], v[204:207], a[12:15]\n\t"
      "ds_read_b128 a[152:155], v114 offset:8192\n\t"
      "ds_read_b128 a[156:159], v115 offset:8192\n\t"
      "v_mfma_f32_16x16x32_bf16 a[16:19], v[84:87], v[192:195], a[16:19]\n\t"
      "v_exp_f32 v184, v184\n\t"
      "v_mfma_f32_16x16x32_bf16 a[20:23], v[84:87], v[196:199], a[20:23]\n\t"
      "ds_read_b128 a[160:163], v112 offset:12288\n\t"
      "ds_read_b128 a[164:167], v113 offset:12288\n\t"
      "v_mfma_f32_16x16x32_bf16 a[24:27], v[84:87], v[200:203], a[24:27]\n\t"
      "v_exp_f32 v185, v185\n\t"
      "v_mfma_f32_16x16x32_bf16 a[28:31], v[84:87], v[204:207], a[28:31]\n\t"
      "ds_read_b128 a[168:171], v114 offset:12288\n\t"
      "ds_read_b128 a[172:175], v115 offset:12288\n\t"
      "v_mfma_f32_16x16x32_bf16 a[32:35], v[88:91], v[192:195], a[32:35]\n\t"
      "v_exp_f32 v186, v186\n\t"
      "v_mfma_f32_16x16x32_bf16 a[36:39], v[88:91], v[196:199], a[36:39]\n\t"
      "v_cvt_pk_bf16_f32 v216, v168, v169\n\t"
      "v_cvt_pk_bf16_f32 v217, v170, v171\n\t"
      "v_mfma_f32_16x16x32_bf16 a[40:43], v[88:91], v[200:203], a[40:43]\n\t"
      "v_exp_f32 v187, v187\n\t"
      "v_mfma_f32_16x16x32_bf16 a[44:47], v[88:91], v[204:207], a[44:47]\n\t"
      "v_cvt_pk_bf16_f32 v218, v184, v185\n\t"
      "v_cvt_pk_bf16_f32 v219, v186, v187\n\t"
      "v_mfma_f32_16x16x32_bf16 a[48:51], v[92:95], v[192:195], a[48:51]\n\t"
      "v_exp_f32 v172, v172\n\t"
      "v_mfma_f32_16x16x32_bf16 a[52:55], v[92:95], v[196:199], a[52:55]\n\t"
      "v_exp_f32 v173, v173\n\t"
      "v_mfma_f32_16x16x32_bf16 a[56:59], v[92:95], v[200:203], a[56:59]\n\t"
      "v_exp_f32 v174, v174\n\t"
      "v_mfma_f32_16x16x32_bf16 a[60:63], v[92:95], v[204:207], a[60:63]\n\t"
      "v_cvt_pk_bf16_f32 v220, v172, v173\n\t"
      "v_mfma_f32_16x16x32_bf16 a[64:67], v[96:99], v[192:195], a[64:67]\n\t"
      "v_exp_f32 v175, v175\n\t"
      "v_mfma_f32_16x16x32_bf16 a[68:71], v[96:99], v[196:199], a[68:71]\n\t"
      "v_cvt_pk_bf16_f32 v221, v174, v175\n\t"
      "v_mfma_f32_16x16x32_bf16 a[72:75], v[96:99], v[200:203], a[72:75]\n\t"
      "v_exp_f32 v188, v188\n\t"
      "v_mfma_f32_16x16x32_bf16 a[76:79], v[96:99], v[204:207], a[76:79]\n\t"
      "v_exp_f32 v189, v189\n\t"
      "v_mfma_f32_16x16x32_bf16 a[80:83], v[100:103], v[192:195], a[80:83]\n\t"
      "v_exp_f32 v190, v190\n\t"
      "v_mfma_f32_16x16x32_bf16 a[84:87], v[100:103], v[196:199], a[84:87]\n\t"
      "v_cvt_pk_bf16_f32 v222, v188, v189\n\t"
      "v_mfma_f32_16x16x32_bf16 a[88:91], v[100:103], v[200:203], a[88:91]\n\t"
      "v_exp_f32 v191, v191\n\t"
      "v_mfma_f32_16x16x32_bf16 a[92:95], v[100:103], v[204:207], a[92:95]\n\t"
      "v_cvt_pk_bf16_f32 v223, v190, v191\n\t"
      "v_mfma_f32_16x16x32_bf16 a[96:99], v[104:107], v[192:195], a[96:99]\n\t"
      "v_mfma_f32_16x16x32_bf16 a[100:103], v[104:107], v[196:199], a[100:103]\n\t"
      "v_mfma_f32_16x16x32_bf16 a[104:107], v[104:107], v[200:203], a[104:107]\n\t"
      "v_mfma_f32_16x16x32_bf16 a[108:111], v[104:107], v[204:207], a[108:111]\n\t"
      "v_mfma_f32_16x16x32_bf16 a[112:115], v[108:111], v[192:195], a[112:115]\n\t"
      "v_mfma_f32_16x16x32_bf16 a[116:119], v[108:111], v[196:199], a[116:119]\n\t"
      "v_mfma_f32_16x16x32_bf16 a[120:123], v[108:111], v[200:203], a[120:123]\n\t"
      "v_mfma_f32_16x16x32_bf16 a[124:127], v[108:111], v[204:207], a[124:127]\n\t"
      "v_mfma_f32_16x16x32_bf16 a[128:131], v[120:123], v[192:195], a[128:131]\n\t"
      "v_mfma_f32_16x16x32_bf16 a[132:135], v[120:123], v[196:199], a[132:135]\n\t"
      "v_mfma_f32_16x16x32_bf16 a[136:139], v[120:123], v[200:203], a[136:139]\n\t"
      "v_mfma_f32_16x16x32_bf16 a[140:143], v[120:123], v[204:207], a[140:143]\n\t"
      "s_sub_u32 s45, s45, 1\n\t"
      "s_cmp_lg_u32 s45, 0\n\t"
      "s_cbranch_scc1 K1atb\n\t"
      "s_getpc_b64 s[90:91]\n\t"
      "91:\n\t"
      "s_lshl_b32 s93, s99, 13\n\t"
      "s_add_u32 s93, s93, K1oep-91b\n\t"
      "v_lshl_add_u32 v14, v0, 3, s93\n\t"
      "v_min_u32 v14, K9end-91b-4, v14\n\t"
      "global_load_dword v15, v14, s[90:91]\n\t"
      "s_lshr_b32 s94, %10, 2\n\t"
      "s_lshl_b32 s94, s94, 7\n\t"
      "s_add_u32 s94, s94, 0x205c400\n\t"
      "s_lshr_b32 s96, %11, 1\n\t"
      "s_mul_i32 s96, s96, 0x300\n\t"
      "s_add_u32 s96, s96, 0x205d000\n\t"
      "v_lshrrev_b32 v252, 2, v0\n\t"
      "v_and_b32 v252, 0x7c, v252\n\t"
      "v_lshrrev_b32 v253, 2, v0\n\t"
      "v_min_u32 v253, 0xd4, v253\n\t"
      "buffer_load_dword v254, v252, s[56:59], s94 offen sc1\n\t"
      "buffer_load_dword v255, v253, s[56:59], s96 offen sc1\n\t"
      "s_waitcnt lgkmcnt(0)\n\t"
      "v_mfma_f32_16x16x32_bf16 v[160:163], a[144:147], v[16:19], v[224:227]\n\t"
      "v_exp_f32 v128, v128\n\t"
      "v_mfma_f32_16x16x32_bf16 v[164:167], a[144:147], v[32:35], v[228:231]\n\t"
      "ds_read_b64_tr_b16 v[80:81], v116 offset:24576\n\t"
      "ds_read_b64_tr_b16 v[82:83], v116 offset:28672\n\t"
      "v_mfma_f32_16x16x32_bf16 v[168:171], a[144:147], v[48:51], v[232:235]\n\t"
      "v_exp_f32 v129, v129\n\t"
      "v_mfma_f32_16x16x32_bf16 v[172:175], a[144:147], v[64:67], v[236:239]\n\t"
      "ds_read_b64_tr_b16 v[84:85], v117 offset:24576\n\t"
      "ds_read_b64_tr_b16 v[86:87], v117 offset:28672\n\t"
      "v_mfma_f32_16x16x32_bf16 v[160:163], a[148:151], v[20:23], v[160:163]\n\t"
      "v_exp_f32 v130, v130\n\t"
      "v_mfma_f32_16x16x32_bf16 v[164:167], a[148:151], v[36:39], v[164:167]\n\t"
      "ds_read_b64_tr_b16 v[88:89], v116 offset:25088\n\t"
      "ds_read_b64_tr_b16 v[90:91], v116 offset:29184\n\t"
      "v_mfma_f32_16x16x32_bf16 v[168:171], a[148:151], v[52:55], v[168:171]\n\t"
      "v_exp_f32 v131, v131\n\t"
      "v_mfma_f32_16x16x32_bf16 v[172:175], a[148:151], v[68:71], v[172:175]\n\t"
      "ds_read_b64_tr_b16 v[92:93], v117 offset:25088\n\t"
      "ds_read_b64_tr_b16 v[94:95], v117 offset:29184\n\t"
      "v_mfma_f32_16x16x32_bf16 v[160:163], a[152:155], v[24:27], v[160:163]\n\t"
      "v_exp_f32 v144, v144\n\t"
      "v_mfma_f32_16x16x32_bf16 v[164:167], a[152:155], v[40:43], v[164:167]\n\t"
      "ds_read_b64_tr_b16 v[96:97], v116 offset:25600\n\t"
      "ds_read_b64_tr_b16 v[98:99], v116 offset:29696\n\t"
      "v_mfma_f32_16x16x32_bf16 v[168:171], a[152:155], v[56:59], v[168:171]\n\t"
      "v_exp_f32 v145, v145\n\t"
      "v_mfma_f32_16x16x32_bf16 v[172:175], a[152:155], v[72:75], v[172:175]\n\t"
      "ds_read_b64_tr_b16 v[100:101], v117 offset:25600\n\t"
      "ds_read_b64_tr_b16 v[102:103], v117 offset:29696\n\t"
      "v_mfma_f32_16x16x32_bf16 v[160:163], a[156:159], v[28:31], v[160:163]\n\t"
      "v_exp_f32 v146, v146\n\t"
      "v_mfma_f32_16x16x32_bf16 v[164:167], a[156:159], v[44:47], v[164:167]\n\t"
      "ds_read_b64_tr_b16 v[104:105], v116 offset:26112\n\t"
      "ds_read_b64_tr_b16 v[106:107], v116 offset:30208\n\t"
      "v_mfma_f32_16x16x32_bf16 v[168:171], a[156:159], v[60:63], v[168:171]\n\t"
      "v_exp_f32 v147, v147\n\t"
      "v_mfma_f32_16x16x32_bf16 v[172:175], a[156:159], v[76:79], v[172:175]\n\t"
      "ds_read_b64_tr_b16 v[108:109], v117 offset:26112\n\t"
      "ds_read_b64_tr_b16 v[110:111], v117 offset:30208\n\t"
      "v_mfma_f32_16x16x32_bf16 v[176:179], a[160:163], v[16:19], v[224:227]\n\t"
      "v_exp_f32 v132, v132\n\t"
      "v_mfma_f32_16x16x32_bf16 v[180:183], a[160:163], v[32:35], v[228:231]\n\t"
      "v_cvt_pk_bf16_f32 v192, v128, v129\n\t"
      "v_cvt_pk_bf16_f32 v193, v130, v131\n\t"
      "v_mfma_f32_16x16x32_bf16 v[184:187], a[160:163], v[48:51], v[232:235]\n\t"
      "v_exp_f32 v133, v133\n\t"
      "v_mfma_f32_16x16x32_bf16 v[188:191], a[160:163], v[64:67], v[236:239]\n\t"
      "v_cvt_pk_bf16_f32 v194, v144, v145\n\t"
      "v_cvt_pk_bf16_f32 v195, v146, v147\n\t"
      "v_mfma_f32_16x16x32_bf16 v[176:179], a[164:167], v[20:23], v[176:179]\n\t"
      "v_exp_f32 v134, v134\n\t"
      "v_mfma_f32_16x16x32_bf16 v[180:183], a[164:167], v[36:39], v[180:183]\n\t"
      "v_cvt_pk_bf16_f32 v196, v132, v133\n\t"
      "v_mfma_f32_16x16x32_bf16 v[184:187], a[164:167], v[52:55], v[184:187]\n\t"
      "v_exp_f32 v135, v135\n\t"
      "v_mfma_f32_16x16x32_bf16 v[188:191], a[164:167], v[68:71], v[188:191]\n\t"
      "v_cvt_pk_bf16_f32 v197, v134, v135\n\t"
      "v_mfma_f32_16x16x32_bf16 v[176:179], a[168:171], v[24:27], v[176:179]\n\t"
      "v_exp_f32 v148, v148\n\t"
      "v_mfma_f32_16x16x32_bf16 v[180:183], a[168:171], v[40:43], v[180:183]\n\t"
      "v_exp_f32 v149, v149\n\t"
      "v_mfma_f32_16x16x32_bf16 v[184:187], a[168:171], v[56:59], v[184:187]\n\t"
      "v_exp_f32 v150, v150\n\t"
      "v_mfma_f32_16x16x32_bf16 v[188:191], a[168:171], v[72:75], v[188:191]\n\t"
      "v_cvt_pk_bf16_f32 v198, v148, v149\n\t"
      "v_mfma_f32_16x16x32_bf16 v[176:179], a[172:175], v[28:31], v[176:179]\n\t"
      "v_exp_f32 v151, v151\n\t"
      "v_mfma_f32_16x16x32_bf16 v[180:183], a[172:175], v[44:47], v[180:183]\n\t"
      "v_cvt_pk_bf16_f32 v199, v150, v151\n\t"
      "v_mfma_f32_16x16x32_bf16 v[184:187], a[172:175], v[60:63], v[184:187]\n\t"
      "v_exp_f32 v136, v136\n\t"
      "v_mfma_f32_16x16x32_bf16 v[188:191], a[172:175], v[76:79], v[188:191]\n\t"
      "v_exp_f32 v137, v137\n\t"
      "s_waitcnt lgkmcnt(0)\n\t"
      "v_mfma_f32_16x16x32_bf16 a[0:3], v[80:83], v[208:211], a[0:3]\n\t"
      "v_exp_f32 v138, v138\n\t"
      "v_mfma_f32_16x16x32_bf16 a[4:7], v[80:83], v[212:215], a[4:7]\n\t"
      "v_add_u32 v116, s49, v116\n\t"
      "v_add_u32 v117, s49, v117\n\t"
      "v_mfma_f32_16x16x32_bf16 a[8:11], v[80:83], v[216:219], a[8:11]\n\t"
      "v_exp_f32 v139, v139\n\t"
      "v_mfma_f32_16x16x32_bf16 a[12:15], v[80:83], v[220:223], a[12:15]\n\t"
      "v_cvt_pk_bf16_f32 v200, v136, v137\n\t"
      "v_cvt_pk_bf16_f32 v201, v138, v139\n\t"
      "v_mfma_f32_16x16x32_bf16 a[16:19], v[84:87], v[208:211], a[16:19]\n\t"
      "v_exp_f32 v152, v152\n\t"
      "v_mfma_f32_16x16x32_bf16 a[20:23], v[84:87], v[212:215], a[20:23]\n\t"
      "ds_read_b64_tr_b16 v[80:81], v116 offset:16384\n\t"
      "ds_read_b64_tr_b16 v[82:83], v116 offset:20480\n\t"
      "v_mfma_f32_16x16x32_bf16 a[24:27], v[84:87], v[216:219], a[24:27]\n\t"
      "v_exp_f32 v153, v153\n\t"
      "v_mfma_f32_16x16x32_bf16 a[28:31], v[84:87], v[220:223], a[28:31]\n\t"
      "v_cvt_pk_bf16_f32 v202, v152, v153\n\t"
      "v_mfma_f32_16x16x32_bf16 a[32:35], v[88:91], v[208:211], a[32:35]\n\t"
      "v_exp_f32 v154, v154\n\t"
      "v_mfma_f32_16x16x32_bf16 a[36:39], v[88:91], v[212:215], a[36:39]\n\t"
      "ds_read_b64_tr_b16 v[84:85], v117 offset:16384\n\t"
      "ds_read_b64_tr_b16 v[86:87], v117 offset:20480\n\t"
      "v_mfma_f32_16x16x32_bf16 a[40:43], v[88:91], v[216:219], a[40:43]\n\t"
      "v_exp_f32 v155, v155\n\t"
      "v_mfma_f32_16x16x32_bf16 a[44:47], v[88:91], v[220:223], a[44:47]\n\t"
      "v_cvt_pk_bf16_f32 v203, v154, v155\n\t"
      "v_mfma_f32_16x16x32_bf16 a[48:51], v[92:95], v[208:211], a[48:51]\n\t"
      "v_exp_f32 v140, v140\n\t"
      "v_mfma_f32_16x16x32_bf16 a[52:55], v[92:95], v[212:215], a[52:55]\n\t"
      "ds_read_b64_tr_b16 v[88:89], v116 offset:16896\n\t"
      "ds_read_b64_tr_b16 v[90:91], v116 offset:20992\n\t"
      "v_mfma_f32_16x16x32_bf16 a[56:59], v[92:95], v[216:219], a[56:59]\n\t"
      "v_exp_f32 v141, v141\n\t"
      "v_mfma_f32_16x16x32_bf16 a[60:63], v[92:95], v[220:223], a[60:63]\n\t"
      "v_cvt_pk_bf16_f32 v204, v140, v141\n\t"
      "v_mfma_f32_16x16x32_bf16 a[64:67], v[96:99], v[208:211], a[64:67]\n\t"
      "v_exp_f32 v142, v142\n\t"
      "v_mfma_f32_16x16x32_bf16 a[68:71], v[96:99], v[212:215], a[68:71]\n\t"
      "ds_read_b64_tr_b16 v[92:93], v117 offset:16896\n\t"
      "ds_read_b64_tr_b16 v[94:95], v117 offset:20992\n\t"
      "v_mfma_f32_16x16x32_bf16 a[72:75], v[96:99], v[216:219], a[72:75]\n\t"
      "v_exp_f32 v143, v143\n\t"
      "v_mfma_f32_16x16x32_bf16 a[76:79], v[96:99], v[220:223], a[76:79]\n\t"
      "v_cvt_pk_bf16_f32 v205, v142, v143\n\t"
      "v_mfma_f32_16x16x32_bf16 a[80:83], v[100:103], v[208:211], a[80:83]\n\t"
      "v_exp_f32 v156, v156\n\t"
      "v_mfma_f32_16x16x32_bf16 a[84:87], v[100:103], v[212:215], a[84:87]\n\t"
      "ds_read_b64_tr_b16 v[96:97], v116 offset:17408\n\t"
      "ds_read_b64_tr_b16 v[98:99], v116 offset:21504\n\t"
      "v_mfma_f32_16x16x32_bf16 a[88:91], v[100:103], v[216:219], a[88:91]\n\t"
      "v_exp_f32 v157, v157\n\t"
      "v_mfma_f32_16x16x32_bf16 a[92:95], v[100:103], v[220:223], a[92:95]\n\t"
      "v_cvt_pk_bf16_f32 v206, v156, v157\n\t"
      "v_mfma_f32_16x16x32_bf16 a[96:99], v[104:107], v[208:211], a[96:99]\n\t"
      "v_exp_f32 v158, v158\n\t"
      "v_mfma_f32_16x16x32_bf16 a[100:103], v[104:107], v[212:215], a[100:103]\n\t"
      "ds_read_b64_tr_b16 v[100:101], v117 offset:17408\n\t"
      "ds_read_b64_tr_b16 v[102:103], v117 offset:21504\n\t"
      "v_mfma_f32_16x16x32_bf16 a[104:107], v[104:107], v[216:219], a[104:107]\n\t"
      "v_exp_f32 v159, v159\n\t"
      "v_mfma_f32_16x16x32_bf16 a[108:111], v[104:107], v[220:223], a[108:111]\n\t"
      "v_cvt_pk_bf16_f32 v207, v158, v159\n\t"
      "v_mfma_f32_16x16x32_bf16 a[112:115], v[108:111], v[208:211], a[112:115]\n\t"
      "ds_read_b64_tr_b16 v[104:105], v116 offset:17920\n\t"
      "ds_read_b64_tr_b16 v[106:107], v116 offset:22016\n\t"
      "v_mfma_f32_16x16x32_bf16 a[116:119], v[108:111], v[212:215], a[116:119]\n\t"
      "v_mfma_f32_16x16x32_bf16 a[120:123], v[108:111], v[216:219], a[120:123]\n\t"
      "v_mfma_f32_16x16x32_bf16 a[124:127], v[108:111], v[220:223], a[124:127]\n\t"
      "v_mfma_f32_16x16x32_bf16 a[128:131], v[120:123], v[208:211], a[128:131]\n\t"
      "ds_read_b64_tr_b16 v[108:109], v117 offset:17920\n\t"
      "ds_read_b64_tr_b16 v[110:111], v117 offset:22016\n\t"
      "v_mfma_f32_16x16x32_bf16 a[132:135], v[120:123], v[212:215], a[132:135]\n\t"
      "v_mfma_f32_16x16x32_bf16 a[136:139], v[120:123], v[216:219], a[136:139]\n\t"
      "v_mfma_f32_16x16x32_bf16 a[140:143], v[120:123], v[220:223], a[140:143]\n\t"
      "s_waitcnt lgkmcnt(0)\n\t"
      "v_mfma_f32_16x16x32_bf16 a[0:3], v[80:83], v[192:195], a[0:3]\n\t"
      "v_exp_f32 v160, v160\n\t"
      "v_mfma_f32_16x16x32_bf16 a[4:7], v[80:83], v[196:199], a[4:7]\n\t"
      "v_exp_f32 v161, v161\n\t"
      "v_mfma_f32_16x16x32_bf16 a[8:11], v[80:83], v[200:203], a[8:11]\n\t"
      "v_exp_f32 v162, v162\n\t"
      "v_mfma_f32_16x16x32_bf16 a[12:15], v[80:83], v[204:207], a[12:15]\n\t"
      "v_exp_f32 v163, v163\n\t"
      "v_mfma_f32_16x16x32_bf16 a[16:19], v[84:87], v[192:195], a[16:19]\n\t"
      "v_exp_f32 v176, v176\n\t"
      "v_mfma_f32_16x16x32_bf16 a[20:23], v[84:87], v[196:199], a[20:23]\n\t"
      "v_exp_f32 v177, v177\n\t"
      "v_mfma_f32_16x16x32_bf16 a[24:27], v[84:87], v[200:203], a[24:27]\n\t"
      "v_exp_f32 v178, v178\n\t"
      "v_mfma_f32_16x16x32_bf16 a[28:31], v[84:87], v[204:207], a[28:31]\n\t"
      "v_exp_f32 v179, v179\n\t"
      "v_mfma_f32_16x16x32_bf16 a[32:35], v[88:91], v[192:195], a[32:35]\n\t"
      "v_exp_f32 v164, v164\n\t"
      "v_mfma_f32_16x16x32_bf16 a[36:39], v[88:91], v[196:199], a[36:39]\n\t"
      "v_exp_f32 v165, v165\n\t"
      "v_mfma_f32_16x16x32_bf16 a[40:43], v[88:91], v[200:203], a[40:43]\n\t"
      "v_exp_f32 v166, v166\n\t"
      "v_mfma_f32_16x16x32_bf16 a[44:47], v[88:91], v[204:207], a[44:47]\n\t"
      "v_exp_f32 v167, v167\n\t"
      "v_mfma_f32_16x16x32_bf16 a[48:51], v[92:95], v[192:195], a[48:51]\n\t"
      "v_exp_f32 v180, v180\n\t"
      "v_mfma_f32_16x16x32_bf16 a[52:55], v[92:95], v[196:199], a[52:55]\n\t"
      "v_exp_f32 v181, v181\n\t"
      "v_mfma_f32_16x16x32_bf16 a[56:59], v[92:95], v[200:203], a[56:59]\n\t"
      "v_exp_f32 v182, v182\n\t"
      "v_mfma_f32_16x16x32_bf16 a[60:63], v[92:95], v[204:207], a[60:63]\n\t"
      "v_exp_f32 v183, v183\n\t"
      "v_mfma_f32_16x16x32_bf16 a[64:67], v[96:99], v[192:195], a[64:67]\n\t"
      "v_exp_f32 v168, v168\n\t"
      "v_mfma_f32_16x16x32_bf16 a[68:71], v[96:99], v[196:199], a[68:71]\n\t"
      "v_exp_f32 v169, v169\n\t"
      "v_mfma_f32_16x16x32_bf16 a[72:75], v[96:99], v[200:203], a[72:75]\n\t"
      "v_exp_f32 v170, v170\n\t"
      "v_mfma_f32_16x16x32_bf16 a[76:79], v[96:99], v[204:207], a[76:79]\n\t"
      "v_exp_f32 v171, v171\n\t"
      "v_mfma_f32_16x16x32_bf16 a[80:83], v[100:103], v[192:195], a[80:83]\n\t"
      "v_exp_f32 v184, v184\n\t"
      "v_mfma_f32_16x16x32_bf16 a[84:87], v[100:103], v[196:199], a[84:87]\n\t"
      "v_exp_f32 v185, v185\n\t"
      "v_mfma_f32_16x16x32_bf16 a[88:91], v[100:103], v[200:203], a[88:91]\n\t"
      "v_exp_f32 v186, v186\n\t"
      "v_mfma_f32_16x16x32_bf16 a[92:95], v[100:103], v[204:207], a[92:95]\n\t"
      "v_exp_f32 v187, v187\n\t"
      "v_mfma_f32_16x16x32_bf16 a[96:99], v[104:107], v[192:195], a[96:99]\n\t"
      "v_exp_f32 v172, v172\n\t"
      "v_mfma_f32_16x16x32_bf16 a[100:103], v[104:107], v[196:199], a[100:103]\n\t"
      "v_exp_f32 v173, v173\n\t"
      "v_mfma_f32_16x16x32_bf16 a[104:107], v[104:107], v[200:203], a[104:107]\n\t"
      "v_exp_f32 v174, v174\n\t"
      "v_mfma_f32_16x16x32_bf16 a[108:111], v[104:107], v[204:207], a[108:111]\n\t"
      "v_exp_f32 v175, v175\n\t"
      "v_mfma_f32_16x16x32_bf16 a[112:115], v[108:111], v[192:195], a[112:115]\n\t"
      "v_exp_f32 v188, v188\n\t"
      "v_mfma_f32_16x16x32_bf16 a[116:119], v[108:111], v[196:199], a[116:119]\n\t"
      "v_exp_f32 v189, v189\n\t"
      "v_mfma_f32_16x16x32_bf16 a[120:123], v[108:111], v[200:203], a[120:123]\n\t"
      "v_exp_f32 v190, v190\n\t"
      "v_mfma_f32_16x16x32_bf16 a[124:127], v[108:111], v[204:207], a[124:127]\n\t"
      "v_exp_f32 v191, v191\n\t"
      "v_mfma_f32_16x16x32_bf16 a[128:131], v[120:123], v[192:195], a[128:131]\n\t"
      "ds_read_b64_tr_b16 v[80:81], v116 offset:24576\n\t"
      "ds_read_b64_tr_b16 v[82:83], v116 offset:28672\n\t"
      "v_mfma_f32_16x16x32_bf16 a[132:135], v[120:123], v[196:199], a[132:135]\n\t"
      "ds_read_b64_tr_b16 v[84:85], v117 offset:24576\n\t"
      "ds_read_b64_tr_b16 v[86:87], v117 offset:28672\n\t"
      "v_mfma_f32_16x16x32_bf16 a[136:139], v[120:123], v[200:203], a[136:139]\n\t"
      "ds_read_b64_tr_b16 v[88:89], v116 offset:25088\n\t"
      "ds_read_b64_tr_b16 v[90:91], v116 offset:29184\n\t"
      "v_mfma_f32_16x16x32_bf16 a[140:143], v[120:123], v[204:207], a[140:143]\n\t"
      "ds_read_b64_tr_b16 v[92:93], v117 offset:25088\n\t"
      "ds_read_b64_tr_b16 v[94:95], v117 offset:29184\n\t"
      "ds_read_b64_tr_b16 v[96:97], v116 offset:25600\n\t"
      "ds_read_b64_tr_b16 v[98:99], v116 offset:29696\n\t"
      "ds_read_b64_tr_b16 v[100:101], v117 offset:25600\n\t"
      "ds_read_b64_tr_b16 v[102:103], v117 offset:29696\n\t"
      "ds_read_b64_tr_b16 v[104:105], v116 offset:26112\n\t"
      "ds_read_b64_tr_b16 v[106:107], v116 offset:30208\n\t"
      "ds_read_b64_tr_b16 v[108:109], v117 offset:26112\n\t"
      "ds_read_b64_tr_b16 v[110:111], v117 offset:30208\n\t"
      "v_cvt_pk_bf16_f32 v208, v160, v161\n\t"
      "v_cvt_pk_bf16_f32 v209, v162, v163\n\t"
      "v_cvt_pk_bf16_f32 v210, v176, v177\n\t"
      "v_cvt_pk_bf16_f32 v211, v178, v179\n\t"
      "v_cvt_pk_bf16_f32 v212, v164, v165\n\t"
      "v_cvt_pk_bf16_f32 v213, v166, v167\n\t"
      "v_cvt_pk_bf16_f32 v214, v180, v181\n\t"
      "v_cvt_pk_bf16_f32 v215, v182, v183\n\t"
      "v_cvt_pk_bf16_f32 v216, v168, v169\n\t"
      "v_cvt_pk_bf16_f32 v217, v170, v171\n\t"
      "v_cvt_pk_bf16_f32 v218, v184, v185\n\t"
      "v_cvt_pk_bf16_f32 v219, v186, v187\n\t"
      "v_cvt_pk_bf16_f32 v220, v172, v173\n\t"
      "v_cvt_pk_bf16_f32 v221, v174, v175\n\t"
      "v_cvt_pk_bf16_f32 v222, v188, v189\n\t"
      "v_cvt_pk_bf16_f32 v223, v190, v191\n\t"
      "s_waitcnt lgkmcnt(0)\n\t"
      "v_mfma_f32_16x16x32_bf16 a[128:131], v[120:123], v[208:211], a[128:131]\n\t"
      "v_mfma_f32_16x16x32_bf16 a[132:135], v[120:123], v[212:215], a[132:135]\n\t"
      "v_mfma_f32_16x16x32_bf16 a[136:139], v[120:123], v[216:219], a[136:139]\n\t"
      "v_mfma_f32_16x16x32_bf16 a[140:143], v[120:123], v[220:223], a[140:143]\n\t"
      "v_mfma_f32_16x16x32_bf16 a[0:3], v[80:83], v[208:211], a[0:3]\n\t"
      "v_mfma_f32_16x16x32_bf16 a[4:7], v[80:83], v[212:215], a[4:7]\n\t"
      "v_mfma_f32_16x16x32_bf16 a[8:11], v[80:83], v[216:219], a[8:11]\n\t"
      "v_mfma_f32_16x16x32_bf16 a[12:15], v[80:83], v[220:223], a[12:15]\n\t"
      "v_mfma_f32_16x16x32_bf16 a[16:19], v[84:87], v[208:211], a[16:19]\n\t"
      "v_mfma_f32_16x16x32_bf16 a[20:23], v[84:87], v[212:215], a[20:23]\n\t"
      "v_mfma_f32_16x16x32_bf16 a[24:27], v[84:87], v[216:219], a[24:27]\n\t"
      "v_mfma_f32_16x16x32_bf16 a[28:31], v[84:87], v[220:223], a[28:31]\n\t"
      "v_mfma_f32_16x16x32_bf16 a[32:35], v[88:91], v[208:211], a[32:35]\n\t"
      "v_mfma_f32_16x16x32_bf16 a[36:39], v[88:91], v[212:215], a[36:39]\n\t"
      "v_mfma_f32_16x16x32_bf16 a[40:43], v[88:91], v[216:219], a[40:43]\n\t"
      "v_mfma_f32_16x16x32_bf16 a[44:47], v[88:91], v[220:223], a[44:47]\n\t"
      "v_mfma_f32_16x16x32_bf16 a[48:51], v[92:95], v[208:211], a[48:51]\n\t"
      "v_mfma_f32_16x16x32_bf16 a[52:55], v[92:95], v[212:215], a[52:55]\n\t"
      "v_mfma_f32_16x16x32_bf16 a[56:59], v[92:95], v[216:219], a[56:59]\n\t"
      "v_mfma_f32_16x16x32_bf16 a[60:63], v[92:95], v[220:223], a[60:63]\n\t"
      "v_mfma_f32_16x16x32_bf16 a[64:67], v[96:99], v[208:211], a[64:67]\n\t"
      "v_mfma_f32_16x16x32_bf16 a[68:71], v[96:99], v[212:215], a[68:71]\n\t"
      "v_mfma_f32_16x16x32_bf16 a[72:75], v[96:99], v[216:219], a[72:75]\n\t"
      "v_mfma_f32_16x16x32_bf16 a[76:79], v[96:99], v[220:223], a[76:79]\n\t"
      "v_mfma_f32_16x16x32_bf16 a[80:83], v[100:103], v[208:211], a[80:83]\n\t"
      "v_mfma_f32_16x16x32_bf16 a[84:87], v[100:103], v[212:215], a[84:87]\n\t"
      "v_mfma_f32_16x16x32_bf16 a[88:91], v[100:103], v[216:219], a[88:91]\n\t"
      "v_mfma_f32_16x16x32_bf16 a[92:95], v[100:103], v[220:223], a[92:95]\n\t"
      "v_mfma_f32_16x16x32_bf16 a[96:99], v[104:107], v[208:211], a[96:99]\n\t"
      "v_mfma_f32_16x16x32_bf16 a[100:103], v[104:107], v[212:215], a[100:103]\n\t"
      "v_mfma_f32_16x16x32_bf16 a[104:107], v[104:107], v[216:219], a[104:107]\n\t"
      "v_mfma_f32_16x16x32_bf16 a[108:111], v[104:107], v[220:223], a[108:111]\n\t"
      "v_mfma_f32_16x16x32_bf16 a[112:115], v[108:111], v[208:211], a[112:115]\n\t"
      "v_mfma_f32_16x16x32_bf16 a[116:119], v[108:111], v[212:215], a[116:119]\n\t"
      "v_mfma_f32_16x16x32_bf16 a[120:123], v[108:111], v[216:219], a[120:123]\n\t"
      "v_mfma_f32_16x16x32_bf16 a[124:127], v[108:111], v[220:223], a[124:127]\n\t"
      "K1oep:\n\t"
      "s_nop 7\n\t"
      "s_nop 7\n\t"
      "v_accvgpr_read_b32 v240, a128\n\t"
      "v_accvgpr_read_b32 v241, a132\n\t"
      "v_accvgpr_read_b32 v242, a136\n\t"
      "v_accvgpr_read_b32 v243, a140\n\t"
      "v_max3_f32 v128, v240, v241, v242\n\t"
      "v_max_f32 v128, v128, v243\n\t"
      "v_add_f32 v129, v240, v240\n\t"
      "v_add_f32 v130, v241, v241\n\t"
      "v_add_f32 v131, v242, v242\n\t"
      "v_add_f32 v132, v243, v243\n\t"
      "v_cmp_ngt_f32 vcc, s48, v128\n\t"
      "v_cmp_u_f32 s[90:91], v240, v240\n\t"
      "s_nop 4\n\t"
      "s_or_b64 vcc, vcc, s[90:91]\n\t"
      "v_cmp_u_f32 s[90:91], v241, v241\n\t"
      "s_nop 4\n\t"
      "s_or_b64 vcc, vcc, s[90:91]\n\t"
      "v_cmp_u_f32 s[90:91], v242, v242\n\t"
      "s_nop 4\n\t"
      "s_or_b64 vcc, vcc, s[90:91]\n\t"
      "v_cmp_u_f32 s[90:91], v243, v243\n\t"
      "s_nop 4\n\t"
      "s_or_b64 vcc, vcc, s[90:91]\n\t"
      "s_cmp_lg_u64 vcc, 0\n\t"
      "s_cselect_b32 %0, 1, 0\n\t"
      "v_rcp_f32 v244, v240\n\t"
      "v_rcp_f32 v246, v241\n\t"
      "v_rcp_f32 v248, v242\n\t"
      "v_rcp_f32 v250, v243\n\t"
      "s_nop 0\n\t"
      "v_mov_b32 v245, v244\n\t"
      "v_mov_b32 v247, v246\n\t"
      "v_mov_b32 v249, v248\n\t"
      "v_mov_b32 v251, v250\n\t"
      "s_add_u32 s93, s54, 0\n\t"
      "v_accvgpr_read_b32 v128, a0\n\t"
      "v_accvgpr_read_b32 v129, a1\n\t"
      "v_accvgpr_read_b32 v130, a2\n\t"
      "v_accvgpr_read_b32 v131, a3\n\t"
      "v_accvgpr_read_b32 v132, a16\n\t"
      "v_accvgpr_read_b32 v133, a17\n\t"
      "v_accvgpr_read_b32 v134, a18\n\t"
      "v_accvgpr_read_b32 v135, a19\n\t"
      "v_pk_mul_f32 v[128:129], v[128:129], v[244:245]\n\t"
      "v_pk_mul_f32 v[130:131], v[130:131], v[244:245]\n\t"
      "v_pk_mul_f32 v[132:133], v[132:133], v[244:245]\n\t"
      "v_pk_mul_f32 v[134:135], v[134:135], v[244:245]\n\t"
      "v_cvt_pk_bf16_f32 v136, v128, v129\n\t"
      "v_cvt_pk_bf16_f32 v137, v130, v131\n\t"
      "v_cvt_pk_bf16_f32 v138, v132, v133\n\t"
      "v_cvt_pk_bf16_f32 v139, v134, v135\n\t"
      "buffer_store_dwordx4 v[136:139], v0, s[56:59], s93 offen sc1\n\t"
      "v_accvgpr_read_b32 v144, a4\n\t"
      "v_accvgpr_read_b32 v145, a5\n\t"
      "v_accvgpr_read_b32 v146, a6\n\t"
      "v_accvgpr_read_b32 v147, a7\n\t"
      "v_accvgpr_read_b32 v148, a20\n\t"
      "v_accvgpr_read_b32 v149, a21\n\t"
      "v_accvgpr_read_b32 v150, a22\n\t"
      "v_accvgpr_read_b32 v151, a23\n\t"
      "v_pk_mul_f32 v[144:145], v[144:145], v[246:247]\n\t"
      "v_pk_mul_f32 v[146:147], v[146:147], v[246:247]\n\t"
      "v_pk_mul_f32 v[148:149], v[148:149], v[246:247]\n\t"
      "v_pk_mul_f32 v[150:151], v[150:151], v[246:247]\n\t"
      "v_cvt_pk_bf16_f32 v152, v144, v145\n\t"
      "v_cvt_pk_bf16_f32 v153, v146, v147\n\t"
      "v_cvt_pk_bf16_f32 v154, v148, v149\n\t"
      "v_cvt_pk_bf16_f32 v155, v150, v151\n\t"
      "buffer_store_dwordx4 v[152:155], v0, s[56:59], s93 offen offset:1024 sc1\n\t"
      "v_accvgpr_read_b32 v128, a8\n\t"
      "v_accvgpr_read_b32 v129, a9\n\t"
      "v_accvgpr_read_b32 v130, a10\n\t"
      "v_accvgpr_read_b32 v131, a11\n\t"
      "v_accvgpr_read_b32 v132, a24\n\t"
      "v_accvgpr_read_b32 v133, a25\n\t"
      "v_accvgpr_read_b32 v134, a26\n\t"
      "v_accvgpr_read_b32 v135, a27\n\t"
      "v_pk_mul_f32 v[128:129], v[128:129], v[248:249]\n\t"
      "v_pk_mul_f32 v[130:131], v[130:131], v[248:249]\n\t"
      "v_pk_mul_f32 v[132:133], v[132:133], v[248:249]\n\t"
      "v_pk_mul_f32 v[134:135], v[134:135], v[248:249]\n\t"
      "v_cvt_pk_bf16_f32 v136, v128, v129\n\t"
      "v_cvt_pk_bf16_f32 v137, v130, v131\n\t"
      "v_cvt_pk_bf16_f32 v138, v132, v133\n\t"
      "v_cvt_pk_bf16_f32 v139, v134, v135\n\t"
      "buffer_store_dwordx4 v[136:139], v0, s[56:59], s93 offen offset:2048 sc1\n\t"
      "v_accvgpr_read_b32 v144, a12\n\t"
      "v_accvgpr_read_b32 v145, a13\n\t"
      "v_accvgpr_read_b32 v146, a14\n\t"
      "v_accvgpr_read_b32 v147, a15\n\t"
      "v_accvgpr_read_b32 v148, a28\n\t"
      "v_accvgpr_read_b32 v149, a29\n\t"
      "v_accvgpr_read_b32 v150, a30\n\t"
      "v_accvgpr_read_b32 v151, a31\n\t"
      "v_pk_mul_f32 v[144:145], v[144:145], v[250:251]\n\t"
      "v_pk_mul_f32 v[146:147], v[146:147], v[250:251]\n\t"
      "v_pk_mul_f32 v[148:149], v[148:149], v[250:251]\n\t"
      "v_pk_mul_f32 v[150:151], v[150:151], v[250:251]\n\t"
      "v_cvt_pk_bf16_f32 v152, v144, v145\n\t"
      "v_cvt_pk_bf16_f32 v153, v146, v147\n\t"
      "v_cvt_pk_bf16_f32 v154, v148, v149\n\t"
      "v_cvt_pk_bf16_f32 v155, v150, v151\n\t"
      "buffer_store_dwordx4 v[152:155], v0, s[56:59], s93 offen offset:3072 sc1\n\t"
      "s_add_u32 s93, s54, 0x1000\n\t"
      "v_accvgpr_read_b32 v160, a32\n\t"
      "v_accvgpr_read_b32 v161, a33\n\t"
      "v_accvgpr_read_b32 v162, a34\n\t"
      "v_accvgpr_read_b32 v163, a35\n\t"
      "v_accvgpr_read_b32 v164, a48\n\t"
      "v_accvgpr_read_b32 v165, a49\n\t"
      "v_accvgpr_read_b32 v166, a50\n\t"
      "v_accvgpr_read_b32 v167, a51\n\t"
      "v_pk_mul_f32 v[160:161], v[160:161], v[244:245]\n\t"
      "v_pk_mul_f32 v[162:163], v[162:163], v[244:245]\n\t"
      "v_pk_mul_f32 v[164:165], v[164:165], v[244:245]\n\t"
      "v_pk_mul_f32 v[166:167], v[166:167], v[244:245]\n\t"
      "v_cvt_pk_bf16_f32 v168, v160, v161\n\t"
      "v_cvt_pk_bf16_f32 v169, v162, v163\n\t"
      "v_cvt_pk_bf16_f32 v170, v164, v165\n\t"
      "v_cvt_pk_bf16_f32 v171, v166, v167\n\t"
      "buffer_store_dwordx4 v[168:171], v0, s[56:59], s93 offen sc1\n\t"
      "v_accvgpr_read_b32 v176, a36\n\t"
      "v_accvgpr_read_b32 v177, a37\n\t"
      "v_accvgpr_read_b32 v178, a38\n\t"
      "v_accvgpr_read_b32 v179, a39\n\t"
      "v_accvgpr_read_b32 v180, a52\n\t"
      "v_accvgpr_read_b32 v181, a53\n\t"
      "v_accvgpr_read_b32 v182, a54\n\t"
      "v_accvgpr_read_b32 v183, a55\n\t"
      "v_pk_mul_f32 v[176:177], v[176:177], v[246:247]\n\t"
      "v_pk_mul_f32 v[178:179], v[178:179], v[246:247]\n\t"
      "v_pk_mul_f32 v[180:181], v[180:181], v[246:247]\n\t"
      "v_pk_mul_f32 v[182:183], v[182:183], v[246:247]\n\t"
      "v_cvt_pk_bf16_f32 v184, v176, v177\n\t"
      "v_cvt_pk_bf16_f32 v185, v178, v179\n\t"
      "v_cvt_pk_bf16_f32 v186, v180, v181\n\t"
      "v_cvt_pk_bf16_f32 v187, v182, v183\n\t"
      "buffer_store_dwordx4 v[184:187], v0, s[56:59], s93 offen offset:1024 sc1\n\t"
      "v_accvgpr_read_b32 v160, a40\n\t"
      "v_accvgpr_read_b32 v161, a41\n\t"
      "v_accvgpr_read_b32 v162, a42\n\t"
      "v_accvgpr_read_b32 v163, a43\n\t"
      "v_accvgpr_read_b32 v164, a56\n\t"
      "v_accvgpr_read_b32 v165, a57\n\t"
      "v_accvgpr_read_b32 v166, a58\n\t"
      "v_accvgpr_read_b32 v167, a59\n\t"
      "v_pk_mul_f32 v[160:161], v[160:161], v[248:249]\n\t"
      "v_pk_mul_f32 v[162:163], v[162:163], v[248:249]\n\t"
      "v_pk_mul_f32 v[164:165], v[164:165], v[248:249]\n\t"
      "v_pk_mul_f32 v[166:167], v[166:167], v[248:249]\n\t"
      "v_cvt_pk_bf16_f32 v168, v160, v161\n\t"
      "v_cvt_pk_bf16_f32 v169, v162, v163\n\t"
      "v_cvt_pk_bf16_f32 v170, v164, v165\n\t"
      "v_cvt_pk_bf16_f32 v171, v166, v167\n\t"
      "buffer_store_dwordx4 v[168:171], v0, s[56:59], s93 offen offset:2048 sc1\n\t"
      "v_accvgpr_read_b32 v176, a44\n\t"
      "v_accvgpr_read_b32 v177, a45\n\t"
      "v_accvgpr_read_b32 v178, a46\n\t"
      "v_accvgpr_read_b32 v179, a47\n\t"
      "v_accvgpr_read_b32 v180, a60\n\t"
      "v_accvgpr_read_b32 v181, a61\n\t"
      "v_accvgpr_read_b32 v182, a62\n\t"
      "v_accvgpr_read_b32 v183, a63\n\t"
      "v_pk_mul_f32 v[176:177], v[176:177], v[250:251]\n\t"
      "v_pk_mul_f32 v[178:179], v[178:179], v[250:251]\n\t"
      "v_pk_mul_f32 v[180:181], v[180:181], v[250:251]\n\t"
      "v_pk_mul_f32 v[182:183], v[182:183], v[250:251]\n\t"
      "v_cvt_pk_bf16_f32 v184, v176, v177\n\t"
      "v_cvt_pk_bf16_f32 v185, v178, v179\n\t"
      "v_cvt_pk_bf16_f32 v186, v180, v181\n\t"
      "v_cvt_pk_bf16_f32 v187, v182, v183\n\t"
      "buffer_store_dwordx4 v[184:187], v0, s[56:59], s93 offen offset:3072 sc1\n\t"
      "s_add_u32 s93, s54, 0x2000\n\t"
      "v_accvgpr_read_b32 v128, a64\n\t"
      "v_accvgpr_read_b32 v129, a65\n\t"
      "v_accvgpr_read_b32 v130, a66\n\t"
      "v_accvgpr_read_b32 v131, a67\n\t"
      "v_accvgpr_read_b32 v132, a80\n\t"
      "v_accvgpr_read_b32 v133, a81\n\t"
      "v_accvgpr_read_b32 v134, a82\n\t"
      "v_accvgpr_read_b32 v135, a83\n\t"
      "v_pk_mul_f32 v[128:129], v[128:129], v[244:245]\n\t"
      "v_pk_mul_f32 v[130:131], v[130:131], v[244:245]\n\t"
      "v_pk_mul_f32 v[132:133], v[132:133], v[244:245]\n\t"
      "v_pk_mul_f32 v[134:135], v[134:135], v[244:245]\n\t"
      "v_cvt_pk_bf16_f32 v136, v128, v129\n\t"
      "v_cvt_pk_bf16_f32 v137, v130, v131\n\t"
      "v_cvt_pk_bf16_f32 v138, v132, v133\n\t"
      "v_cvt_pk_bf16_f32 v139, v134, v135\n\t"
      "buffer_store_dwordx4 v[136:139], v0, s[56:59], s93 offen sc1\n\t"
      "v_accvgpr_read_b32 v144, a68\n\t"
      "v_accvgpr_read_b32 v145, a69\n\t"
      "v_accvgpr_read_b32 v146, a70\n\t"
      "v_accvgpr_read_b32 v147, a71\n\t"
      "v_accvgpr_read_b32 v148, a84\n\t"
      "v_accvgpr_read_b32 v149, a85\n\t"
      "v_accvgpr_read_b32 v150, a86\n\t"
      "v_accvgpr_read_b32 v151, a87\n\t"
      "v_pk_mul_f32 v[144:145], v[144:145], v[246:247]\n\t"
      "v_pk_mul_f32 v[146:147], v[146:147], v[246:247]\n\t"
      "v_pk_mul_f32 v[148:149], v[148:149], v[246:247]\n\t"
      "v_pk_mul_f32 v[150:151], v[150:151], v[246:247]\n\t"
      "v_cvt_pk_bf16_f32 v152, v144, v145\n\t"
      "v_cvt_pk_bf16_f32 v153, v146, v147\n\t"
      "v_cvt_pk_bf16_f32 v154, v148, v149\n\t"
      "v_cvt_pk_bf16_f32 v155, v150, v151\n\t"
      "buffer_store_dwordx4 v[152:155], v0, s[56:59], s93 offen offset:1024 sc1\n\t"
      "v_accvgpr_read_b32 v128, a72\n\t"
      "v_accvgpr_read_b32 v129, a73\n\t"
      "v_accvgpr_read_b32 v130, a74\n\t"
      "v_accvgpr_read_b32 v131, a75\n\t"
      "v_accvgpr_read_b32 v132, a88\n\t"
      "v_accvgpr_read_b32 v133, a89\n\t"
      "v_accvgpr_read_b32 v134, a90\n\t"
      "v_accvgpr_read_b32 v135, a91\n\t"
      "v_pk_mul_f32 v[128:129], v[128:129], v[248:249]\n\t"
      "v_pk_mul_f32 v[130:131], v[130:131], v[248:249]\n\t"
      "v_pk_mul_f32 v[132:133], v[132:133], v[248:249]\n\t"
      "v_pk_mul_f32 v[134:135], v[134:135], v[248:249]\n\t"
      "v_cvt_pk_bf16_f32 v136, v128, v129\n\t"
      "v_cvt_pk_bf16_f32 v137, v130, v131\n\t"
      "v_cvt_pk_bf16_f32 v138, v132, v133\n\t"
      "v_cvt_pk_bf16_f32 v139, v134, v135\n\t"
      "buffer_store_dwordx4 v[136:139], v0, s[56:59], s93 offen offset:2048 sc1\n\t"
      "v_accvgpr_read_b32 v144, a76\n\t"
      "v_accvgpr_read_b32 v145, a77\n\t"
      "v_accvgpr_read_b32 v146, a78\n\t"
      "v_accvgpr_read_b32 v147, a79\n\t"
      "v_accvgpr_read_b32 v148, a92\n\t"
      "v_accvgpr_read_b32 v149, a93\n\t"
      "v_accvgpr_read_b32 v150, a94\n\t"
      "v_accvgpr_read_b32 v151, a95\n\t"
      "v_pk_mul_f32 v[144:145], v[144:145], v[250:251]\n\t"
      "v_pk_mul_f32 v[146:147], v[146:147], v[250:251]\n\t"
      "v_pk_mul_f32 v[148:149], v[148:149], v[250:251]\n\t"
      "v_pk_mul_f32 v[150:151], v[150:151], v[250:251]\n\t"
      "v_cvt_pk_bf16_f32 v152, v144, v145\n\t"
      "v_cvt_pk_bf16_f32 v153, v146, v147\n\t"
      "v_cvt_pk_bf16_f32 v154, v148, v149\n\t"
      "v_cvt_pk_bf16_f32 v155, v150, v151\n\t"
      "buffer_store_dwordx4 v[152:155], v0, s[56:59], s93 offen offset:3072 sc1\n\t"
      "s_add_u32 s93, s54, 0x3000\n\t"
      "v_accvgpr_read_b32 v160, a96\n\t"
      "v_accvgpr_read_b32 v161, a97\n\t"
      "v_accvgpr_read_b32 v162, a98\n\t"
      "v_accvgpr_read_b32 v163, a99\n\t"
      "v_accvgpr_read_b32 v164, a112\n\t"
      "v_accvgpr_read_b32 v165, a113\n\t"
      "v_accvgpr_read_b32 v166, a114\n\t"
      "v_accvgpr_read_b32 v167, a115\n\t"
      "v_pk_mul_f32 v[160:161], v[160:161], v[244:245]\n\t"
      "v_pk_mul_f32 v[162:163], v[162:163], v[244:245]\n\t"
      "v_pk_mul_f32 v[164:165], v[164:165], v[244:245]\n\t"
      "v_pk_mul_f32 v[166:167], v[166:167], v[244:245]\n\t"
      "v_cvt_pk_bf16_f32 v168, v160, v161\n\t"
      "v_cvt_pk_bf16_f32 v169, v162, v163\n\t"
      "v_cvt_pk_bf16_f32 v170, v164, v165\n\t"
      "v_cvt_pk_bf16_f32 v171, v166, v167\n\t"
      "buffer_store_dwordx4 v[168:171], v0, s[56:59], s93 offen sc1\n\t"
      "v_accvgpr_read_b32 v176, a100\n\t"
      "v_accvgpr_read_b32 v177, a101\n\t"
      "v_accvgpr_read_b32 v178, a102\n\t"
      "v_accvgpr_read_b32 v179, a103\n\t"
      "v_accvgpr_read_b32 v180, a116\n\t"
      "v_accvgpr_read_b32 v181, a117\n\t"
      "v_accvgpr_read_b32 v182, a118\n\t"
      "v_accvgpr_read_b32 v183, a119\n\t"
      "v_pk_mul_f32 v[176:177], v[176:177], v[246:247]\n\t"
      "v_pk_mul_f32 v[178:179], v[178:179], v[246:247]\n\t"
      "v_pk_mul_f32 v[180:181], v[180:181], v[246:247]\n\t"
      "v_pk_mul_f32 v[182:183], v[182:183], v[246:247]\n\t"
      "v_cvt_pk_bf16_f32 v184, v176, v177\n\t"
      "v_cvt_pk_bf16_f32 v185, v178, v179\n\t"
      "v_cvt_pk_bf16_f32 v186, v180, v181\n\t"
      "v_cvt_pk_bf16_f32 v187, v182, v183\n\t"
      "buffer_store_dwordx4 v[184:187], v0, s[56:59], s93 offen offset:1024 sc1\n\t"
      "v_accvgpr_read_b32 v160, a104\n\t"
      "v_accvgpr_read_b32 v161, a105\n\t"
      "v_accvgpr_read_b32 v162, a106\n\t"
      "v_accvgpr_read_b32 v163, a107\n\t"
      "v_accvgpr_read_b32 v164, a120\n\t"
      "v_accvgpr_read_b32 v165, a121\n\t"
      "v_accvgpr_read_b32 v166, a122\n\t"
      "v_accvgpr_read_b32 v167, a123\n\t"
      "v_pk_mul_f32 v[160:161], v[160:161], v[248:249]\n\t"
      "v_pk_mul_f32 v[162:163], v[162:163], v[248:249]\n\t"
      "v_pk_mul_f32 v[164:165], v[164:165], v[248:249]\n\t"
      "v_pk_mul_f32 v[166:167], v[166:167], v[248:249]\n\t"
      "v_cvt_pk_bf16_f32 v168, v160, v161\n\t"
      "v_cvt_pk_bf16_f32 v169, v162, v163\n\t"
      "v_cvt_pk_bf16_f32 v170, v164, v165\n\t"
      "v_cvt_pk_bf16_f32 v171, v166, v167\n\t"
      "buffer_store_dwordx4 v[168:171], v0, s[56:59], s93 offen offset:2048 sc1\n\t"
      "v_accvgpr_read_b32 v176, a108\n\t"
      "v_accvgpr_read_b32 v177, a109\n\t"
      "v_accvgpr_read_b32 v178, a110\n\t"
      "v_accvgpr_read_b32 v179, a111\n\t"
      "v_accvgpr_read_b32 v180, a124\n\t"
      "v_accvgpr_read_b32 v181, a125\n\t"
      "v_accvgpr_read_b32 v182, a126\n\t"
      "v_accvgpr_read_b32 v183, a127\n\t"
      "v_pk_mul_f32 v[176:177], v[176:177], v[250:251]\n\t"
      "v_pk_mul_f32 v[178:179], v[178:179], v[250:251]\n\t"
      "v_pk_mul_f32 v[180:181], v[180:181], v[250:251]\n\t"
      "v_pk_mul_f32 v[182:183], v[182:183], v[250:251]\n\t"
      "v_cvt_pk_bf16_f32 v184, v176, v177\n\t"
      "v_cvt_pk_bf16_f32 v185, v178, v179\n\t"
      "v_cvt_pk_bf16_f32 v186, v180, v181\n\t"
      "v_cvt_pk_bf16_f32 v187, v182, v183\n\t"
      "buffer_store_dwordx4 v[184:187], v0, s[56:59], s93 offen offset:3072 sc1\n\t"
      "s_mov_b32 s46, 0\n\t"
      "s_waitcnt vmcnt(16)\n\t"
      "K1hw:\n\t"
      "v_cmp_ne_u32 vcc, s52, v254\n\t"
      "v_cmp_ne_u32 s[90:91], s52, v255\n\t"
      "s_nop 4\n\t"
      "s_or_b64 vcc, vcc, s[90:91]\n\t"
      "s_nop 1\n\t"
      "s_cbranch_vccz K1hwd\n\t"
      "s_sleep 2\n\t"
      "buffer_load_dword v254, v252, s[56:59], s94 offen sc1\n\t"
      "buffer_load_dword v255, v253, s[56:59], s96 offen sc1\n\t"
      "s_waitcnt vmcnt(0)\n\t"
      "s_add_u32 s46, s46, 1\n\t"
      "s_cmp_lt_u32 s46, 0x40000\n\t"
      "s_cbranch_scc1 K1hw\n\t"
      "K1hwd:\n\t"
      "s_mul_i32 s40, %11, 0xe3800\n\t"
      "s_add_u32 s40, s40, 0x1940000\n\t"
      "s_mul_i32 s41, %10, 0x36000\n\t"
      "s_add_u32 s41, s41, 0x280000\n\t"
      "v_add_u32 v1, 0x1000, v0\n\t"
      "s_and_b32 s80, s99, 1\n\t"
      "s_mul_i32 s81, s80, 0x1400\n\t"
      "s_add_u32 s81, s81, s40\n\t"
      "s_lshl_b32 s82, s80, 12\n\t"
      "s_add_u32 s82, s82, s41\n\t"
      "buffer_load_dwordx4 v[36:39], v0, s[36:39], s82 offen sc1\n\t"
      "buffer_load_dwordx4 v[40:43], v0, s[36:39], s82 offen offset:1024 sc1\n\t"
      "buffer_load_dwordx4 v[44:47], v0, s[36:39], s82 offen offset:2048 sc1\n\t"
      "buffer_load_dwordx4 v[48:51], v0, s[36:39], s82 offen offset:3072 sc1\n\t"
      "buffer_load_dwordx4 v[16:19], v0, s[36:39], s81 offen sc1\n\t"
      "buffer_load_dwordx4 v[20:23], v0, s[36:39], s81 offen offset:1024 sc1\n\t"
      "buffer_load_dwordx4 v[24:27], v0, s[36:39], s81 offen offset:2048 sc1\n\t"
      "buffer_load_dwordx4 v[28:31], v0, s[36:39], s81 offen offset:3072 sc1\n\t"
      "buffer_load_dwordx4 v[32:35], v1, s[36:39], s81 offen sc1\n\t"
      "s_add_u32 s80, s99, 2\n\t"
      "s_mul_i32 s81, s80, 0x1400\n\t"
      "s_add_u32 s81, s81, s40\n\t"
      "s_lshl_b32 s82, s80, 12\n\t"
      "s_add_u32 s82, s82, s41\n\t"
      "buffer_load_dwordx4 v[72:75], v0, s[36:39], s82 offen sc1\n\t"
      "buffer_load_dwordx4 v[76:79], v0, s[36:39], s82 offen offset:1024 sc1\n\t"
      "buffer_load_dwordx4 v[80:83], v0, s[36:39], s82 offen offset:2048 sc1\n\t"
      "buffer_load_dwordx4 v[84:87], v0, s[36:39], s82 offen offset:3072 sc1\n\t"
      "buffer_load_dwordx4 v[52:55], v0, s[36:39], s81 offen sc1\n\t"
      "buffer_load_dwordx4 v[56:59], v0, s[36:39], s81 offen offset:1024 sc1\n\t"
      "buffer_load_dwordx4 v[60:63], v0, s[36:39], s81 offen offset:2048 sc1\n\t"
      "buffer_load_dwordx4 v[64:67], v0, s[36:39], s81 offen offset:3072 sc1\n\t"
      "buffer_load_dwordx4 v[68:71], v1, s[36:39], s81 offen sc1\n\t"
      "s_add_u32 s80, s99, 6\n\t"
      "s_mul_i32 s81, s80, 0x1400\n\t"
      "s_add_u32 s81, s81, s40\n\t"
      "s_lshl_b32 s82, s80, 12\n\t"
      "s_add_u32 s82, s82, s41\n\t"
      "buffer_load_dwordx4 v[108:111], v0, s[36:39], s82 offen sc1\n\t"
      "buffer_load_dwordx4 v[112:115], v0, s[36:39], s82 offen offset:1024 sc1\n\t"
      "buffer_load_dwordx4 v[116:119], v0, s[36:39], s82 offen offset:2048 sc1\n\t"
      "buffer_load_dwordx4 v[120:123], v0, s[36:39], s82 offen offset:3072 sc1\n\t"
      "buffer_load_dwordx4 v[88:91], v0, s[36:39], s81 offen sc1\n\t"
      "buffer_load_dwordx4 v[92:95], v0, s[36:39], s81 offen offset:1024 sc1\n\t"
      "buffer_load_dwordx4 v[96:99], v0, s[36:39], s81 offen offset:2048 sc1\n\t"
      "buffer_load_dwordx4 v[100:103], v0, s[36:39], s81 offen offset:3072 sc1\n\t"
      "buffer_load_dwordx4 v[104:107], v1, s[36:39], s81 offen sc1\n\t"
      "s_lshl_b32 s93, s99, 2\n\t"
      "s_add_u32 s93, s93, 0x8000\n\t"
      "v_mov_b32 v232, s93\n\t"
      "v_mov_b32 v228, %0\n\t"
      "ds_write_b32 v232, v228\n\t"
      "s_waitcnt lgkmcnt(0)\n\t"
      "s_barrier\n\t"
      "v_mov_b32 v232, 0x8000\n\t"
      "ds_read_b128 v[228:231], v232\n\t"
      "s_waitcnt lgkmcnt(0)\n\t"
      "v_or3_b32 v228, v228, v229, v230\n\t"
      "v_or_b32 v228, v228, v231\n\t"
      "s_nop 1\n\t"
      "v_readfirstlane_b32 s93, v228\n\t"
      "s_nop 3\n\t"
      "s_lshl_b32 s93, s93, 1\n\t"
      "s_or_b32 %0, %0, s93\n\t"
      "s_bitcmp1_b32 %0, 1\n\t"
      "s_cbranch_scc0 K1go\n\t"
      "s_waitcnt vmcnt(0)\n\t"
      "s_branch K1exit\n\t"
      "K1go:\n\t"
      "s_mov_b32 s54, s40\n\t"
      "s_mov_b32 s52, s41\n\t"
      "s_load_dwordx2 s[40:41], %1, 0\n\t"
      "s_mov_b32 s42, 0x280000\n\t"
      "s_mov_b32 s43, 0x20000\n\t"
      "s_load_dwordx2 s[44:45], %1, 72\n\t"
      "s_mov_b32 s46, 0x280000\n\t"
      "s_mov_b32 s47, 0x20000\n\t"
      "s_mul_i32 s53, %10, 0x80000\n\t"
      "s_add_u32 s53, s53, 0x940000\n\t"
      "s_mov_b32 s51, %4\n\t"
      "s_mul_i32 s55, %10, 0x14000\n\t"
      "s_mul_i32 s50, %11, 0xa0\n\t"
      "s_add_u32 s55, s55, s50\n\t"
      "s_mov_b32 s56, %8\n\t"
      "s_lshl_b32 s57, %5, 7\n\t"
      "s_lshl_b32 s50, %2, 2\n\t"
      "s_add_u32 s57, s57, s50\n\t"
      "s_add_u32 s57, s57, 0x205e000\n\t"
      "s_lshr_b32 s50, %11, 1\n\t"
      "s_mul_i32 s62, s50, 0x300\n\t"
      "s_add_u32 s62, s62, 0x205d0d8\n\t"
      "s_waitcnt lgkmcnt(0)\n\t"
      "v_mbcnt_lo_u32_b32 v0, -1, 0\n\t"
      "v_mbcnt_hi_u32_b32 v0, -1, v0\n\t"
      "v_lshlrev_b32 v0, 4, v0\n\t"
      "v_add_u32 v1, 0x1000, v0\n\t"
      "v_lshrrev_b32 v11, 2, v0\n\t"
      "v_and_b32 v11, 0x7c, v11\n\t"
      "s_lshl_b32 s59, %10, 7\n\t"
      "s_add_u32 s59, s59, 0x205e000\n\t"
      "v_lshrrev_b32 v4, 4, v0\n\t"
      "v_and_b32 v5, 15, v4\n\t"
      "v_lshrrev_b32 v6, 4, v4\n\t"
      "v_mul_u32_u24 v5, 0x500, v5\n\t"
      "v_lshlrev_b32 v6, 3, v6\n\t"
      "v_add_u32 v3, v5, v6\n\t"
      "s_mul_i32 s50, s51, 0x5000\n\t"
      "s_add_u32 s55, s55, s50\n\t"
      "v_lshrrev_b32 v6, 1, v0\n\t"
      "s_mul_i32 s50, s51, 0x1400\n\t"
      "v_add_u32 v8, s50, v0\n\t"
      "v_add_u32 v14, 0xa000, v8\n\t"
      "s_mul_i32 s50, s51, 0x5000\n\t"
      "v_add_u32 v9, s50, v0\n\t"
      "buffer_load_dwordx2 v[236:237], v3, s[40:43], s55 offen\n\t"
      "buffer_load_dwordx2 v[238:239], v3, s[40:43], s55 offen offset:32\n\t"
      "buffer_load_dwordx2 v[240:241], v3, s[40:43], s55 offen offset:64\n\t"
      "buffer_load_dwordx2 v[242:243], v3, s[40:43], s55 offen offset:96\n\t"
      "buffer_load_dwordx2 v[244:245], v3, s[40:43], s55 offen offset:128\n\t"
      "s_add_u32 s50, s51, 10\n\t"
      "s_mul_i32 s48, s50, 0x1400\n\t"
      "s_add_u32 s48, s48, s54\n\t"
      "s_lshl_b32 s49, s50, 12\n\t"
      "s_add_u32 s49, s49, s52\n\t"
      "buffer_load_dwordx4 v[144:147], v0, s[36:39], s49 offen sc1\n\t"
      "buffer_load_dwordx4 v[148:151], v0, s[36:39], s49 offen offset:1024 sc1\n\t"
      "buffer_load_dwordx4 v[152:155], v0, s[36:39], s49 offen offset:2048 sc1\n\t"
      "buffer_load_dwordx4 v[156:159], v0, s[36:39], s49 offen offset:3072 sc1\n\t"
      "buffer_load_dwordx4 v[124:127], v0, s[36:39], s48 offen sc1\n\t"
      "buffer_load_dwordx4 v[128:131], v0, s[36:39], s48 offen offset:1024 sc1\n\t"
      "buffer_load_dwordx4 v[132:135], v0, s[36:39], s48 offen offset:2048 sc1\n\t"
      "buffer_load_dwordx4 v[136:139], v0, s[36:39], s48 offen offset:3072 sc1\n\t"
      "buffer_load_dwordx4 v[140:143], v1, s[36:39], s48 offen sc1\n\t"
      "s_waitcnt vmcnt(41)\n\t"
      "v_mov_b32 v15, s56\n\t"
      "v_mov_b32 v10, 0\n\t"
      "s_mov_b64 exec, 1\n\t"
      "s_nop 1\n\t"
      "buffer_store_dword v15, v10, s[36:39], s57 offen sc1\n\t"
      "s_mov_b64 exec, -1\n\t"
      "v_accvgpr_write_b32 a0, 0\n\t"
      "v_accvgpr_write_b32 a1, 0\n\t"
      "v_accvgpr_write_b32 a2, 0\n\t"
      "v_accvgpr_write_b32 a3, 0\n\t"
      "v_accvgpr_write_b32 a4, 0\n\t"
      "v_accvgpr_write_b32 a5, 0\n\t"
      "v_accvgpr_write_b32 a6, 0\n\t"
      "v_accvgpr_write_b32 a7, 0\n\t"
      "v_accvgpr_write_b32 a8, 0\n\t"
      "v_accvgpr_write_b32 a9, 0\n\t"
      "v_accvgpr_write_b32 a10, 0\n\t"
      "v_accvgpr_write_b32 a11, 0\n\t"
      "v_accvgpr_write_b32 a12, 0\n\t"
      "v_accvgpr_write_b32 a13, 0\n\t"
      "v_accvgpr_write_b32 a14, 0\n\t"
      "v_accvgpr_write_b32 a15, 0\n\t"
      "v_accvgpr_write_b32 a16, 0\n\t"
      "v_accvgpr_write_b32 a17, 0\n\t"
      "v_accvgpr_write_b32 a18, 0\n\t"
      "v_accvgpr_write_b32 a19, 0\n\t"
      "v_accvgpr_write_b32 a20, 0\n\t"
      "v_accvgpr_write_b32 a21, 0\n\t"
      "v_accvgpr_write_b32 a22, 0\n\t"
      "v_accvgpr_write_b32 a23, 0\n\t"
      "v_accvgpr_write_b32 a24, 0\n\t"
      "v_accvgpr_write_b32 a25, 0\n\t"
      "v_accvgpr_write_b32 a26, 0\n\t"
      "v_accvgpr_write_b32 a27, 0\n\t"
      "v_accvgpr_write_b32 a28, 0\n\t"
      "v_accvgpr_write_b32 a29, 0\n\t"
      "v_accvgpr_write_b32 a30, 0\n\t"
      "v_accvgpr_write_b32 a31, 0\n\t"
      "v_accvgpr_write_b32 a32, 0\n\t"
      "v_accvgpr_write_b32 a33, 0\n\t"
      "v_accvgpr_write_b32 a34, 0\n\t"
      "v_accvgpr_write_b32 a35, 0\n\t"
      "v_accvgpr_write_b32 a36, 0\n\t"
      "v_accvgpr_write_b32 a37, 0\n\t"
      "v_accvgpr_write_b32 a38, 0\n\t"
      "v_accvgpr_write_b32 a39, 0\n\t"
      "v_accvgpr_write_b32 a40, 0\n\t"
      "v_accvgpr_write_b32 a41, 0\n\t"
      "v_accvgpr_write_b32 a42, 0\n\t"
      "v_accvgpr_write_b32 a43, 0\n\t"
      "v_accvgpr_write_b32 a44, 0\n\t"
      "v_accvgpr_write_b32 a45, 0\n\t"
      "v_accvgpr_write_b32 a46, 0\n\t"
      "v_accvgpr_write_b32 a47, 0\n\t"
      "v_accvgpr_write_b32 a48, 0\n\t"
      "v_accvgpr_write_b32 a49, 0\n\t"
      "v_accvgpr_write_b32 a50, 0\n\t"
      "v_accvgpr_write_b32 a51, 0\n\t"
      "v_accvgpr_write_b32 a52, 0\n\t"
      "v_accvgpr_write_b32 a53, 0\n\t"
      "v_accvgpr_write_b32 a54, 0\n\t"
      "v_accvgpr_write_b32 a55, 0\n\t"
      "v_accvgpr_write_b32 a56, 0\n\t"
      "v_accvgpr_write_b32 a57, 0\n\t"
      "v_accvgpr_write_b32 a58, 0\n\t"
      "v_accvgpr_write_b32 a59, 0\n\t"
      "v_accvgpr_write_b32 a60, 0\n\t"
      "v_accvgpr_write_b32 a61, 0\n\t"
      "v_accvgpr_write_b32 a62, 0\n\t"
      "v_accvgpr_write_b32 a63, 0\n\t"
      "v_accvgpr_write_b32 a64, 0\n\t"
      "v_accvgpr_write_b32 a65, 0\n\t"
      "v_accvgpr_write_b32 a66, 0\n\t"
      "v_accvgpr_write_b32 a67, 0\n\t"
      "v_accvgpr_write_b32 a68, 0\n\t"
      "v_accvgpr_write_b32 a69, 0\n\t"
      "v_accvgpr_write_b32 a70, 0\n\t"
      "v_accvgpr_write_b32 a71, 0\n\t"
      "v_accvgpr_write_b32 a72, 0\n\t"
      "v_accvgpr_write_b32 a73, 0\n\t"
      "v_accvgpr_write_b32 a74, 0\n\t"
      "v_accvgpr_write_b32 a75, 0\n\t"
      "v_accvgpr_write_b32 a76, 0\n\t"
      "v_accvgpr_write_b32 a77, 0\n\t"
      "v_accvgpr_write_b32 a78, 0\n\t"
      "v_accvgpr_write_b32 a79, 0\n\t"
      "v_accvgpr_write_b32 a80, 0\n\t"
      "v_accvgpr_write_b32 a81, 0\n\t"
      "v_accvgpr_write_b32 a82, 0\n\t"
      "v_accvgpr_write_b32 a83, 0\n\t"
      "v_accvgpr_write_b32 a84, 0\n\t"
      "v_accvgpr_write_b32 a85, 0\n\t"
      "v_accvgpr_write_b32 a86, 0\n\t"
      "v_accvgpr_write_b32 a87, 0\n\t"
      "v_accvgpr_write_b32 a88, 0\n\t"
      "v_accvgpr_write_b32 a89, 0\n\t"
      "v_accvgpr_write_b32 a90, 0\n\t"
      "v_accvgpr_write_b32 a91, 0\n\t"
      "v_accvgpr_write_b32 a92, 0\n\t"
      "v_accvgpr_write_b32 a93, 0\n\t"
      "v_accvgpr_write_b32 a94, 0\n\t"
      "v_accvgpr_write_b32 a95, 0\n\t"
      "v_accvgpr_write_b32 a96, 0\n\t"
      "v_accvgpr_write_b32 a97, 0\n\t"
      "v_accvgpr_write_b32 a98, 0\n\t"
      "v_accvgpr_write_b32 a99, 0\n\t"
      "v_accvgpr_write_b32 a100, 0\n\t"
      "v_accvgpr_write_b32 a101, 0\n\t"
      "v_accvgpr_write_b32 a102, 0\n\t"
      "v_accvgpr_write_b32 a103, 0\n\t"
      "v_accvgpr_write_b32 a104, 0\n\t"
      "v_accvgpr_write_b32 a105, 0\n\t"
      "v_accvgpr_write_b32 a106, 0\n\t"
      "v_accvgpr_write_b32 a107, 0\n\t"
      "v_accvgpr_write_b32 a108, 0\n\t"
      "v_accvgpr_write_b32 a109, 0\n\t"
      "v_accvgpr_write_b32 a110, 0\n\t"
      "v_accvgpr_write_b32 a111, 0\n\t"
      "v_accvgpr_write_b32 a112, 0\n\t"
      "v_accvgpr_write_b32 a113, 0\n\t"
      "v_accvgpr_write_b32 a114, 0\n\t"
      "v_accvgpr_write_b32 a115, 0\n\t"
      "v_accvgpr_write_b32 a116, 0\n\t"
      "v_accvgpr_write_b32 a117, 0\n\t"
      "v_accvgpr_write_b32 a118, 0\n\t"
      "v_accvgpr_write_b32 a119, 0\n\t"
      "v_accvgpr_write_b32 a120, 0\n\t"
      "v_accvgpr_write_b32 a121, 0\n\t"
      "v_accvgpr_write_b32 a122, 0\n\t"
      "v_accvgpr_write_b32 a123, 0\n\t"
      "v_accvgpr_write_b32 a124, 0\n\t"
      "v_accvgpr_write_b32 a125, 0\n\t"
      "v_accvgpr_write_b32 a126, 0\n\t"
      "v_accvgpr_write_b32 a127, 0\n\t"
      "v_accvgpr_write_b32 a128, 0\n\t"
      "v_accvgpr_write_b32 a129, 0\n\t"
      "v_accvgpr_write_b32 a130, 0\n\t"
      "v_accvgpr_write_b32 a131, 0\n\t"
      "v_accvgpr_write_b32 a132, 0\n\t"
      "v_accvgpr_write_b32 a133, 0\n\t"
      "v_accvgpr_write_b32 a134, 0\n\t"
      "v_accvgpr_write_b32 a135, 0\n\t"
      "v_accvgpr_write_b32 a136, 0\n\t"
      "v_accvgpr_write_b32 a137, 0\n\t"
      "v_accvgpr_write_b32 a138, 0\n\t"
      "v_accvgpr_write_b32 a139, 0\n\t"
      "v_accvgpr_write_b32 a140, 0\n\t"
      "v_accvgpr_write_b32 a141, 0\n\t"
      "v_accvgpr_write_b32 a142, 0\n\t"
      "v_accvgpr_write_b32 a143, 0\n\t"
      "v_accvgpr_write_b32 a144, 0\n\t"
      "v_accvgpr_write_b32 a145, 0\n\t"
      "v_accvgpr_write_b32 a146, 0\n\t"
      "v_accvgpr_write_b32 a147, 0\n\t"
      "v_accvgpr_write_b32 a148, 0\n\t"
      "v_accvgpr_write_b32 a149, 0\n\t"
      "v_accvgpr_write_b32 a150, 0\n\t"
      "v_accvgpr_write_b32 a151, 0\n\t"
      "v_accvgpr_write_b32 a152, 0\n\t"
      "v_accvgpr_write_b32 a153, 0\n\t"
      "v_accvgpr_write_b32 a154, 0\n\t"
      "v_accvgpr_write_b32 a155, 0\n\t"
      "v_accvgpr_write_b32 a156, 0\n\t"
      "v_accvgpr_write_b32 a157, 0\n\t"
      "v_accvgpr_write_b32 a158, 0\n\t"
      "v_accvgpr_write_b32 a159, 0\n\t"
      "s_add_u32 s50, s51, 14\n\t"
      "s_mul_i32 s48, s50, 0x1400\n\t"
      "s_add_u32 s48, s48, s54\n\t"
      "s_lshl_b32 s49, s50, 12\n\t"
      "s_add_u32 s49, s49, s52\n\t"
      "s_waitcnt vmcnt(32)\n\t"
      "s_cmp_ge_u32 s51, 2\n\t"
      "s_cbranch_scc1 K2skip0m\n\t"
      "v_mfma_f32_16x16x32_bf16 a[0:3], v[16:19], v[36:39], a[0:3]\n\t"
      "v_mfma_f32_16x16x32_bf16 a[20:23], v[16:19], v[40:43], a[20:23]\n\t"
      "v_mfma_f32_16x16x32_bf16 a[40:43], v[16:19], v[44:47], a[40:43]\n\t"
      "v_mfma_f32_16x16x32_bf16 a[60:63], v[16:19], v[48:51], a[60:63]\n\t"
      "v_mfma_f32_16x16x32_bf16 a[4:7], v[20:23], v[36:39], a[4:7]\n\t"
      "v_mfma_f32_16x16x32_bf16 a[24:27], v[20:23], v[40:43], a[24:27]\n\t"
      "v_mfma_f32_16x16x32_bf16 a[44:47], v[20:23], v[44:47], a[44:47]\n\t"
      "v_mfma_f32_16x16x32_bf16 a[64:67], v[20:23], v[48:51], a[64:67]\n\t"
      "v_mfma_f32_16x16x32_bf16 a[8:11], v[24:27], v[36:39], a[8:11]\n\t"
      "v_mfma_f32_16x16x32_bf16 a[28:31], v[24:27], v[40:43], a[28:31]\n\t"
      "v_mfma_f32_16x16x32_bf16 a[48:51], v[24:27], v[44:47], a[48:51]\n\t"
      "v_mfma_f32_16x16x32_bf16 a[68:71], v[24:27], v[48:51], a[68:71]\n\t"
      "v_mfma_f32_16x16x32_bf16 a[12:15], v[28:31], v[36:39], a[12:15]\n\t"
      "v_mfma_f32_16x16x32_bf16 a[32:35], v[28:31], v[40:43], a[32:35]\n\t"
      "v_mfma_f32_16x16x32_bf16 a[52:55], v[28:31], v[44:47], a[52:55]\n\t"
      "v_mfma_f32_16x16x32_bf16 a[72:75], v[28:31], v[48:51], a[72:75]\n\t"
      "v_mfma_f32_16x16x32_bf16 a[16:19], v[32:35], v[36:39], a[16:19]\n\t"
      "v_mfma_f32_16x16x32_bf16 a[36:39], v[32:35], v[40:43], a[36:39]\n\t"
      "v_mfma_f32_16x16x32_bf16 a[56:59], v[32:35], v[44:47], a[56:59]\n\t"
      "v_mfma_f32_16x16x32_bf16 a[76:79], v[32:35], v[48:51], a[76:79]\n\t"
      "K2skip0m:\n\t"
      "buffer_load_dwordx4 v[180:183], v0, s[36:39], s49 offen sc1\n\t"
      "buffer_load_dwordx4 v[184:187], v0, s[36:39], s49 offen offset:1024 sc1\n\t"
      "buffer_load_dwordx4 v[188:191], v0, s[36:39], s49 offen offset:2048 sc1\n\t"
      "buffer_load_dwordx4 v[192:195], v0, s[36:39], s49 offen offset:3072 sc1\n\t"
      "buffer_load_dwordx4 v[160:163], v0, s[36:39], s48 offen sc1\n\t"
      "buffer_load_dwordx4 v[164:167], v0, s[36:39], s48 offen offset:1024 sc1\n\t"
      "buffer_load_dwordx4 v[168:171], v0, s[36:39], s48 offen offset:2048 sc1\n\t"
      "buffer_load_dwordx4 v[172:175], v0, s[36:39], s48 offen offset:3072 sc1\n\t"
      "buffer_load_dwordx4 v[176:179], v1, s[36:39], s48 offen sc1\n\t"
      "s_add_u32 s50, s51, 18\n\t"
      "s_mul_i32 s48, s50, 0x1400\n\t"
      "s_add_u32 s48, s48, s54\n\t"
      "s_lshl_b32 s49, s50, 12\n\t"
      "s_add_u32 s49, s49, s52\n\t"
      "s_waitcnt vmcnt(32)\n\t"
      "v_mfma_f32_16x16x32_bf16 a[0:3], v[52:55], v[72:75], a[0:3]\n\t"
      "v_mfma_f32_16x16x32_bf16 a[20:23], v[52:55], v[76:79], a[20:23]\n\t"
      "buffer_load_dwordx4 v[36:39], v0, s[36:39], s49 offen sc1\n\t"
      "v_mfma_f32_16x16x32_bf16 a[40:43], v[52:55], v[80:83], a[40:43]\n\t"
      "v_mfma_f32_16x16x32_bf16 a[60:63], v[52:55], v[84:87], a[60:63]\n\t"
      "buffer_load_dwordx4 v[40:43], v0, s[36:39], s49 offen offset:1024 sc1\n\t"
      "v_mfma_f32_16x16x32_bf16 a[4:7], v[56:59], v[72:75], a[4:7]\n\t"
      "v_mfma_f32_16x16x32_bf16 a[24:27], v[56:59], v[76:79], a[24:27]\n\t"
      "buffer_load_dwordx4 v[44:47], v0, s[36:39], s49 offen offset:2048 sc1\n\t"
      "v_mfma_f32_16x16x32_bf16 a[44:47], v[56:59], v[80:83], a[44:47]\n\t"
      "v_mfma_f32_16x16x32_bf16 a[64:67], v[56:59], v[84:87], a[64:67]\n\t"
      "buffer_load_dwordx4 v[48:51], v0, s[36:39], s49 offen offset:3072 sc1\n\t"
      "v_mfma_f32_16x16x32_bf16 a[8:11], v[60:63], v[72:75], a[8:11]\n\t"
      "v_mfma_f32_16x16x32_bf16 a[28:31], v[60:63], v[76:79], a[28:31]\n\t"
      "buffer_load_dwordx4 v[16:19], v0, s[36:39], s48 offen sc1\n\t"
      "v_mfma_f32_16x16x32_bf16 a[48:51], v[60:63], v[80:83], a[48:51]\n\t"
      "v_mfma_f32_16x16x32_bf16 a[68:71], v[60:63], v[84:87], a[68:71]\n\t"
      "buffer_load_dwordx4 v[20:23], v0, s[36:39], s48 offen offset:1024 sc1\n\t"
      "v_mfma_f32_16x16x32_bf16 a[12:15], v[64:67], v[72:75], a[12:15]\n\t"
      "v_mfma_f32_16x16x32_bf16 a[32:35], v[64:67], v[76:79], a[32:35]\n\t"
      "buffer_load_dwordx4 v[24:27], v0, s[36:39], s48 offen offset:2048 sc1\n\t"
      "v_mfma_f32_16x16x32_bf16 a[52:55], v[64:67], v[80:83], a[52:55]\n\t"
      "v_mfma_f32_16x16x32_bf16 a[72:75], v[64:67], v[84:87], a[72:75]\n\t"
      "buffer_load_dwordx4 v[28:31], v0, s[36:39], s48 offen offset:3072 sc1\n\t"
      "v_mfma_f32_16x16x32_bf16 a[16:19], v[68:71], v[72:75], a[16:19]\n\t"
      "v_mfma_f32_16x16x32_bf16 a[36:39], v[68:71], v[76:79], a[36:39]\n\t"
      "buffer_load_dwordx4 v[32:35], v1, s[36:39], s48 offen sc1\n\t"
      "v_mfma_f32_16x16x32_bf16 a[56:59], v[68:71], v[80:83], a[56:59]\n\t"
      "v_mfma_f32_16x16x32_bf16 a[76:79], v[68:71], v[84:87], a[76:79]\n\t"
      "s_add_u32 s50, s51, 22\n\t"
      "s_mul_i32 s48, s50, 0x1400\n\t"
      "s_add_u32 s48, s48, s54\n\t"
      "s_lshl_b32 s49, s50, 12\n\t"
      "s_add_u32 s49, s49, s52\n\t"
      "s_waitcnt vmcnt(32)\n\t"
      "v_mfma_f32_16x16x32_bf16 a[0:3], v[88:91], v[108:111], a[0:3]\n\t"
      "v_mfma_f32_16x16x32_bf16 a[20:23], v[88:91], v[112:115], a[20:23]\n\t"
      "buffer_load_dwordx4 v[72:75], v0, s[36:39], s49 offen sc1\n\t"
      "v_mfma_f32_16x16x32_bf16 a[40:43], v[88:91], v[116:119], a[40:43]\n\t"
      "v_mfma_f32_16x16x32_bf16 a[60:63], v[88:91], v[120:123], a[60:63]\n\t"
      "buffer_load_dwordx4 v[76:79], v0, s[36:39], s49 offen offset:1024 sc1\n\t"
      "v_mfma_f32_16x16x32_bf16 a[4:7], v[92:95], v[108:111], a[4:7]\n\t"
      "v_mfma_f32_16x16x32_bf16 a[24:27], v[92:95], v[112:115], a[24:27]\n\t"
      "buffer_load_dwordx4 v[80:83], v0, s[36:39], s49 offen offset:2048 sc1\n\t"
      "v_mfma_f32_16x16x32_bf16 a[44:47], v[92:95], v[116:119], a[44:47]\n\t"
      "v_mfma_f32_16x16x32_bf16 a[64:67], v[92:95], v[120:123], a[64:67]\n\t"
      "buffer_load_dwordx4 v[84:87], v0, s[36:39], s49 offen offset:3072 sc1\n\t"
      "v_mfma_f32_16x16x32_bf16 a[8:11], v[96:99], v[108:111], a[8:11]\n\t"
      "v_mfma_f32_16x16x32_bf16 a[28:31], v[96:99], v[112:115], a[28:31]\n\t"
      "buffer_load_dwordx4 v[52:55], v0, s[36:39], s48 offen sc1\n\t"
      "v_mfma_f32_16x16x32_bf16 a[48:51], v[96:99], v[116:119], a[48:51]\n\t"
      "v_mfma_f32_16x16x32_bf16 a[68:71], v[96:99], v[120:123], a[68:71]\n\t"
      "buffer_load_dwordx4 v[56:59], v0, s[36:39], s48 offen offset:1024 sc1\n\t"
      "v_mfma_f32_16x16x32_bf16 a[12:15], v[100:103], v[108:111], a[12:15]\n\t"
      "v_mfma_f32_16x16x32_bf16 a[32:35], v[100:103], v[112:115], a[32:35]\n\t"
      "buffer_load_dwordx4 v[60:63], v0, s[36:39], s48 offen offset:2048 sc1\n\t"
      "v_mfma_f32_16x16x32_bf16 a[52:55], v[100:103], v[116:119], a[52:55]\n\t"
      "v_mfma_f32_16x16x32_bf16 a[72:75], v[100:103], v[120:123], a[72:75]\n\t"
      "buffer_load_dwordx4 v[64:67], v0, s[36:39], s48 offen offset:3072 sc1\n\t"
      "v_mfma_f32_16x16x32_bf16 a[16:19], v[104:107], v[108:111], a[16:19]\n\t"
      "v_mfma_f32_16x16x32_bf16 a[36:39], v[104:107], v[112:115], a[36:39]\n\t"
      "buffer_load_dwordx4 v[68:71], v1, s[36:39], s48 offen sc1\n\t"
      "v_mfma_f32_16x16x32_bf16 a[56:59], v[104:107], v[116:119], a[56:59]\n\t"
      "v_mfma_f32_16x16x32_bf16 a[76:79], v[104:107], v[120:123], a[76:79]\n\t"
      "s_add_u32 s50, s51, 26\n\t"
      "s_mul_i32 s48, s50, 0x1400\n\t"
      "s_add_u32 s48, s48, s54\n\t"
      "s_lshl_b32 s49, s50, 12\n\t"
      "s_add_u32 s49, s49, s52\n\t"
      "s_waitcnt vmcnt(27)\n\t"
      "v_mfma_f32_16x16x32_bf16 a[0:3], v[124:127], v[144:147], a[0:3]\n\t"
      "v_mfma_f32_16x16x32_bf16 a[20:23], v[124:127], v[148:151], a[20:23]\n\t"
      "buffer_load_dwordx4 v[108:111], v0, s[36:39], s49 offen sc1\n\t"
      "v_mfma_f32_16x16x32_bf16 a[40:43], v[124:127], v[152:155], a[40:43]\n\t"
      "v_mfma_f32_16x16x32_bf16 a[60:63], v[124:127], v[156:159], a[60:63]\n\t"
      "buffer_load_dwordx4 v[112:115], v0, s[36:39], s49 offen offset:1024 sc1\n\t"
      "v_mfma_f32_16x16x32_bf16 a[4:7], v[128:131], v[144:147], a[4:7]\n\t"
      "v_mfma_f32_16x16x32_bf16 a[24:27], v[128:131], v[148:151], a[24:27]\n\t"
      "buffer_load_dwordx4 v[116:119], v0, s[36:39], s49 offen offset:2048 sc1\n\t"
      "v_mfma_f32_16x16x32_bf16 a[44:47], v[128:131], v[152:155], a[44:47]\n\t"
      "v_mfma_f32_16x16x32_bf16 a[64:67], v[128:131], v[156:159], a[64:67]\n\t"
      "buffer_load_dwordx4 v[120:123], v0, s[36:39], s49 offen offset:3072 sc1\n\t"
      "v_mfma_f32_16x16x32_bf16 a[8:11], v[132:135], v[144:147], a[8:11]\n\t"
      "v_mfma_f32_16x16x32_bf16 a[28:31], v[132:135], v[148:151], a[28:31]\n\t"
      "buffer_load_dwordx4 v[88:91], v0, s[36:39], s48 offen sc1\n\t"
      "v_mfma_f32_16x16x32_bf16 a[48:51], v[132:135], v[152:155], a[48:51]\n\t"
      "v_mfma_f32_16x16x32_bf16 a[68:71], v[132:135], v[156:159], a[68:71]\n\t"
      "buffer_load_dwordx4 v[92:95], v0, s[36:39], s48 offen offset:1024 sc1\n\t"
      "v_mfma_f32_16x16x32_bf16 a[12:15], v[136:139], v[144:147], a[12:15]\n\t"
      "v_mfma_f32_16x16x32_bf16 a[32:35], v[136:139], v[148:151], a[32:35]\n\t"
      "buffer_load_dwordx4 v[96:99], v0, s[36:39], s48 offen offset:2048 sc1\n\t"
      "v_mfma_f32_16x16x32_bf16 a[52:55], v[136:139], v[152:155], a[52:55]\n\t"
      "v_mfma_f32_16x16x32_bf16 a[72:75], v[136:139], v[156:159], a[72:75]\n\t"
      "buffer_load_dwordx4 v[100:103], v0, s[36:39], s48 offen offset:3072 sc1\n\t"
      "v_mfma_f32_16x16x32_bf16 a[16:19], v[140:143], v[144:147], a[16:19]\n\t"
      "v_mfma_f32_16x16x32_bf16 a[36:39], v[140:143], v[148:151], a[36:39]\n\t"
      "buffer_load_dwordx4 v[104:107], v1, s[36:39], s48 offen sc1\n\t"
      "v_mfma_f32_16x16x32_bf16 a[56:59], v[140:143], v[152:155], a[56:59]\n\t"
      "v_mfma_f32_16x16x32_bf16 a[76:79], v[140:143], v[156:159], a[76:79]\n\t"
      "s_add_u32 s50, s51, 30\n\t"
      "s_mul_i32 s48, s50, 0x1400\n\t"
      "s_add_u32 s48, s48, s54\n\t"
      "s_lshl_b32 s49, s50, 12\n\t"
      "s_add_u32 s49, s49, s52\n\t"
      "s_waitcnt vmcnt(27)\n\t"
      "v_mfma_f32_16x16x32_bf16 a[0:3], v[160:163], v[180:183], a[0:3]\n\t"
      "v_mfma_f32_16x16x32_bf16 a[20:23], v[160:163], v[184:187], a[20:23]\n\t"
      "buffer_load_dwordx4 v[144:147], v0, s[36:39], s49 offen sc1\n\t"
      "v_mfma_f32_16x16x32_bf16 a[40:43], v[160:163], v[188:191], a[40:43]\n\t"
      "v_mfma_f32_16x16x32_bf16 a[60:63], v[160:163], v[192:195], a[60:63]\n\t"
      "buffer_load_dwordx4 v[148:151], v0, s[36:39], s49 offen offset:1024 sc1\n\t"
      "v_mfma_f32_16x16x32_bf16 a[4:7], v[164:167], v[180:183], a[4:7]\n\t"
      "v_mfma_f32_16x16x32_bf16 a[24:27], v[164:167], v[184:187], a[24:27]\n\t"
      "buffer_load_dwordx4 v[152:155], v0, s[36:39], s49 offen offset:2048 sc1\n\t"
      "v_mfma_f32_16x16x32_bf16 a[44:47], v[164:167], v[188:191], a[44:47]\n\t"
      "v_mfma_f32_16x16x32_bf16 a[64:67], v[164:167], v[192:195], a[64:67]\n\t"
      "buffer_load_dwordx4 v[156:159], v0, s[36:39], s49 offen offset:3072 sc1\n\t"
      "v_mfma_f32_16x16x32_bf16 a[8:11], v[168:171], v[180:183], a[8:11]\n\t"
      "v_mfma_f32_16x16x32_bf16 a[28:31], v[168:171], v[184:187], a[28:31]\n\t"
      "buffer_load_dwordx4 v[124:127], v0, s[36:39], s48 offen sc1\n\t"
      "v_mfma_f32_16x16x32_bf16 a[48:51], v[168:171], v[188:191], a[48:51]\n\t"
      "v_mfma_f32_16x16x32_bf16 a[68:71], v[168:171], v[192:195], a[68:71]\n\t"
      "buffer_load_dwordx4 v[128:131], v0, s[36:39], s48 offen offset:1024 sc1\n\t"
      "v_mfma_f32_16x16x32_bf16 a[12:15], v[172:175], v[180:183], a[12:15]\n\t"
      "v_mfma_f32_16x16x32_bf16 a[32:35], v[172:175], v[184:187], a[32:35]\n\t"
      "buffer_load_dwordx4 v[132:135], v0, s[36:39], s48 offen offset:2048 sc1\n\t"
      "v_mfma_f32_16x16x32_bf16 a[52:55], v[172:175], v[188:191], a[52:55]\n\t"
      "v_mfma_f32_16x16x32_bf16 a[72:75], v[172:175], v[192:195], a[72:75]\n\t"
      "buffer_load_dwordx4 v[136:139], v0, s[36:39], s48 offen offset:3072 sc1\n\t"
      "v_mfma_f32_16x16x32_bf16 a[16:19], v[176:179], v[180:183], a[16:19]\n\t"
      "v_mfma_f32_16x16x32_bf16 a[36:39], v[176:179], v[184:187], a[36:39]\n\t"
      "buffer_load_dwordx4 v[140:143], v1, s[36:39], s48 offen sc1\n\t"
      "v_mfma_f32_16x16x32_bf16 a[56:59], v[176:179], v[188:191], a[56:59]\n\t"
      "v_mfma_f32_16x16x32_bf16 a[76:79], v[176:179], v[192:195], a[76:79]\n\t"
      "s_add_u32 s50, s51, 34\n\t"
      "s_mul_i32 s48, s50, 0x1400\n\t"
      "s_add_u32 s48, s48, s54\n\t"
      "s_lshl_b32 s49, s50, 12\n\t"
      "s_add_u32 s49, s49, s52\n\t"
      "s_waitcnt vmcnt(27)\n\t"
      "v_mfma_f32_16x16x32_bf16 a[0:3], v[16:19], v[36:39], a[0:3]\n\t"
      "v_mfma_f32_16x16x32_bf16 a[20:23], v[16:19], v[40:43], a[20:23]\n\t"
      "buffer_load_dwordx4 v[180:183], v0, s[36:39], s49 offen sc1\n\t"
      "v_mfma_f32_16x16x32_bf16 a[40:43], v[16:19], v[44:47], a[40:43]\n\t"
      "v_mfma_f32_16x16x32_bf16 a[60:63], v[16:19], v[48:51], a[60:63]\n\t"
      "buffer_load_dwordx4 v[184:187], v0, s[36:39], s49 offen offset:1024 sc1\n\t"
      "v_mfma_f32_16x16x32_bf16 a[4:7], v[20:23], v[36:39], a[4:7]\n\t"
      "v_mfma_f32_16x16x32_bf16 a[24:27], v[20:23], v[40:43], a[24:27]\n\t"
      "buffer_load_dwordx4 v[188:191], v0, s[36:39], s49 offen offset:2048 sc1\n\t"
      "v_mfma_f32_16x16x32_bf16 a[44:47], v[20:23], v[44:47], a[44:47]\n\t"
      "v_mfma_f32_16x16x32_bf16 a[64:67], v[20:23], v[48:51], a[64:67]\n\t"
      "buffer_load_dwordx4 v[192:195], v0, s[36:39], s49 offen offset:3072 sc1\n\t"
      "v_mfma_f32_16x16x32_bf16 a[8:11], v[24:27], v[36:39], a[8:11]\n\t"
      "v_mfma_f32_16x16x32_bf16 a[28:31], v[24:27], v[40:43], a[28:31]\n\t"
      "buffer_load_dwordx4 v[160:163], v0, s[36:39], s48 offen sc1\n\t"
      "v_mfma_f32_16x16x32_bf16 a[48:51], v[24:27], v[44:47], a[48:51]\n\t"
      "v_mfma_f32_16x16x32_bf16 a[68:71], v[24:27], v[48:51], a[68:71]\n\t"
      "buffer_load_dwordx4 v[164:167], v0, s[36:39], s48 offen offset:1024 sc1\n\t"
      "v_mfma_f32_16x16x32_bf16 a[12:15], v[28:31], v[36:39], a[12:15]\n\t"
      "v_mfma_f32_16x16x32_bf16 a[32:35], v[28:31], v[40:43], a[32:35]\n\t"
      "buffer_load_dwordx4 v[168:171], v0, s[36:39], s48 offen offset:2048 sc1\n\t"
      "v_mfma_f32_16x16x32_bf16 a[52:55], v[28:31], v[44:47], a[52:55]\n\t"
      "v_mfma_f32_16x16x32_bf16 a[72:75], v[28:31], v[48:51], a[72:75]\n\t"
      "buffer_load_dwordx4 v[172:175], v0, s[36:39], s48 offen offset:3072 sc1\n\t"
      "v_mfma_f32_16x16x32_bf16 a[16:19], v[32:35], v[36:39], a[16:19]\n\t"
      "v_mfma_f32_16x16x32_bf16 a[36:39], v[32:35], v[40:43], a[36:39]\n\t"
      "buffer_load_dwordx4 v[176:179], v1, s[36:39], s48 offen sc1\n\t"
      "v_mfma_f32_16x16x32_bf16 a[56:59], v[32:35], v[44:47], a[56:59]\n\t"
      "v_mfma_f32_16x16x32_bf16 a[76:79], v[32:35], v[48:51], a[76:79]\n\t"
      "s_add_u32 s50, s51, 38\n\t"
      "s_mul_i32 s48, s50, 0x1400\n\t"
      "s_add_u32 s48, s48, s54\n\t"
      "s_lshl_b32 s49, s50, 12\n\t"
      "s_add_u32 s49, s49, s52\n\t"
      "s_waitcnt vmcnt(27)\n\t"
      "v_mfma_f32_16x16x32_bf16 a[0:3], v[52:55], v[72:75], a[0:3]\n\t"
      "v_mfma_f32_16x16x32_bf16 a[20:23], v[52:55], v[76:79], a[20:23]\n\t"
      "buffer_load_dwordx4 v[36:39], v0, s[36:39], s49 offen sc1\n\t"
      "v_mfma_f32_16x16x32_bf16 a[40:43], v[52:55], v[80:83], a[40:43]\n\t"
      "v_mfma_f32_16x16x32_bf16 a[60:63], v[52:55], v[84:87], a[60:63]\n\t"
      "buffer_load_dwordx4 v[40:43], v0, s[36:39], s49 offen offset:1024 sc1\n\t"
      "v_mfma_f32_16x16x32_bf16 a[4:7], v[56:59], v[72:75], a[4:7]\n\t"
      "v_mfma_f32_16x16x32_bf16 a[24:27], v[56:59], v[76:79], a[24:27]\n\t"
      "buffer_load_dwordx4 v[44:47], v0, s[36:39], s49 offen offset:2048 sc1\n\t"
      "v_mfma_f32_16x16x32_bf16 a[44:47], v[56:59], v[80:83], a[44:47]\n\t"
      "v_mfma_f32_16x16x32_bf16 a[64:67], v[56:59], v[84:87], a[64:67]\n\t"
      "buffer_load_dwordx4 v[48:51], v0, s[36:39], s49 offen offset:3072 sc1\n\t"
      "v_mfma_f32_16x16x32_bf16 a[8:11], v[60:63], v[72:75], a[8:11]\n\t"
      "v_mfma_f32_16x16x32_bf16 a[28:31], v[60:63], v[76:79], a[28:31]\n\t"
      "buffer_load_dwordx4 v[16:19], v0, s[36:39], s48 offen sc1\n\t"
      "v_mfma_f32_16x16x32_bf16 a[48:51], v[60:63], v[80:83], a[48:51]\n\t"
      "v_mfma_f32_16x16x32_bf16 a[68:71], v[60:63], v[84:87], a[68:71]\n\t"
      "buffer_load_dwordx4 v[20:23], v0, s[36:39], s48 offen offset:1024 sc1\n\t"
      "v_mfma_f32_16x16x32_bf16 a[12:15], v[64:67], v[72:75], a[12:15]\n\t"
      "v_mfma_f32_16x16x32_bf16 a[32:35], v[64:67], v[76:79], a[32:35]\n\t"
      "buffer_load_dwordx4 v[24:27], v0, s[36:39], s48 offen offset:2048 sc1\n\t"
      "v_mfma_f32_16x16x32_bf16 a[52:55], v[64:67], v[80:83], a[52:55]\n\t"
      "v_mfma_f32_16x16x32_bf16 a[72:75], v[64:67], v[84:87], a[72:75]\n\t"
      "buffer_load_dwordx4 v[28:31], v0, s[36:39], s48 offen offset:3072 sc1\n\t"
      "v_mfma_f32_16x16x32_bf16 a[16:19], v[68:71], v[72:75], a[16:19]\n\t"
      "v_mfma_f32_16x16x32_bf16 a[36:39], v[68:71], v[76:79], a[36:39]\n\t"
      "buffer_load_dwordx4 v[32:35], v1, s[36:39], s48 offen sc1\n\t"
      "v_mfma_f32_16x16x32_bf16 a[56:59], v[68:71], v[80:83], a[56:59]\n\t"
      "v_mfma_f32_16x16x32_bf16 a[76:79], v[68:71], v[84:87], a[76:79]\n\t"
      "buffer_load_dword v12, v11, s[36:39], s59 offen sc1\n\t"
      "buffer_load_dwordx2 v[4:5], v6, s[36:39], s62 offen sc1\n\t"
      "s_add_u32 s50, s51, 42\n\t"
      "s_mul_i32 s48, s50, 0x1400\n\t"
      "s_add_u32 s48, s48, s54\n\t"
      "s_lshl_b32 s49, s50, 12\n\t"
      "s_add_u32 s49, s49, s52\n\t"
      "s_waitcnt vmcnt(27)\n\t"
      "v_mfma_f32_16x16x32_bf16 a[0:3], v[88:91], v[108:111], a[0:3]\n\t"
      "v_mfma_f32_16x16x32_bf16 a[20:23], v[88:91], v[112:115], a[20:23]\n\t"
      "buffer_load_dwordx4 v[72:75], v0, s[36:39], s49 offen sc1\n\t"
      "v_mfma_f32_16x16x32_bf16 a[40:43], v[88:91], v[116:119], a[40:43]\n\t"
      "v_mfma_f32_16x16x32_bf16 a[60:63], v[88:91], v[120:123], a[60:63]\n\t"
      "buffer_load_dwordx4 v[76:79], v0, s[36:39], s49 offen offset:1024 sc1\n\t"
      "v_mfma_f32_16x16x32_bf16 a[4:7], v[92:95], v[108:111], a[4:7]\n\t"
      "v_mfma_f32_16x16x32_bf16 a[24:27], v[92:95], v[112:115], a[24:27]\n\t"
      "buffer_load_dwordx4 v[80:83], v0, s[36:39], s49 offen offset:2048 sc1\n\t"
      "v_mfma_f32_16x16x32_bf16 a[44:47], v[92:95], v[116:119], a[44:47]\n\t"
      "v_mfma_f32_16x16x32_bf16 a[64:67], v[92:95], v[120:123], a[64:67]\n\t"
      "buffer_load_dwordx4 v[84:87], v0, s[36:39], s49 offen offset:3072 sc1\n\t"
      "v_mfma_f32_16x16x32_bf16 a[8:11], v[96:99], v[108:111], a[8:11]\n\t"
      "v_mfma_f32_16x16x32_bf16 a[28:31], v[96:99], v[112:115], a[28:31]\n\t"
      "buffer_load_dwordx4 v[52:55], v0, s[36:39], s48 offen sc1\n\t"
      "v_mfma_f32_16x16x32_bf16 a[48:51], v[96:99], v[116:119], a[48:51]\n\t"
      "v_mfma_f32_16x16x32_bf16 a[68:71], v[96:99], v[120:123], a[68:71]\n\t"
      "buffer_load_dwordx4 v[56:59], v0, s[36:39], s48 offen offset:1024 sc1\n\t"
      "v_mfma_f32_16x16x32_bf16 a[12:15], v[100:103], v[108:111], a[12:15]\n\t"
      "v_mfma_f32_16x16x32_bf16 a[32:35], v[100:103], v[112:115], a[32:35]\n\t"
      "buffer_load_dwordx4 v[60:63], v0, s[36:39], s48 offen offset:2048 sc1\n\t"
      "v_mfma_f32_16x16x32_bf16 a[52:55], v[100:103], v[116:119], a[52:55]\n\t"
      "v_mfma_f32_16x16x32_bf16 a[72:75], v[100:103], v[120:123], a[72:75]\n\t"
      "buffer_load_dwordx4 v[64:67], v0, s[36:39], s48 offen offset:3072 sc1\n\t"
      "v_mfma_f32_16x16x32_bf16 a[16:19], v[104:107], v[108:111], a[16:19]\n\t"
      "v_mfma_f32_16x16x32_bf16 a[36:39], v[104:107], v[112:115], a[36:39]\n\t"
      "buffer_load_dwordx4 v[68:71], v1, s[36:39], s48 offen sc1\n\t"
      "v_mfma_f32_16x16x32_bf16 a[56:59], v[104:107], v[116:119], a[56:59]\n\t"
      "v_mfma_f32_16x16x32_bf16 a[76:79], v[104:107], v[120:123], a[76:79]\n\t"
      "s_add_u32 s50, s51, 46\n\t"
      "s_mul_i32 s48, s50, 0x1400\n\t"
      "s_add_u32 s48, s48, s54\n\t"
      "s_lshl_b32 s49, s50, 12\n\t"
      "s_add_u32 s49, s49, s52\n\t"
      "s_waitcnt vmcnt(27)\n\t"
      "v_mfma_f32_16x16x32_bf16 a[0:3], v[124:127], v[144:147], a[0:3]\n\t"
      "v_mfma_f32_16x16x32_bf16 a[20:23], v[124:127], v[148:151], a[20:23]\n\t"
      "buffer_load_dwordx4 v[108:111], v0, s[36:39], s49 offen sc1\n\t"
      "v_mfma_f32_16x16x32_bf16 a[40:43], v[124:127], v[152:155], a[40:43]\n\t"
      "v_mfma_f32_16x16x32_bf16 a[60:63], v[124:127], v[156:159], a[60:63]\n\t"
      "buffer_load_dwordx4 v[112:115], v0, s[36:39], s49 offen offset:1024 sc1\n\t"
      "v_mfma_f32_16x16x32_bf16 a[4:7], v[128:131], v[144:147], a[4:7]\n\t"
      "v_mfma_f32_16x16x32_bf16 a[24:27], v[128:131], v[148:151], a[24:27]\n\t"
      "buffer_load_dwordx4 v[116:119], v0, s[36:39], s49 offen offset:2048 sc1\n\t"
      "v_mfma_f32_16x16x32_bf16 a[44:47], v[128:131], v[152:155], a[44:47]\n\t"
      "v_mfma_f32_16x16x32_bf16 a[64:67], v[128:131], v[156:159], a[64:67]\n\t"
      "buffer_load_dwordx4 v[120:123], v0, s[36:39], s49 offen offset:3072 sc1\n\t"
      "v_mfma_f32_16x16x32_bf16 a[8:11], v[132:135], v[144:147], a[8:11]\n\t"
      "v_mfma_f32_16x16x32_bf16 a[28:31], v[132:135], v[148:151], a[28:31]\n\t"
      "buffer_load_dwordx4 v[88:91], v0, s[36:39], s48 offen sc1\n\t"
      "v_mfma_f32_16x16x32_bf16 a[48:51], v[132:135], v[152:155], a[48:51]\n\t"
      "v_mfma_f32_16x16x32_bf16 a[68:71], v[132:135], v[156:159], a[68:71]\n\t"
      "buffer_load_dwordx4 v[92:95], v0, s[36:39], s48 offen offset:1024 sc1\n\t"
      "v_mfma_f32_16x16x32_bf16 a[12:15], v[136:139], v[144:147], a[12:15]\n\t"
      "v_mfma_f32_16x16x32_bf16 a[32:35], v[136:139], v[148:151], a[32:35]\n\t"
      "buffer_load_dwordx4 v[96:99], v0, s[36:39], s48 offen offset:2048 sc1\n\t"
      "v_mfma_f32_16x16x32_bf16 a[52:55], v[136:139], v[152:155], a[52:55]\n\t"
      "v_mfma_f32_16x16x32_bf16 a[72:75], v[136:139], v[156:159], a[72:75]\n\t"
      "buffer_load_dwordx4 v[100:103], v0, s[36:39], s48 offen offset:3072 sc1\n\t"
      "v_mfma_f32_16x16x32_bf16 a[16:19], v[140:143], v[144:147], a[16:19]\n\t"
      "v_mfma_f32_16x16x32_bf16 a[36:39], v[140:143], v[148:151], a[36:39]\n\t"
      "buffer_load_dwordx4 v[104:107], v1, s[36:39], s48 offen sc1\n\t"
      "v_mfma_f32_16x16x32_bf16 a[56:59], v[140:143], v[152:155], a[56:59]\n\t"
      "v_mfma_f32_16x16x32_bf16 a[76:79], v[140:143], v[156:159], a[76:79]\n\t"
      "s_add_u32 s50, s51, 50\n\t"
      "s_mul_i32 s48, s50, 0x1400\n\t"
      "s_add_u32 s48, s48, s54\n\t"
      "s_lshl_b32 s49, s50, 12\n\t"
      "s_add_u32 s49, s49, s52\n\t"
      "s_waitcnt vmcnt(27)\n\t"
      "v_mfma_f32_16x16x32_bf16 a[0:3], v[160:163], v[180:183], a[0:3]\n\t"
      "v_mfma_f32_16x16x32_bf16 a[20:23], v[160:163], v[184:187], a[20:23]\n\t"
      "buffer_load_dwordx4 v[144:147], v0, s[36:39], s49 offen sc1\n\t"
      "v_mfma_f32_16x16x32_bf16 a[40:43], v[160:163], v[188:191], a[40:43]\n\t"
      "v_mfma_f32_16x16x32_bf16 a[60:63], v[160:163], v[192:195], a[60:63]\n\t"
      "buffer_load_dwordx4 v[148:151], v0, s[36:39], s49 offen offset:1024 sc1\n\t"
      "v_mfma_f32_16x16x32_bf16 a[4:7], v[164:167], v[180:183], a[4:7]\n\t"
      "v_mfma_f32_16x16x32_bf16 a[24:27], v[164:167], v[184:187], a[24:27]\n\t"
      "buffer_load_dwordx4 v[152:155], v0, s[36:39], s49 offen offset:2048 sc1\n\t"
      "v_mfma_f32_16x16x32_bf16 a[44:47], v[164:167], v[188:191], a[44:47]\n\t"
      "v_mfma_f32_16x16x32_bf16 a[64:67], v[164:167], v[192:195], a[64:67]\n\t"
      "buffer_load_dwordx4 v[156:159], v0, s[36:39], s49 offen offset:3072 sc1\n\t"
      "v_mfma_f32_16x16x32_bf16 a[8:11], v[168:171], v[180:183], a[8:11]\n\t"
      "v_mfma_f32_16x16x32_bf16 a[28:31], v[168:171], v[184:187], a[28:31]\n\t"
      "buffer_load_dwordx4 v[124:127], v0, s[36:39], s48 offen sc1\n\t"
      "v_mfma_f32_16x16x32_bf16 a[48:51], v[168:171], v[188:191], a[48:51]\n\t"
      "v_mfma_f32_16x16x32_bf16 a[68:71], v[168:171], v[192:195], a[68:71]\n\t"
      "buffer_load_dwordx4 v[128:131], v0, s[36:39], s48 offen offset:1024 sc1\n\t"
      "v_mfma_f32_16x16x32_bf16 a[12:15], v[172:175], v[180:183], a[12:15]\n\t"
      "v_mfma_f32_16x16x32_bf16 a[32:35], v[172:175], v[184:187], a[32:35]\n\t"
      "buffer_load_dwordx4 v[132:135], v0, s[36:39], s48 offen offset:2048 sc1\n\t"
      "v_mfma_f32_16x16x32_bf16 a[52:55], v[172:175], v[188:191], a[52:55]\n\t"
      "v_mfma_f32_16x16x32_bf16 a[72:75], v[172:175], v[192:195], a[72:75]\n\t"
      "buffer_load_dwordx4 v[136:139], v0, s[36:39], s48 offen offset:3072 sc1\n\t"
      "v_mfma_f32_16x16x32_bf16 a[16:19], v[176:179], v[180:183], a[16:19]\n\t"
      "v_mfma_f32_16x16x32_bf16 a[36:39], v[176:179], v[184:187], a[36:39]\n\t"
      "buffer_load_dwordx4 v[140:143], v1, s[36:39], s48 offen sc1\n\t"
      "v_mfma_f32_16x16x32_bf16 a[56:59], v[176:179], v[188:191], a[56:59]\n\t"
      "v_mfma_f32_16x16x32_bf16 a[76:79], v[176:179], v[192:195], a[76:79]\n\t"
      "s_waitcnt vmcnt(27)\n\t"
      "s_mov_b32 s58, 0\n\t"
      "v_cmp_ne_u32 vcc, s56, v12\n\t"
      "v_cmp_ne_u32 s[60:61], s56, v4\n\t"
      "s_nop 4\n\t"
      "s_or_b64 vcc, vcc, s[60:61]\n\t"
      "v_cmp_ne_u32 s[60:61], s56, v5\n\t"
      "s_nop 4\n\t"
      "s_or_b64 vcc, vcc, s[60:61]\n\t"
      "s_nop 1\n\t"
      "s_cbranch_vccz K2pbdm\n\t"
      "s_sleep 2\n\t"
      "s_add_u32 s58, s58, 1\n\t"
      "s_cmp_lt_u32 s58, 0x40000\n\t"
      "s_cbranch_scc1 K2pcm\n\t"
      "K2pbdm:\n\t"
      "s_branch K2pem\n\t"
      "K2pcm:\n\t"
      "buffer_load_dword v12, v11, s[36:39], s59 offen sc1\n\t"
      "buffer_load_dwordx2 v[4:5], v6, s[36:39], s62 offen sc1\n\t"
      "s_waitcnt vmcnt(0)\n\t"
      "v_cmp_ne_u32 vcc, s56, v12\n\t"
      "v_cmp_ne_u32 s[60:61], s56, v4\n\t"
      "s_nop 4\n\t"
      "s_or_b64 vcc, vcc, s[60:61]\n\t"
      "v_cmp_ne_u32 s[60:61], s56, v5\n\t"
      "s_nop 4\n\t"
      "s_or_b64 vcc, vcc, s[60:61]\n\t"
      "s_nop 1\n\t"
      "s_cbranch_vccz K2pfdm\n\t"
      "s_sleep 2\n\t"
      "s_add_u32 s58, s58, 1\n\t"
      "s_cmp_lt_u32 s58, 0x40000\n\t"
      "s_cbranch_scc1 K2pcm\n\t"
      "K2pfdm:\n\t"
      "K2pem:\n\t"
      "s_add_u32 s50, s51, 0\n\t"
      "s_lshl_b32 s49, s50, 12\n\t"
      "s_add_u32 s49, s49, s53\n\t"
      "s_add_u32 s50, s50, 54\n\t"
      "s_mul_i32 s48, s50, 0x1400\n\t"
      "s_add_u32 s48, s48, s54\n\t"
      "s_waitcnt vmcnt(27)\n\t"
      "v_mfma_f32_16x16x32_bf16 a[0:3], v[16:19], v[36:39], a[0:3]\n\t"
      "v_mfma_f32_16x16x32_bf16 a[20:23], v[16:19], v[40:43], a[20:23]\n\t"
      "buffer_load_dwordx4 v[180:183], v0, s[36:39], s49 offen sc1\n\t"
      "v_mfma_f32_16x16x32_bf16 a[40:43], v[16:19], v[44:47], a[40:43]\n\t"
      "v_mfma_f32_16x16x32_bf16 a[60:63], v[16:19], v[48:51], a[60:63]\n\t"
      "buffer_load_dwordx4 v[184:187], v0, s[36:39], s49 offen offset:1024 sc1\n\t"
      "v_mfma_f32_16x16x32_bf16 a[4:7], v[20:23], v[36:39], a[4:7]\n\t"
      "v_mfma_f32_16x16x32_bf16 a[24:27], v[20:23], v[40:43], a[24:27]\n\t"
      "buffer_load_dwordx4 v[188:191], v0, s[36:39], s49 offen offset:2048 sc1\n\t"
      "v_mfma_f32_16x16x32_bf16 a[44:47], v[20:23], v[44:47], a[44:47]\n\t"
      "v_mfma_f32_16x16x32_bf16 a[64:67], v[20:23], v[48:51], a[64:67]\n\t"
      "buffer_load_dwordx4 v[192:195], v0, s[36:39], s49 offen offset:3072 sc1\n\t"
      "v_mfma_f32_16x16x32_bf16 a[8:11], v[24:27], v[36:39], a[8:11]\n\t"
      "v_mfma_f32_16x16x32_bf16 a[28:31], v[24:27], v[40:43], a[28:31]\n\t"
      "buffer_load_dwordx4 v[160:163], v0, s[36:39], s48 offen sc1\n\t"
      "v_mfma_f32_16x16x32_bf16 a[48:51], v[24:27], v[44:47], a[48:51]\n\t"
      "v_mfma_f32_16x16x32_bf16 a[68:71], v[24:27], v[48:51], a[68:71]\n\t"
      "buffer_load_dwordx4 v[164:167], v0, s[36:39], s48 offen offset:1024 sc1\n\t"
      "v_mfma_f32_16x16x32_bf16 a[12:15], v[28:31], v[36:39], a[12:15]\n\t"
      "v_mfma_f32_16x16x32_bf16 a[32:35], v[28:31], v[40:43], a[32:35]\n\t"
      "buffer_load_dwordx4 v[168:171], v0, s[36:39], s48 offen offset:2048 sc1\n\t"
      "v_mfma_f32_16x16x32_bf16 a[52:55], v[28:31], v[44:47], a[52:55]\n\t"
      "v_mfma_f32_16x16x32_bf16 a[72:75], v[28:31], v[48:51], a[72:75]\n\t"
      "buffer_load_dwordx4 v[172:175], v0, s[36:39], s48 offen offset:3072 sc1\n\t"
      "v_mfma_f32_16x16x32_bf16 a[16:19], v[32:35], v[36:39], a[16:19]\n\t"
      "v_mfma_f32_16x16x32_bf16 a[36:39], v[32:35], v[40:43], a[36:39]\n\t"
      "buffer_load_dwordx4 v[176:179], v1, s[36:39], s48 offen sc1\n\t"
      "v_mfma_f32_16x16x32_bf16 a[56:59], v[32:35], v[44:47], a[56:59]\n\t"
      "v_mfma_f32_16x16x32_bf16 a[76:79], v[32:35], v[48:51], a[76:79]\n\t"
      "s_add_u32 s50, s51, 4\n\t"
      "s_lshl_b32 s49, s50, 12\n\t"
      "s_add_u32 s49, s49, s53\n\t"
      "s_add_u32 s50, s50, 54\n\t"
      "s_mul_i32 s48, s50, 0x1400\n\t"
      "s_add_u32 s48, s48, s54\n\t"
      "s_waitcnt vmcnt(27)\n\t"
      "v_mfma_f32_16x16x32_bf16 a[0:3], v[52:55], v[72:75], a[0:3]\n\t"
      "v_mfma_f32_16x16x32_bf16 a[20:23], v[52:55], v[76:79], a[20:23]\n\t"
      "buffer_load_dwordx4 v[36:39], v0, s[36:39], s49 offen sc1\n\t"
      "v_mfma_f32_16x16x32_bf16 a[40:43], v[52:55], v[80:83], a[40:43]\n\t"
      "v_mfma_f32_16x16x32_bf16 a[60:63], v[52:55], v[84:87], a[60:63]\n\t"
      "buffer_load_dwordx4 v[40:43], v0, s[36:39], s49 offen offset:1024 sc1\n\t"
      "v_mfma_f32_16x16x32_bf16 a[4:7], v[56:59], v[72:75], a[4:7]\n\t"
      "v_mfma_f32_16x16x32_bf16 a[24:27], v[56:59], v[76:79], a[24:27]\n\t"
      "buffer_load_dwordx4 v[44:47], v0, s[36:39], s49 offen offset:2048 sc1\n\t"
      "v_mfma_f32_16x16x32_bf16 a[44:47], v[56:59], v[80:83], a[44:47]\n\t"
      "v_mfma_f32_16x16x32_bf16 a[64:67], v[56:59], v[84:87], a[64:67]\n\t"
      "buffer_load_dwordx4 v[48:51], v0, s[36:39], s49 offen offset:3072 sc1\n\t"
      "v_mfma_f32_16x16x32_bf16 a[8:11], v[60:63], v[72:75], a[8:11]\n\t"
      "v_mfma_f32_16x16x32_bf16 a[28:31], v[60:63], v[76:79], a[28:31]\n\t"
      "buffer_load_dwordx4 v[16:19], v0, s[36:39], s48 offen sc1\n\t"
      "v_mfma_f32_16x16x32_bf16 a[48:51], v[60:63], v[80:83], a[48:51]\n\t"
      "v_mfma_f32_16x16x32_bf16 a[68:71], v[60:63], v[84:87], a[68:71]\n\t"
      "buffer_load_dwordx4 v[20:23], v0, s[36:39], s48 offen offset:1024 sc1\n\t"
      "v_mfma_f32_16x16x32_bf16 a[12:15], v[64:67], v[72:75], a[12:15]\n\t"
      "v_mfma_f32_16x16x32_bf16 a[32:35], v[64:67], v[76:79], a[32:35]\n\t"
      "buffer_load_dwordx4 v[24:27], v0, s[36:39], s48 offen offset:2048 sc1\n\t"
      "v_mfma_f32_16x16x32_bf16 a[52:55], v[64:67], v[80:83], a[52:55]\n\t"
      "v_mfma_f32_16x16x32_bf16 a[72:75], v[64:67], v[84:87], a[72:75]\n\t"
      "buffer_load_dwordx4 v[28:31], v0, s[36:39], s48 offen offset:3072 sc1\n\t"
      "v_mfma_f32_16x16x32_bf16 a[16:19], v[68:71], v[72:75], a[16:19]\n\t"
      "v_mfma_f32_16x16x32_bf16 a[36:39], v[68:71], v[76:79], a[36:39]\n\t"
      "buffer_load_dwordx4 v[32:35], v1, s[36:39], s48 offen sc1\n\t"
      "v_mfma_f32_16x16x32_bf16 a[56:59], v[68:71], v[80:83], a[56:59]\n\t"
      "v_mfma_f32_16x16x32_bf16 a[76:79], v[68:71], v[84:87], a[76:79]\n\t"
      "s_add_u32 s50, s51, 8\n\t"
      "s_lshl_b32 s49, s50, 12\n\t"
      "s_add_u32 s49, s49, s53\n\t"
      "s_add_u32 s50, s50, 54\n\t"
      "s_mul_i32 s48, s50, 0x1400\n\t"
      "s_add_u32 s48, s48, s54\n\t"
      "s_waitcnt vmcnt(27)\n\t"
      "v_mfma_f32_16x16x32_bf16 a[0:3], v[88:91], v[108:111], a[0:3]\n\t"
      "v_mfma_f32_16x16x32_bf16 a[20:23], v[88:91], v[112:115], a[20:23]\n\t"
      "buffer_load_dwordx4 v[72:75], v0, s[36:39], s49 offen sc1\n\t"
      "v_mfma_f32_16x16x32_bf16 a[40:43], v[88:91], v[116:119], a[40:43]\n\t"
      "v_mfma_f32_16x16x32_bf16 a[60:63], v[88:91], v[120:123], a[60:63]\n\t"
      "buffer_load_dwordx4 v[76:79], v0, s[36:39], s49 offen offset:1024 sc1\n\t"
      "v_mfma_f32_16x16x32_bf16 a[4:7], v[92:95], v[108:111], a[4:7]\n\t"
      "v_mfma_f32_16x16x32_bf16 a[24:27], v[92:95], v[112:115], a[24:27]\n\t"
      "buffer_load_dwordx4 v[80:83], v0, s[36:39], s49 offen offset:2048 sc1\n\t"
      "v_mfma_f32_16x16x32_bf16 a[44:47], v[92:95], v[116:119], a[44:47]\n\t"
      "v_mfma_f32_16x16x32_bf16 a[64:67], v[92:95], v[120:123], a[64:67]\n\t"
      "buffer_load_dwordx4 v[84:87], v0, s[36:39], s49 offen offset:3072 sc1\n\t"
      "v_mfma_f32_16x16x32_bf16 a[8:11], v[96:99], v[108:111], a[8:11]\n\t"
      "v_mfma_f32_16x16x32_bf16 a[28:31], v[96:99], v[112:115], a[28:31]\n\t"
      "buffer_load_dwordx4 v[52:55], v0, s[36:39], s48 offen sc1\n\t"
      "v_mfma_f32_16x16x32_bf16 a[48:51], v[96:99], v[116:119], a[48:51]\n\t"
      "v_mfma_f32_16x16x32_bf16 a[68:71], v[96:99], v[120:123], a[68:71]\n\t"
      "buffer_load_dwordx4 v[56:59], v0, s[36:39], s48 offen offset:1024 sc1\n\t"
      "v_mfma_f32_16x16x32_bf16 a[12:15], v[100:103], v[108:111], a[12:15]\n\t"
      "v_mfma_f32_16x16x32_bf16 a[32:35], v[100:103], v[112:115], a[32:35]\n\t"
      "buffer_load_dwordx4 v[60:63], v0, s[36:39], s48 offen offset:2048 sc1\n\t"
      "v_mfma_f32_16x16x32_bf16 a[52:55], v[100:103], v[116:119], a[52:55]\n\t"
      "v_mfma_f32_16x16x32_bf16 a[72:75], v[100:103], v[120:123], a[72:75]\n\t"
      "buffer_load_dwordx4 v[64:67], v0, s[36:39], s48 offen offset:3072 sc1\n\t"
      "v_mfma_f32_16x16x32_bf16 a[16:19], v[104:107], v[108:111], a[16:19]\n\t"
      "v_mfma_f32_16x16x32_bf16 a[36:39], v[104:107], v[112:115], a[36:39]\n\t"
      "buffer_load_dwordx4 v[68:71], v1, s[36:39], s48 offen sc1\n\t"
      "v_mfma_f32_16x16x32_bf16 a[56:59], v[104:107], v[116:119], a[56:59]\n\t"
      "v_mfma_f32_16x16x32_bf16 a[76:79], v[104:107], v[120:123], a[76:79]\n\t"
      "s_add_u32 s50, s51, 12\n\t"
      "s_lshl_b32 s49, s50, 12\n\t"
      "s_add_u32 s49, s49, s53\n\t"
      "s_add_u32 s50, s50, 54\n\t"
      "s_mul_i32 s48, s50, 0x1400\n\t"
      "s_add_u32 s48, s48, s54\n\t"
      "s_waitcnt vmcnt(27)\n\t"
      "v_mfma_f32_16x16x32_bf16 a[0:3], v[124:127], v[144:147], a[0:3]\n\t"
      "v_mfma_f32_16x16x32_bf16 a[20:23], v[124:127], v[148:151], a[20:23]\n\t"
      "buffer_load_dwordx4 v[108:111], v0, s[36:39], s49 offen sc1\n\t"
      "v_mfma_f32_16x16x32_bf16 a[40:43], v[124:127], v[152:155], a[40:43]\n\t"
      "v_mfma_f32_16x16x32_bf16 a[60:63], v[124:127], v[156:159], a[60:63]\n\t"
      "buffer_load_dwordx4 v[112:115], v0, s[36:39], s49 offen offset:1024 sc1\n\t"
      "v_mfma_f32_16x16x32_bf16 a[4:7], v[128:131], v[144:147], a[4:7]\n\t"
      "v_mfma_f32_16x16x32_bf16 a[24:27], v[128:131], v[148:151], a[24:27]\n\t"
      "buffer_load_dwordx4 v[116:119], v0, s[36:39], s49 offen offset:2048 sc1\n\t"
      "v_mfma_f32_16x16x32_bf16 a[44:47], v[128:131], v[152:155], a[44:47]\n\t"
      "v_mfma_f32_16x16x32_bf16 a[64:67], v[128:131], v[156:159], a[64:67]\n\t"
      "buffer_load_dwordx4 v[120:123], v0, s[36:39], s49 offen offset:3072 sc1\n\t"
      "v_mfma_f32_16x16x32_bf16 a[8:11], v[132:135], v[144:147], a[8:11]\n\t"
      "v_mfma_f32_16x16x32_bf16 a[28:31], v[132:135], v[148:151], a[28:31]\n\t"
      "buffer_load_dwordx4 v[88:91], v0, s[36:39], s48 offen sc1\n\t"
      "v_mfma_f32_16x16x32_bf16 a[48:51], v[132:135], v[152:155], a[48:51]\n\t"
      "v_mfma_f32_16x16x32_bf16 a[68:71], v[132:135], v[156:159], a[68:71]\n\t"
      "buffer_load_dwordx4 v[92:95], v0, s[36:39], s48 offen offset:1024 sc1\n\t"
      "v_mfma_f32_16x16x32_bf16 a[12:15], v[136:139], v[144:147], a[12:15]\n\t"
      "v_mfma_f32_16x16x32_bf16 a[32:35], v[136:139], v[148:151], a[32:35]\n\t"
      "buffer_load_dwordx4 v[96:99], v0, s[36:39], s48 offen offset:2048 sc1\n\t"
      "v_mfma_f32_16x16x32_bf16 a[52:55], v[136:139], v[152:155], a[52:55]\n\t"
      "v_mfma_f32_16x16x32_bf16 a[72:75], v[136:139], v[156:159], a[72:75]\n\t"
      "buffer_load_dwordx4 v[100:103], v0, s[36:39], s48 offen offset:3072 sc1\n\t"
      "v_mfma_f32_16x16x32_bf16 a[16:19], v[140:143], v[144:147], a[16:19]\n\t"
      "v_mfma_f32_16x16x32_bf16 a[36:39], v[140:143], v[148:151], a[36:39]\n\t"
      "buffer_load_dwordx4 v[104:107], v1, s[36:39], s48 offen sc1\n\t"
      "v_mfma_f32_16x16x32_bf16 a[56:59], v[140:143], v[152:155], a[56:59]\n\t"
      "v_mfma_f32_16x16x32_bf16 a[76:79], v[140:143], v[156:159], a[76:79]\n\t"
      "s_add_u32 s50, s51, 16\n\t"
      "s_lshl_b32 s49, s50, 12\n\t"
      "s_add_u32 s49, s49, s53\n\t"
      "s_add_u32 s50, s50, 54\n\t"
      "s_mul_i32 s48, s50, 0x1400\n\t"
      "s_add_u32 s48, s48, s54\n\t"
      "s_waitcnt vmcnt(27)\n\t"
      "v_mfma_f32_16x16x32_bf16 a[80:83], v[160:163], v[180:183], a[80:83]\n\t"
      "v_mfma_f32_16x16x32_bf16 a[100:103], v[160:163], v[184:187], a[100:103]\n\t"
      "buffer_load_dwordx4 v[144:147], v0, s[36:39], s49 offen sc1\n\t"
      "v_mfma_f32_16x16x32_bf16 a[120:123], v[160:163], v[188:191], a[120:123]\n\t"
      "v_mfma_f32_16x16x32_bf16 a[140:143], v[160:163], v[192:195], a[140:143]\n\t"
      "buffer_load_dwordx4 v[148:151], v0, s[36:39], s49 offen offset:1024 sc1\n\t"
      "v_mfma_f32_16x16x32_bf16 a[84:87], v[164:167], v[180:183], a[84:87]\n\t"
      "v_mfma_f32_16x16x32_bf16 a[104:107], v[164:167], v[184:187], a[104:107]\n\t"
      "buffer_load_dwordx4 v[152:155], v0, s[36:39], s49 offen offset:2048 sc1\n\t"
      "v_mfma_f32_16x16x32_bf16 a[124:127], v[164:167], v[188:191], a[124:127]\n\t"
      "v_mfma_f32_16x16x32_bf16 a[144:147], v[164:167], v[192:195], a[144:147]\n\t"
      "buffer_load_dwordx4 v[156:159], v0, s[36:39], s49 offen offset:3072 sc1\n\t"
      "v_mfma_f32_16x16x32_bf16 a[88:91], v[168:171], v[180:183], a[88:91]\n\t"
      "v_mfma_f32_16x16x32_bf16 a[108:111], v[168:171], v[184:187], a[108:111]\n\t"
      "buffer_load_dwordx4 v[124:127], v0, s[36:39], s48 offen sc1\n\t"
      "v_mfma_f32_16x16x32_bf16 a[128:131], v[168:171], v[188:191], a[128:131]\n\t"
      "v_mfma_f32_16x16x32_bf16 a[148:151], v[168:171], v[192:195], a[148:151]\n\t"
      "buffer_load_dwordx4 v[128:131], v0, s[36:39], s48 offen offset:1024 sc1\n\t"
      "v_mfma_f32_16x16x32_bf16 a[92:95], v[172:175], v[180:183], a[92:95]\n\t"
      "v_mfma_f32_16x16x32_bf16 a[112:115], v[172:175], v[184:187], a[112:115]\n\t"
      "buffer_load_dwordx4 v[132:135], v0, s[36:39], s48 offen offset:2048 sc1\n\t"
      "v_mfma_f32_16x16x32_bf16 a[132:135], v[172:175], v[188:191], a[132:135]\n\t"
      "v_mfma_f32_16x16x32_bf16 a[152:155], v[172:175], v[192:195], a[152:155]\n\t"
      "buffer_load_dwordx4 v[136:139], v0, s[36:39], s48 offen offset:3072 sc1\n\t"
      "v_mfma_f32_16x16x32_bf16 a[96:99], v[176:179], v[180:183], a[96:99]\n\t"
      "v_mfma_f32_16x16x32_bf16 a[116:119], v[176:179], v[184:187], a[116:119]\n\t"
      "buffer_load_dwordx4 v[140:143], v1, s[36:39], s48 offen sc1\n\t"
      "v_mfma_f32_16x16x32_bf16 a[136:139], v[176:179], v[188:191], a[136:139]\n\t"
      "v_mfma_f32_16x16x32_bf16 a[156:159], v[176:179], v[192:195], a[156:159]\n\t"
      "s_add_u32 s50, s51, 20\n\t"
      "s_lshl_b32 s49, s50, 12\n\t"
      "s_add_u32 s49, s49, s53\n\t"
      "s_add_u32 s50, s50, 54\n\t"
      "s_mul_i32 s48, s50, 0x1400\n\t"
      "s_add_u32 s48, s48, s54\n\t"
      "s_waitcnt vmcnt(27)\n\t"
      "v_mfma_f32_16x16x32_bf16 a[80:83], v[16:19], v[36:39], a[80:83]\n\t"
      "ds_write_b128 v8, a[0:3]\n\t"
      "v_mfma_f32_16x16x32_bf16 a[100:103], v[16:19], v[40:43], a[100:103]\n\t"
      "buffer_load_dwordx4 v[180:183], v0, s[36:39], s49 offen sc1\n\t"
      "v_mfma_f32_16x16x32_bf16 a[120:123], v[16:19], v[44:47], a[120:123]\n\t"
      "ds_write_b128 v8, a[4:7] offset:1024\n\t"
      "v_mfma_f32_16x16x32_bf16 a[140:143], v[16:19], v[48:51], a[140:143]\n\t"
      "buffer_load_dwordx4 v[184:187], v0, s[36:39], s49 offen offset:1024 sc1\n\t"
      "v_mfma_f32_16x16x32_bf16 a[84:87], v[20:23], v[36:39], a[84:87]\n\t"
      "ds_write_b128 v8, a[8:11] offset:2048\n\t"
      "v_mfma_f32_16x16x32_bf16 a[104:107], v[20:23], v[40:43], a[104:107]\n\t"
      "buffer_load_dwordx4 v[188:191], v0, s[36:39], s49 offen offset:2048 sc1\n\t"
      "v_mfma_f32_16x16x32_bf16 a[124:127], v[20:23], v[44:47], a[124:127]\n\t"
      "ds_write_b128 v8, a[12:15] offset:3072\n\t"
      "v_mfma_f32_16x16x32_bf16 a[144:147], v[20:23], v[48:51], a[144:147]\n\t"
      "buffer_load_dwordx4 v[192:195], v0, s[36:39], s49 offen offset:3072 sc1\n\t"
      "v_mfma_f32_16x16x32_bf16 a[88:91], v[24:27], v[36:39], a[88:91]\n\t"
      "ds_write_b128 v8, a[16:19] offset:4096\n\t"
      "ds_write_b128 v8, a[20:23] offset:20480\n\t"
      "v_mfma_f32_16x16x32_bf16 a[108:111], v[24:27], v[40:43], a[108:111]\n\t"
      "buffer_load_dwordx4 v[160:163], v0, s[36:39], s48 offen sc1\n\t"
      "v_mfma_f32_16x16x32_bf16 a[128:131], v[24:27], v[44:47], a[128:131]\n\t"
      "ds_write_b128 v8, a[24:27] offset:21504\n\t"
      "ds_write_b128 v8, a[28:31] offset:22528\n\t"
      "v_mfma_f32_16x16x32_bf16 a[148:151], v[24:27], v[48:51], a[148:151]\n\t"
      "buffer_load_dwordx4 v[164:167], v0, s[36:39], s48 offen offset:1024 sc1\n\t"
      "v_mfma_f32_16x16x32_bf16 a[92:95], v[28:31], v[36:39], a[92:95]\n\t"
      "ds_write_b128 v8, a[32:35] offset:23552\n\t"
      "ds_write_b128 v8, a[36:39] offset:24576\n\t"
      "v_mfma_f32_16x16x32_bf16 a[112:115], v[28:31], v[40:43], a[112:115]\n\t"
      "buffer_load_dwordx4 v[168:171], v0, s[36:39], s48 offen offset:2048 sc1\n\t"
      "v_mfma_f32_16x16x32_bf16 a[132:135], v[28:31], v[44:47], a[132:135]\n\t"
      "ds_write_b128 v14, a[40:43]\n\t"
      "ds_write_b128 v14, a[44:47] offset:1024\n\t"
      "v_mfma_f32_16x16x32_bf16 a[152:155], v[28:31], v[48:51], a[152:155]\n\t"
      "buffer_load_dwordx4 v[172:175], v0, s[36:39], s48 offen offset:3072 sc1\n\t"
      "v_mfma_f32_16x16x32_bf16 a[96:99], v[32:35], v[36:39], a[96:99]\n\t"
      "ds_write_b128 v14, a[48:51] offset:2048\n\t"
      "ds_write_b128 v14, a[52:55] offset:3072\n\t"
      "v_mfma_f32_16x16x32_bf16 a[116:119], v[32:35], v[40:43], a[116:119]\n\t"
      "buffer_load_dwordx4 v[176:179], v1, s[36:39], s48 offen sc1\n\t"
      "v_mfma_f32_16x16x32_bf16 a[136:139], v[32:35], v[44:47], a[136:139]\n\t"
      "ds_write_b128 v14, a[56:59] offset:4096\n\t"
      "ds_write_b128 v14, a[60:63] offset:20480\n\t"
      "v_mfma_f32_16x16x32_bf16 a[156:159], v[32:35], v[48:51], a[156:159]\n\t"
      "ds_write_b128 v14, a[64:67] offset:21504\n\t"
      "ds_write_b128 v14, a[68:71] offset:22528\n\t"
      "ds_write_b128 v14, a[72:75] offset:23552\n\t"
      "ds_write_b128 v14, a[76:79] offset:24576\n\t"
      "s_add_u32 s50, s51, 24\n\t"
      "s_lshl_b32 s49, s50, 12\n\t"
      "s_add_u32 s49, s49, s53\n\t"
      "s_add_u32 s50, s50, 54\n\t"
      "s_mul_i32 s48, s50, 0x1400\n\t"
      "s_add_u32 s48, s48, s54\n\t"
      "s_waitcnt vmcnt(27)\n\t"
      "v_mfma_f32_16x16x32_bf16 a[80:83], v[52:55], v[72:75], a[80:83]\n\t"
      "v_mfma_f32_16x16x32_bf16 a[100:103], v[52:55], v[76:79], a[100:103]\n\t"
      "buffer_load_dwordx4 v[36:39], v0, s[36:39], s49 offen sc1\n\t"
      "v_mfma_f32_16x16x32_bf16 a[120:123], v[52:55], v[80:83], a[120:123]\n\t"
      "v_mfma_f32_16x16x32_bf16 a[140:143], v[52:55], v[84:87], a[140:143]\n\t"
      "buffer_load_dwordx4 v[40:43], v0, s[36:39], s49 offen offset:1024 sc1\n\t"
      "v_mfma_f32_16x16x32_bf16 a[84:87], v[56:59], v[72:75], a[84:87]\n\t"
      "v_mfma_f32_16x16x32_bf16 a[104:107], v[56:59], v[76:79], a[104:107]\n\t"
      "buffer_load_dwordx4 v[44:47], v0, s[36:39], s49 offen offset:2048 sc1\n\t"
      "v_mfma_f32_16x16x32_bf16 a[124:127], v[56:59], v[80:83], a[124:127]\n\t"
      "v_mfma_f32_16x16x32_bf16 a[144:147], v[56:59], v[84:87], a[144:147]\n\t"
      "buffer_load_dwordx4 v[48:51], v0, s[36:39], s49 offen offset:3072 sc1\n\t"
      "v_mfma_f32_16x16x32_bf16 a[88:91], v[60:63], v[72:75], a[88:91]\n\t"
      "v_mfma_f32_16x16x32_bf16 a[108:111], v[60:63], v[76:79], a[108:111]\n\t"
      "buffer_load_dwordx4 v[16:19], v0, s[36:39], s48 offen sc1\n\t"
      "v_mfma_f32_16x16x32_bf16 a[128:131], v[60:63], v[80:83], a[128:131]\n\t"
      "v_mfma_f32_16x16x32_bf16 a[148:151], v[60:63], v[84:87], a[148:151]\n\t"
      "buffer_load_dwordx4 v[20:23], v0, s[36:39], s48 offen offset:1024 sc1\n\t"
      "v_mfma_f32_16x16x32_bf16 a[92:95], v[64:67], v[72:75], a[92:95]\n\t"
      "v_mfma_f32_16x16x32_bf16 a[112:115], v[64:67], v[76:79], a[112:115]\n\t"
      "buffer_load_dwordx4 v[24:27], v0, s[36:39], s48 offen offset:2048 sc1\n\t"
      "v_mfma_f32_16x16x32_bf16 a[132:135], v[64:67], v[80:83], a[132:135]\n\t"
      "v_mfma_f32_16x16x32_bf16 a[152:155], v[64:67], v[84:87], a[152:155]\n\t"
      "buffer_load_dwordx4 v[28:31], v0, s[36:39], s48 offen offset:3072 sc1\n\t"
      "v_mfma_f32_16x16x32_bf16 a[96:99], v[68:71], v[72:75], a[96:99]\n\t"
      "v_mfma_f32_16x16x32_bf16 a[116:119], v[68:71], v[76:79], a[116:119]\n\t"
      "buffer_load_dwordx4 v[32:35], v1, s[36:39], s48 offen sc1\n\t"
      "v_mfma_f32_16x16x32_bf16 a[136:139], v[68:71], v[80:83], a[136:139]\n\t"
      "v_mfma_f32_16x16x32_bf16 a[156:159], v[68:71], v[84:87], a[156:159]\n\t"
      "s_add_u32 s50, s51, 28\n\t"
      "s_lshl_b32 s49, s50, 12\n\t"
      "s_add_u32 s49, s49, s53\n\t"
      "s_add_u32 s50, s50, 54\n\t"
      "s_mul_i32 s48, s50, 0x1400\n\t"
      "s_add_u32 s48, s48, s54\n\t"
      "s_waitcnt vmcnt(27)\n\t"
      "v_mfma_f32_16x16x32_bf16 a[80:83], v[88:91], v[108:111], a[80:83]\n\t"
      "v_mfma_f32_16x16x32_bf16 a[100:103], v[88:91], v[112:115], a[100:103]\n\t"
      "buffer_load_dwordx4 v[72:75], v0, s[36:39], s49 offen sc1\n\t"
      "v_mfma_f32_16x16x32_bf16 a[120:123], v[88:91], v[116:119], a[120:123]\n\t"
      "v_mfma_f32_16x16x32_bf16 a[140:143], v[88:91], v[120:123], a[140:143]\n\t"
      "buffer_load_dwordx4 v[76:79], v0, s[36:39], s49 offen offset:1024 sc1\n\t"
      "v_mfma_f32_16x16x32_bf16 a[84:87], v[92:95], v[108:111], a[84:87]\n\t"
      "v_mfma_f32_16x16x32_bf16 a[104:107], v[92:95], v[112:115], a[104:107]\n\t"
      "buffer_load_dwordx4 v[80:83], v0, s[36:39], s49 offen offset:2048 sc1\n\t"
      "v_mfma_f32_16x16x32_bf16 a[124:127], v[92:95], v[116:119], a[124:127]\n\t"
      "v_mfma_f32_16x16x32_bf16 a[144:147], v[92:95], v[120:123], a[144:147]\n\t"
      "buffer_load_dwordx4 v[84:87], v0, s[36:39], s49 offen offset:3072 sc1\n\t"
      "v_mfma_f32_16x16x32_bf16 a[88:91], v[96:99], v[108:111], a[88:91]\n\t"
      "v_mfma_f32_16x16x32_bf16 a[108:111], v[96:99], v[112:115], a[108:111]\n\t"
      "buffer_load_dwordx4 v[52:55], v0, s[36:39], s48 offen sc1\n\t"
      "v_mfma_f32_16x16x32_bf16 a[128:131], v[96:99], v[116:119], a[128:131]\n\t"
      "v_mfma_f32_16x16x32_bf16 a[148:151], v[96:99], v[120:123], a[148:151]\n\t"
      "buffer_load_dwordx4 v[56:59], v0, s[36:39], s48 offen offset:1024 sc1\n\t"
      "v_mfma_f32_16x16x32_bf16 a[92:95], v[100:103], v[108:111], a[92:95]\n\t"
      "v_mfma_f32_16x16x32_bf16 a[112:115], v[100:103], v[112:115], a[112:115]\n\t"
      "buffer_load_dwordx4 v[60:63], v0, s[36:39], s48 offen offset:2048 sc1\n\t"
      "v_mfma_f32_16x16x32_bf16 a[132:135], v[100:103], v[116:119], a[132:135]\n\t"
      "v_mfma_f32_16x16x32_bf16 a[152:155], v[100:103], v[120:123], a[152:155]\n\t"
      "buffer_load_dwordx4 v[64:67], v0, s[36:39], s48 offen offset:3072 sc1\n\t"
      "v_mfma_f32_16x16x32_bf16 a[96:99], v[104:107], v[108:111], a[96:99]\n\t"
      "v_mfma_f32_16x16x32_bf16 a[116:119], v[104:107], v[112:115], a[116:119]\n\t"
      "buffer_load_dwordx4 v[68:71], v1, s[36:39], s48 offen sc1\n\t"
      "v_mfma_f32_16x16x32_bf16 a[136:139], v[104:107], v[116:119], a[136:139]\n\t"
      "v_mfma_f32_16x16x32_bf16 a[156:159], v[104:107], v[120:123], a[156:159]\n\t"
      "s_add_u32 s50, s51, 32\n\t"
      "s_lshl_b32 s49, s50, 12\n\t"
      "s_add_u32 s49, s49, s53\n\t"
      "s_add_u32 s50, s50, 54\n\t"
      "s_mul_i32 s48, s50, 0x1400\n\t"
      "s_add_u32 s48, s48, s54\n\t"
      "s_waitcnt vmcnt(27)\n\t"
      "v_mfma_f32_16x16x32_bf16 a[80:83], v[124:127], v[144:147], a[80:83]\n\t"
      "s_waitcnt lgkmcnt(0)\n\t"
      "v_mfma_f32_16x16x32_bf16 a[100:103], v[124:127], v[148:151], a[100:103]\n\t"
      "buffer_load_dwordx4 v[108:111], v0, s[36:39], s49 offen sc1\n\t"
      "v_mfma_f32_16x16x32_bf16 a[120:123], v[124:127], v[152:155], a[120:123]\n\t"
      "s_barrier\n\t"
      "v_mfma_f32_16x16x32_bf16 a[140:143], v[124:127], v[156:159], a[140:143]\n\t"
      "buffer_load_dwordx4 v[112:115], v0, s[36:39], s49 offen offset:1024 sc1\n\t"
      "v_mfma_f32_16x16x32_bf16 a[84:87], v[128:131], v[144:147], a[84:87]\n\t"
      "ds_read_b128 v[196:199], v9\n\t"
      "v_mfma_f32_16x16x32_bf16 a[104:107], v[128:131], v[148:151], a[104:107]\n\t"
      "buffer_load_dwordx4 v[116:119], v0, s[36:39], s49 offen offset:2048 sc1\n\t"
      "v_mfma_f32_16x16x32_bf16 a[124:127], v[128:131], v[152:155], a[124:127]\n\t"
      "ds_read_b128 v[200:203], v9 offset:1024\n\t"
      "v_mfma_f32_16x16x32_bf16 a[144:147], v[128:131], v[156:159], a[144:147]\n\t"
      "buffer_load_dwordx4 v[120:123], v0, s[36:39], s49 offen offset:3072 sc1\n\t"
      "v_mfma_f32_16x16x32_bf16 a[88:91], v[132:135], v[144:147], a[88:91]\n\t"
      "ds_read_b128 v[204:207], v9 offset:2048\n\t"
      "ds_read_b128 v[208:211], v9 offset:3072\n\t"
      "v_mfma_f32_16x16x32_bf16 a[108:111], v[132:135], v[148:151], a[108:111]\n\t"
      "buffer_load_dwordx4 v[88:91], v0, s[36:39], s48 offen sc1\n\t"
      "v_mfma_f32_16x16x32_bf16 a[128:131], v[132:135], v[152:155], a[128:131]\n\t"
      "ds_read_b128 v[212:215], v9 offset:4096\n\t"
      "v_mfma_f32_16x16x32_bf16 a[148:151], v[132:135], v[156:159], a[148:151]\n\t"
      "buffer_load_dwordx4 v[92:95], v0, s[36:39], s48 offen offset:1024 sc1\n\t"
      "v_mfma_f32_16x16x32_bf16 a[92:95], v[136:139], v[144:147], a[92:95]\n\t"
      "v_mfma_f32_16x16x32_bf16 a[112:115], v[136:139], v[148:151], a[112:115]\n\t"
      "buffer_load_dwordx4 v[96:99], v0, s[36:39], s48 offen offset:2048 sc1\n\t"
      "v_mfma_f32_16x16x32_bf16 a[132:135], v[136:139], v[152:155], a[132:135]\n\t"
      "v_mfma_f32_16x16x32_bf16 a[152:155], v[136:139], v[156:159], a[152:155]\n\t"
      "buffer_load_dwordx4 v[100:103], v0, s[36:39], s48 offen offset:3072 sc1\n\t"
      "v_mfma_f32_16x16x32_bf16 a[96:99], v[140:143], v[144:147], a[96:99]\n\t"
      "v_mfma_f32_16x16x32_bf16 a[116:119], v[140:143], v[148:151], a[116:119]\n\t"
      "buffer_load_dwordx4 v[104:107], v1, s[36:39], s48 offen sc1\n\t"
      "v_mfma_f32_16x16x32_bf16 a[136:139], v[140:143], v[152:155], a[136:139]\n\t"
      "v_mfma_f32_16x16x32_bf16 a[156:159], v[140:143], v[156:159], a[156:159]\n\t"
      "s_add_u32 s50, s51, 36\n\t"
      "s_lshl_b32 s49, s50, 12\n\t"
      "s_add_u32 s49, s49, s53\n\t"
      "s_add_u32 s50, s50, 54\n\t"
      "s_mul_i32 s48, s50, 0x1400\n\t"
      "s_add_u32 s48, s48, s54\n\t"
      "s_waitcnt vmcnt(27)\n\t"
      "v_mfma_f32_16x16x32_bf16 a[80:83], v[160:163], v[180:183], a[80:83]\n\t"
      "ds_read_b128 v[216:219], v9 offset:5120\n\t"
      "v_mfma_f32_16x16x32_bf16 a[100:103], v[160:163], v[184:187], a[100:103]\n\t"
      "buffer_load_dwordx4 v[144:147], v0, s[36:39], s49 offen sc1\n\t"
      "v_mfma_f32_16x16x32_bf16 a[120:123], v[160:163], v[188:191], a[120:123]\n\t"
      "ds_read_b128 v[220:223], v9 offset:6144\n\t"
      "v_mfma_f32_16x16x32_bf16 a[140:143], v[160:163], v[192:195], a[140:143]\n\t"
      "buffer_load_dwordx4 v[148:151], v0, s[36:39], s49 offen offset:1024 sc1\n\t"
      "v_mfma_f32_16x16x32_bf16 a[84:87], v[164:167], v[180:183], a[84:87]\n\t"
      "ds_read_b128 v[224:227], v9 offset:7168\n\t"
      "v_mfma_f32_16x16x32_bf16 a[104:107], v[164:167], v[184:187], a[104:107]\n\t"
      "buffer_load_dwordx4 v[152:155], v0, s[36:39], s49 offen offset:2048 sc1\n\t"
      "v_mfma_f32_16x16x32_bf16 a[124:127], v[164:167], v[188:191], a[124:127]\n\t"
      "ds_read_b128 v[228:231], v9 offset:8192\n\t"
      "v_mfma_f32_16x16x32_bf16 a[144:147], v[164:167], v[192:195], a[144:147]\n\t"
      "buffer_load_dwordx4 v[156:159], v0, s[36:39], s49 offen offset:3072 sc1\n\t"
      "v_mfma_f32_16x16x32_bf16 a[88:91], v[168:171], v[180:183], a[88:91]\n\t"
      "ds_read_b128 v[232:235], v9 offset:9216\n\t"
      "v_mfma_f32_16x16x32_bf16 a[108:111], v[168:171], v[184:187], a[108:111]\n\t"
      "buffer_load_dwordx4 v[124:127], v0, s[36:39], s48 offen sc1\n\t"
      "v_mfma_f32_16x16x32_bf16 a[128:131], v[168:171], v[188:191], a[128:131]\n\t"
      "v_mfma_f32_16x16x32_bf16 a[148:151], v[168:171], v[192:195], a[148:151]\n\t"
      "buffer_load_dwordx4 v[128:131], v0, s[36:39], s48 offen offset:1024 sc1\n\t"
      "v_mfma_f32_16x16x32_bf16 a[92:95], v[172:175], v[180:183], a[92:95]\n\t"
      "v_mfma_f32_16x16x32_bf16 a[112:115], v[172:175], v[184:187], a[112:115]\n\t"
      "buffer_load_dwordx4 v[132:135], v0, s[36:39], s48 offen offset:2048 sc1\n\t"
      "v_mfma_f32_16x16x32_bf16 a[132:135], v[172:175], v[188:191], a[132:135]\n\t"
      "v_mfma_f32_16x16x32_bf16 a[152:155], v[172:175], v[192:195], a[152:155]\n\t"
      "buffer_load_dwordx4 v[136:139], v0, s[36:39], s48 offen offset:3072 sc1\n\t"
      "v_mfma_f32_16x16x32_bf16 a[96:99], v[176:179], v[180:183], a[96:99]\n\t"
      "v_mfma_f32_16x16x32_bf16 a[116:119], v[176:179], v[184:187], a[116:119]\n\t"
      "buffer_load_dwordx4 v[140:143], v1, s[36:39], s48 offen sc1\n\t"
      "v_mfma_f32_16x16x32_bf16 a[136:139], v[176:179], v[188:191], a[136:139]\n\t"
      "v_mfma_f32_16x16x32_bf16 a[156:159], v[176:179], v[192:195], a[156:159]\n\t"
      "s_add_u32 s50, s51, 40\n\t"
      "s_lshl_b32 s49, s50, 12\n\t"
      "s_add_u32 s49, s49, s53\n\t"
      "s_add_u32 s50, s50, 54\n\t"
      "s_mul_i32 s48, s50, 0x1400\n\t"
      "s_add_u32 s48, s48, s54\n\t"
      "s_waitcnt vmcnt(27)\n\t"
      "v_mfma_f32_16x16x32_bf16 a[80:83], v[16:19], v[36:39], a[80:83]\n\t"
      "s_waitcnt lgkmcnt(0)\n\t"
      "v_mfma_f32_16x16x32_bf16 a[100:103], v[16:19], v[40:43], a[100:103]\n\t"
      "buffer_load_dwordx4 v[180:183], v0, s[36:39], s49 offen sc1\n\t"
      "v_mfma_f32_16x16x32_bf16 a[120:123], v[16:19], v[44:47], a[120:123]\n\t"
      "v_pk_add_f32 v[196:197], v[196:197], v[216:217]\n\t"
      "v_mfma_f32_16x16x32_bf16 a[140:143], v[16:19], v[48:51], a[140:143]\n\t"
      "buffer_load_dwordx4 v[184:187], v0, s[36:39], s49 offen offset:1024 sc1\n\t"
      "v_mfma_f32_16x16x32_bf16 a[84:87], v[20:23], v[36:39], a[84:87]\n\t"
      "v_pk_add_f32 v[198:199], v[198:199], v[218:219]\n\t"
      "v_mfma_f32_16x16x32_bf16 a[104:107], v[20:23], v[40:43], a[104:107]\n\t"
      "buffer_load_dwordx4 v[188:191], v0, s[36:39], s49 offen offset:2048 sc1\n\t"
      "v_mfma_f32_16x16x32_bf16 a[124:127], v[20:23], v[44:47], a[124:127]\n\t"
      "v_pk_add_f32 v[200:201], v[200:201], v[220:221]\n\t"
      "v_mfma_f32_16x16x32_bf16 a[144:147], v[20:23], v[48:51], a[144:147]\n\t"
      "buffer_load_dwordx4 v[192:195], v0, s[36:39], s49 offen offset:3072 sc1\n\t"
      "v_mfma_f32_16x16x32_bf16 a[88:91], v[24:27], v[36:39], a[88:91]\n\t"
      "v_pk_add_f32 v[202:203], v[202:203], v[222:223]\n\t"
      "v_pk_add_f32 v[204:205], v[204:205], v[224:225]\n\t"
      "v_mfma_f32_16x16x32_bf16 a[108:111], v[24:27], v[40:43], a[108:111]\n\t"
      "buffer_load_dwordx4 v[160:163], v0, s[36:39], s48 offen sc1\n\t"
      "v_mfma_f32_16x16x32_bf16 a[128:131], v[24:27], v[44:47], a[128:131]\n\t"
      "v_pk_add_f32 v[206:207], v[206:207], v[226:227]\n\t"
      "v_pk_add_f32 v[208:209], v[208:209], v[228:229]\n\t"
      "v_mfma_f32_16x16x32_bf16 a[148:151], v[24:27], v[48:51], a[148:151]\n\t"
      "buffer_load_dwordx4 v[164:167], v0, s[36:39], s48 offen offset:1024 sc1\n\t"
      "v_mfma_f32_16x16x32_bf16 a[92:95], v[28:31], v[36:39], a[92:95]\n\t"
      "v_pk_add_f32 v[210:211], v[210:211], v[230:231]\n\t"
      "v_pk_add_f32 v[212:213], v[212:213], v[232:233]\n\t"
      "v_mfma_f32_16x16x32_bf16 a[112:115], v[28:31], v[40:43], a[112:115]\n\t"
      "buffer_load_dwordx4 v[168:171], v0, s[36:39], s48 offen offset:2048 sc1\n\t"
      "v_mfma_f32_16x16x32_bf16 a[132:135], v[28:31], v[44:47], a[132:135]\n\t"
      "v_pk_add_f32 v[214:215], v[214:215], v[234:235]\n\t"
      "v_mfma_f32_16x16x32_bf16 a[152:155], v[28:31], v[48:51], a[152:155]\n\t"
      "buffer_load_dwordx4 v[172:175], v0, s[36:39], s48 offen offset:3072 sc1\n\t"
      "v_mfma_f32_16x16x32_bf16 a[96:99], v[32:35], v[36:39], a[96:99]\n\t"
      "v_mfma_f32_16x16x32_bf16 a[116:119], v[32:35], v[40:43], a[116:119]\n\t"
      "buffer_load_dwordx4 v[176:179], v1, s[36:39], s48 offen sc1\n\t"
      "v_mfma_f32_16x16x32_bf16 a[136:139], v[32:35], v[44:47], a[136:139]\n\t"
      "v_mfma_f32_16x16x32_bf16 a[156:159], v[32:35], v[48:51], a[156:159]\n\t"
      "s_add_u32 s50, s51, 44\n\t"
      "s_lshl_b32 s49, s50, 12\n\t"
      "s_add_u32 s49, s49, s53\n\t"
      "s_add_u32 s50, s50, 54\n\t"
      "s_mul_i32 s48, s50, 0x1400\n\t"
      "s_add_u32 s48, s48, s54\n\t"
      "s_waitcnt vmcnt(27)\n\t"
      "v_mfma_f32_16x16x32_bf16 a[80:83], v[52:55], v[72:75], a[80:83]\n\t"
      "ds_read_b128 v[216:219], v9 offset:10240\n\t"
      "v_mfma_f32_16x16x32_bf16 a[100:103], v[52:55], v[76:79], a[100:103]\n\t"
      "buffer_load_dwordx4 v[36:39], v0, s[36:39], s49 offen sc1\n\t"
      "v_mfma_f32_16x16x32_bf16 a[120:123], v[52:55], v[80:83], a[120:123]\n\t"
      "ds_read_b128 v[220:223], v9 offset:11264\n\t"
      "v_mfma_f32_16x16x32_bf16 a[140:143], v[52:55], v[84:87], a[140:143]\n\t"
      "buffer_load_dwordx4 v[40:43], v0, s[36:39], s49 offen offset:1024 sc1\n\t"
      "v_mfma_f32_16x16x32_bf16 a[84:87], v[56:59], v[72:75], a[84:87]\n\t"
      "ds_read_b128 v[224:227], v9 offset:12288\n\t"
      "v_mfma_f32_16x16x32_bf16 a[104:107], v[56:59], v[76:79], a[104:107]\n\t"
      "buffer_load_dwordx4 v[44:47], v0, s[36:39], s49 offen offset:2048 sc1\n\t"
      "v_mfma_f32_16x16x32_bf16 a[124:127], v[56:59], v[80:83], a[124:127]\n\t"
      "ds_read_b128 v[228:231], v9 offset:13312\n\t"
      "v_mfma_f32_16x16x32_bf16 a[144:147], v[56:59], v[84:87], a[144:147]\n\t"
      "buffer_load_dwordx4 v[48:51], v0, s[36:39], s49 offen offset:3072 sc1\n\t"
      "v_mfma_f32_16x16x32_bf16 a[88:91], v[60:63], v[72:75], a[88:91]\n\t"
      "ds_read_b128 v[232:235], v9 offset:14336\n\t"
      "v_mfma_f32_16x16x32_bf16 a[108:111], v[60:63], v[76:79], a[108:111]\n\t"
      "buffer_load_dwordx4 v[16:19], v0, s[36:39], s48 offen sc1\n\t"
      "v_mfma_f32_16x16x32_bf16 a[128:131], v[60:63], v[80:83], a[128:131]\n\t"
      "v_mfma_f32_16x16x32_bf16 a[148:151], v[60:63], v[84:87], a[148:151]\n\t"
      "buffer_load_dwordx4 v[20:23], v0, s[36:39], s48 offen offset:1024 sc1\n\t"
      "v_mfma_f32_16x16x32_bf16 a[92:95], v[64:67], v[72:75], a[92:95]\n\t"
      "v_mfma_f32_16x16x32_bf16 a[112:115], v[64:67], v[76:79], a[112:115]\n\t"
      "buffer_load_dwordx4 v[24:27], v0, s[36:39], s48 offen offset:2048 sc1\n\t"
      "v_mfma_f32_16x16x32_bf16 a[132:135], v[64:67], v[80:83], a[132:135]\n\t"
      "v_mfma_f32_16x16x32_bf16 a[152:155], v[64:67], v[84:87], a[152:155]\n\t"
      "buffer_load_dwordx4 v[28:31], v0, s[36:39], s48 offen offset:3072 sc1\n\t"
      "v_mfma_f32_16x16x32_bf16 a[96:99], v[68:71], v[72:75], a[96:99]\n\t"
      "v_mfma_f32_16x16x32_bf16 a[116:119], v[68:71], v[76:79], a[116:119]\n\t"
      "buffer_load_dwordx4 v[32:35], v1, s[36:39], s48 offen sc1\n\t"
      "v_mfma_f32_16x16x32_bf16 a[136:139], v[68:71], v[80:83], a[136:139]\n\t"
      "v_mfma_f32_16x16x32_bf16 a[156:159], v[68:71], v[84:87], a[156:159]\n\t"
      "s_add_u32 s50, s51, 48\n\t"
      "s_lshl_b32 s49, s50, 12\n\t"
      "s_add_u32 s49, s49, s53\n\t"
      "s_add_u32 s50, s50, 54\n\t"
      "s_mul_i32 s48, s50, 0x1400\n\t"
      "s_add_u32 s48, s48, s54\n\t"
      "s_waitcnt vmcnt(27)\n\t"
      "v_mfma_f32_16x16x32_bf16 a[80:83], v[88:91], v[108:111], a[80:83]\n\t"
      "s_waitcnt lgkmcnt(0)\n\t"
      "v_mfma_f32_16x16x32_bf16 a[100:103], v[88:91], v[112:115], a[100:103]\n\t"
      "buffer_load_dwordx4 v[72:75], v0, s[36:39], s49 offen sc1\n\t"
      "v_mfma_f32_16x16x32_bf16 a[120:123], v[88:91], v[116:119], a[120:123]\n\t"
      "v_pk_add_f32 v[196:197], v[196:197], v[216:217]\n\t"
      "v_mfma_f32_16x16x32_bf16 a[140:143], v[88:91], v[120:123], a[140:143]\n\t"
      "buffer_load_dwordx4 v[76:79], v0, s[36:39], s49 offen offset:1024 sc1\n\t"
      "v_mfma_f32_16x16x32_bf16 a[84:87], v[92:95], v[108:111], a[84:87]\n\t"
      "v_pk_add_f32 v[198:199], v[198:199], v[218:219]\n\t"
      "v_mfma_f32_16x16x32_bf16 a[104:107], v[92:95], v[112:115], a[104:107]\n\t"
      "buffer_load_dwordx4 v[80:83], v0, s[36:39], s49 offen offset:2048 sc1\n\t"
      "v_mfma_f32_16x16x32_bf16 a[124:127], v[92:95], v[116:119], a[124:127]\n\t"
      "v_pk_add_f32 v[200:201], v[200:201], v[220:221]\n\t"
      "v_mfma_f32_16x16x32_bf16 a[144:147], v[92:95], v[120:123], a[144:147]\n\t"
      "buffer_load_dwordx4 v[84:87], v0, s[36:39], s49 offen offset:3072 sc1\n\t"
      "v_mfma_f32_16x16x32_bf16 a[88:91], v[96:99], v[108:111], a[88:91]\n\t"
      "v_pk_add_f32 v[202:203], v[202:203], v[222:223]\n\t"
      "v_pk_add_f32 v[204:205], v[204:205], v[224:225]\n\t"
      "v_mfma_f32_16x16x32_bf16 a[108:111], v[96:99], v[112:115], a[108:111]\n\t"
      "buffer_load_dwordx4 v[52:55], v0, s[36:39], s48 offen sc1\n\t"
      "v_mfma_f32_16x16x32_bf16 a[128:131], v[96:99], v[116:119], a[128:131]\n\t"
      "v_pk_add_f32 v[206:207], v[206:207], v[226:227]\n\t"
      "v_pk_add_f32 v[208:209], v[208:209], v[228:229]\n\t"
      "v_mfma_f32_16x16x32_bf16 a[148:151], v[96:99], v[120:123], a[148:151]\n\t"
      "buffer_load_dwordx4 v[56:59], v0, s[36:39], s48 offen offset:1024 sc1\n\t"
      "v_mfma_f32_16x16x32_bf16 a[92:95], v[100:103], v[108:111], a[92:95]\n\t"
      "v_pk_add_f32 v[210:211], v[210:211], v[230:231]\n\t"
      "v_pk_add_f32 v[212:213], v[212:213], v[232:233]\n\t"
      "v_mfma_f32_16x16x32_bf16 a[112:115], v[100:103], v[112:115], a[112:115]\n\t"
      "buffer_load_dwordx4 v[60:63], v0, s[36:39], s48 offen offset:2048 sc1\n\t"
      "v_mfma_f32_16x16x32_bf16 a[132:135], v[100:103], v[116:119], a[132:135]\n\t"
      "v_pk_add_f32 v[214:215], v[214:215], v[234:235]\n\t"
      "v_mfma_f32_16x16x32_bf16 a[152:155], v[100:103], v[120:123], a[152:155]\n\t"
      "buffer_load_dwordx4 v[64:67], v0, s[36:39], s48 offen offset:3072 sc1\n\t"
      "v_mfma_f32_16x16x32_bf16 a[96:99], v[104:107], v[108:111], a[96:99]\n\t"
      "v_mfma_f32_16x16x32_bf16 a[116:119], v[104:107], v[112:115], a[116:119]\n\t"
      "buffer_load_dwordx4 v[68:71], v1, s[36:39], s48 offen sc1\n\t"
      "v_mfma_f32_16x16x32_bf16 a[136:139], v[104:107], v[116:119], a[136:139]\n\t"
      "v_mfma_f32_16x16x32_bf16 a[156:159], v[104:107], v[120:123], a[156:159]\n\t"
      "s_add_u32 s50, s51, 52\n\t"
      "s_lshl_b32 s49, s50, 12\n\t"
      "s_add_u32 s49, s49, s53\n\t"
      "s_add_u32 s50, s50, 54\n\t"
      "s_mul_i32 s48, s50, 0x1400\n\t"
      "s_add_u32 s48, s48, s54\n\t"
      "s_waitcnt vmcnt(27)\n\t"
      "v_mfma_f32_16x16x32_bf16 a[80:83], v[124:127], v[144:147], a[80:83]\n\t"
      "ds_read_b128 v[216:219], v9 offset:15360\n\t"
      "v_mfma_f32_16x16x32_bf16 a[100:103], v[124:127], v[148:151], a[100:103]\n\t"
      "buffer_load_dwordx4 v[108:111], v0, s[36:39], s49 offen sc1\n\t"
      "v_mfma_f32_16x16x32_bf16 a[120:123], v[124:127], v[152:155], a[120:123]\n\t"
      "ds_read_b128 v[220:223], v9 offset:16384\n\t"
      "v_mfma_f32_16x16x32_bf16 a[140:143], v[124:127], v[156:159], a[140:143]\n\t"
      "buffer_load_dwordx4 v[112:115], v0, s[36:39], s49 offen offset:1024 sc1\n\t"
      "v_mfma_f32_16x16x32_bf16 a[84:87], v[128:131], v[144:147], a[84:87]\n\t"
      "ds_read_b128 v[224:227], v9 offset:17408\n\t"
      "v_mfma_f32_16x16x32_bf16 a[104:107], v[128:131], v[148:151], a[104:107]\n\t"
      "buffer_load_dwordx4 v[116:119], v0, s[36:39], s49 offen offset:2048 sc1\n\t"
      "v_mfma_f32_16x16x32_bf16 a[124:127], v[128:131], v[152:155], a[124:127]\n\t"
      "ds_read_b128 v[228:231], v9 offset:18432\n\t"
      "v_mfma_f32_16x16x32_bf16 a[144:147], v[128:131], v[156:159], a[144:147]\n\t"
      "buffer_load_dwordx4 v[120:123], v0, s[36:39], s49 offen offset:3072 sc1\n\t"
      "v_mfma_f32_16x16x32_bf16 a[88:91], v[132:135], v[144:147], a[88:91]\n\t"
      "ds_read_b128 v[232:235], v9 offset:19456\n\t"
      "v_mfma_f32_16x16x32_bf16 a[108:111], v[132:135], v[148:151], a[108:111]\n\t"
      "buffer_load_dwordx4 v[88:91], v0, s[36:39], s48 offen sc1\n\t"
      "v_mfma_f32_16x16x32_bf16 a[128:131], v[132:135], v[152:155], a[128:131]\n\t"
      "v_mfma_f32_16x16x32_bf16 a[148:151], v[132:135], v[156:159], a[148:151]\n\t"
      "buffer_load_dwordx4 v[92:95], v0, s[36:39], s48 offen offset:1024 sc1\n\t"
      "v_mfma_f32_16x16x32_bf16 a[92:95], v[136:139], v[144:147], a[92:95]\n\t"
      "v_mfma_f32_16x16x32_bf16 a[112:115], v[136:139], v[148:151], a[112:115]\n\t"
      "buffer_load_dwordx4 v[96:99], v0, s[36:39], s48 offen offset:2048 sc1\n\t"
      "v_mfma_f32_16x16x32_bf16 a[132:135], v[136:139], v[152:155], a[132:135]\n\t"
      "v_mfma_f32_16x16x32_bf16 a[152:155], v[136:139], v[156:159], a[152:155]\n\t"
      "buffer_load_dwordx4 v[100:103], v0, s[36:39], s48 offen offset:3072 sc1\n\t"
      "v_mfma_f32_16x16x32_bf16 a[96:99], v[140:143], v[144:147], a[96:99]\n\t"
      "v_mfma_f32_16x16x32_bf16 a[116:119], v[140:143], v[148:151], a[116:119]\n\t"
      "buffer_load_dwordx4 v[104:107], v1, s[36:39], s48 offen sc1\n\t"
      "v_mfma_f32_16x16x32_bf16 a[136:139], v[140:143], v[152:155], a[136:139]\n\t"
      "v_mfma_f32_16x16x32_bf16 a[156:159], v[140:143], v[156:159], a[156:159]\n\t"
      "s_add_u32 s50, s51, 56\n\t"
      "s_lshl_b32 s49, s50, 12\n\t"
      "s_add_u32 s49, s49, s53\n\t"
      "s_add_u32 s50, s50, 54\n\t"
      "s_mul_i32 s48, s50, 0x1400\n\t"
      "s_add_u32 s48, s48, s54\n\t"
      "s_waitcnt vmcnt(27)\n\t"
      "v_mfma_f32_16x16x32_bf16 a[80:83], v[160:163], v[180:183], a[80:83]\n\t"
      "s_waitcnt lgkmcnt(0)\n\t"
      "v_mfma_f32_16x16x32_bf16 a[100:103], v[160:163], v[184:187], a[100:103]\n\t"
      "buffer_load_dwordx4 v[144:147], v0, s[36:39], s49 offen sc1\n\t"
      "v_mfma_f32_16x16x32_bf16 a[120:123], v[160:163], v[188:191], a[120:123]\n\t"
      "v_pk_add_f32 v[196:197], v[196:197], v[216:217]\n\t"
      "v_mfma_f32_16x16x32_bf16 a[140:143], v[160:163], v[192:195], a[140:143]\n\t"
      "buffer_load_dwordx4 v[148:151], v0, s[36:39], s49 offen offset:1024 sc1\n\t"
      "v_mfma_f32_16x16x32_bf16 a[84:87], v[164:167], v[180:183], a[84:87]\n\t"
      "v_pk_add_f32 v[198:199], v[198:199], v[218:219]\n\t"
      "v_mfma_f32_16x16x32_bf16 a[104:107], v[164:167], v[184:187], a[104:107]\n\t"
      "buffer_load_dwordx4 v[152:155], v0, s[36:39], s49 offen offset:2048 sc1\n\t"
      "v_mfma_f32_16x16x32_bf16 a[124:127], v[164:167], v[188:191], a[124:127]\n\t"
      "v_pk_add_f32 v[200:201], v[200:201], v[220:221]\n\t"
      "v_mfma_f32_16x16x32_bf16 a[144:147], v[164:167], v[192:195], a[144:147]\n\t"
      "buffer_load_dwordx4 v[156:159], v0, s[36:39], s49 offen offset:3072 sc1\n\t"
      "v_mfma_f32_16x16x32_bf16 a[88:91], v[168:171], v[180:183], a[88:91]\n\t"
      "v_pk_add_f32 v[202:203], v[202:203], v[222:223]\n\t"
      "v_pk_add_f32 v[204:205], v[204:205], v[224:225]\n\t"
      "v_mfma_f32_16x16x32_bf16 a[108:111], v[168:171], v[184:187], a[108:111]\n\t"
      "buffer_load_dwordx4 v[124:127], v0, s[36:39], s48 offen sc1\n\t"
      "v_mfma_f32_16x16x32_bf16 a[128:131], v[168:171], v[188:191], a[128:131]\n\t"
      "v_pk_add_f32 v[206:207], v[206:207], v[226:227]\n\t"
      "v_pk_add_f32 v[208:209], v[208:209], v[228:229]\n\t"
      "v_mfma_f32_16x16x32_bf16 a[148:151], v[168:171], v[192:195], a[148:151]\n\t"
      "buffer_load_dwordx4 v[128:131], v0, s[36:39], s48 offen offset:1024 sc1\n\t"
      "v_mfma_f32_16x16x32_bf16 a[92:95], v[172:175], v[180:183], a[92:95]\n\t"
      "v_pk_add_f32 v[210:211], v[210:211], v[230:231]\n\t"
      "v_pk_add_f32 v[212:213], v[212:213], v[232:233]\n\t"
      "v_mfma_f32_16x16x32_bf16 a[112:115], v[172:175], v[184:187], a[112:115]\n\t"
      "buffer_load_dwordx4 v[132:135], v0, s[36:39], s48 offen offset:2048 sc1\n\t"
      "v_mfma_f32_16x16x32_bf16 a[132:135], v[172:175], v[188:191], a[132:135]\n\t"
      "v_pk_add_f32 v[214:215], v[214:215], v[234:235]\n\t"
      "v_mfma_f32_16x16x32_bf16 a[152:155], v[172:175], v[192:195], a[152:155]\n\t"
      "buffer_load_dwordx4 v[136:139], v0, s[36:39], s48 offen offset:3072 sc1\n\t"
      "v_mfma_f32_16x16x32_bf16 a[96:99], v[176:179], v[180:183], a[96:99]\n\t"
      "v_mfma_f32_16x16x32_bf16 a[116:119], v[176:179], v[184:187], a[116:119]\n\t"
      "buffer_load_dwordx4 v[140:143], v1, s[36:39], s48 offen sc1\n\t"
      "v_mfma_f32_16x16x32_bf16 a[136:139], v[176:179], v[188:191], a[136:139]\n\t"
      "v_mfma_f32_16x16x32_bf16 a[156:159], v[176:179], v[192:195], a[156:159]\n\t"
      "s_add_u32 s50, s51, 60\n\t"
      "s_lshl_b32 s49, s50, 12\n\t"
      "s_add_u32 s49, s49, s53\n\t"
      "s_add_u32 s50, s50, 54\n\t"
      "s_mul_i32 s48, s50, 0x1400\n\t"
      "s_add_u32 s48, s48, s54\n\t"
      "s_waitcnt vmcnt(27)\n\t"
      "v_mfma_f32_16x16x32_bf16 a[80:83], v[16:19], v[36:39], a[80:83]\n\t"
      "v_mfma_f32_16x16x32_bf16 a[100:103], v[16:19], v[40:43], a[100:103]\n\t"
      "buffer_load_dwordx4 v[180:183], v0, s[36:39], s49 offen sc1\n\t"
      "v_mfma_f32_16x16x32_bf16 a[120:123], v[16:19], v[44:47], a[120:123]\n\t"
      "v_mfma_f32_16x16x32_bf16 a[140:143], v[16:19], v[48:51], a[140:143]\n\t"
      "buffer_load_dwordx4 v[184:187], v0, s[36:39], s49 offen offset:1024 sc1\n\t"
      "v_mfma_f32_16x16x32_bf16 a[84:87], v[20:23], v[36:39], a[84:87]\n\t"
      "v_mfma_f32_16x16x32_bf16 a[104:107], v[20:23], v[40:43], a[104:107]\n\t"
      "buffer_load_dwordx4 v[188:191], v0, s[36:39], s49 offen offset:2048 sc1\n\t"
      "v_mfma_f32_16x16x32_bf16 a[124:127], v[20:23], v[44:47], a[124:127]\n\t"
      "v_mfma_f32_16x16x32_bf16 a[144:147], v[20:23], v[48:51], a[144:147]\n\t"
      "buffer_load_dwordx4 v[192:195], v0, s[36:39], s49 offen offset:3072 sc1\n\t"
      "v_mfma_f32_16x16x32_bf16 a[88:91], v[24:27], v[36:39], a[88:91]\n\t"
      "v_mfma_f32_16x16x32_bf16 a[108:111], v[24:27], v[40:43], a[108:111]\n\t"
      "buffer_load_dwordx4 v[160:163], v0, s[36:39], s48 offen sc1\n\t"
      "v_mfma_f32_16x16x32_bf16 a[128:131], v[24:27], v[44:47], a[128:131]\n\t"
      "v_mfma_f32_16x16x32_bf16 a[148:151], v[24:27], v[48:51], a[148:151]\n\t"
      "buffer_load_dwordx4 v[164:167], v0, s[36:39], s48 offen offset:1024 sc1\n\t"
      "v_mfma_f32_16x16x32_bf16 a[92:95], v[28:31], v[36:39], a[92:95]\n\t"
      "v_mfma_f32_16x16x32_bf16 a[112:115], v[28:31], v[40:43], a[112:115]\n\t"
      "buffer_load_dwordx4 v[168:171], v0, s[36:39], s48 offen offset:2048 sc1\n\t"
      "v_mfma_f32_16x16x32_bf16 a[132:135], v[28:31], v[44:47], a[132:135]\n\t"
      "v_mfma_f32_16x16x32_bf16 a[152:155], v[28:31], v[48:51], a[152:155]\n\t"
      "buffer_load_dwordx4 v[172:175], v0, s[36:39], s48 offen offset:3072 sc1\n\t"
      "v_mfma_f32_16x16x32_bf16 a[96:99], v[32:35], v[36:39], a[96:99]\n\t"
      "v_mfma_f32_16x16x32_bf16 a[116:119], v[32:35], v[40:43], a[116:119]\n\t"
      "buffer_load_dwordx4 v[176:179], v1, s[36:39], s48 offen sc1\n\t"
      "v_mfma_f32_16x16x32_bf16 a[136:139], v[32:35], v[44:47], a[136:139]\n\t"
      "v_mfma_f32_16x16x32_bf16 a[156:159], v[32:35], v[48:51], a[156:159]\n\t"
      "s_add_u32 s50, s51, 64\n\t"
      "s_lshl_b32 s49, s50, 12\n\t"
      "s_add_u32 s49, s49, s53\n\t"
      "s_add_u32 s50, s50, 54\n\t"
      "s_mul_i32 s48, s50, 0x1400\n\t"
      "s_add_u32 s48, s48, s54\n\t"
      "s_waitcnt vmcnt(27)\n\t"
      "v_mfma_f32_16x16x32_bf16 a[80:83], v[52:55], v[72:75], a[80:83]\n\t"
      "v_mul_f32 v216, 0xbfb8aa3b, v196\n\t"
      "v_mfma_f32_16x16x32_bf16 a[100:103], v[52:55], v[76:79], a[100:103]\n\t"
      "buffer_load_dwordx4 v[36:39], v0, s[36:39], s49 offen sc1\n\t"
      "v_mfma_f32_16x16x32_bf16 a[120:123], v[52:55], v[80:83], a[120:123]\n\t"
      "v_mul_f32 v217, 0xbfb8aa3b, v197\n\t"
      "v_mfma_f32_16x16x32_bf16 a[140:143], v[52:55], v[84:87], a[140:143]\n\t"
      "buffer_load_dwordx4 v[40:43], v0, s[36:39], s49 offen offset:1024 sc1\n\t"
      "v_mfma_f32_16x16x32_bf16 a[84:87], v[56:59], v[72:75], a[84:87]\n\t"
      "v_mul_f32 v218, 0xbfb8aa3b, v198\n\t"
      "v_mfma_f32_16x16x32_bf16 a[104:107], v[56:59], v[76:79], a[104:107]\n\t"
      "buffer_load_dwordx4 v[44:47], v0, s[36:39], s49 offen offset:2048 sc1\n\t"
      "v_mfma_f32_16x16x32_bf16 a[124:127], v[56:59], v[80:83], a[124:127]\n\t"
      "v_mul_f32 v219, 0xbfb8aa3b, v199\n\t"
      "v_mfma_f32_16x16x32_bf16 a[144:147], v[56:59], v[84:87], a[144:147]\n\t"
      "buffer_load_dwordx4 v[48:51], v0, s[36:39], s49 offen offset:3072 sc1\n\t"
      "v_mfma_f32_16x16x32_bf16 a[88:91], v[60:63], v[72:75], a[88:91]\n\t"
      "v_mul_f32 v220, 0xbfb8aa3b, v200\n\t"
      "v_mul_f32 v221, 0xbfb8aa3b, v201\n\t"
      "v_mfma_f32_16x16x32_bf16 a[108:111], v[60:63], v[76:79], a[108:111]\n\t"
      "buffer_load_dwordx4 v[16:19], v0, s[36:39], s48 offen sc1\n\t"
      "v_mfma_f32_16x16x32_bf16 a[128:131], v[60:63], v[80:83], a[128:131]\n\t"
      "v_mul_f32 v222, 0xbfb8aa3b, v202\n\t"
      "v_mul_f32 v223, 0xbfb8aa3b, v203\n\t"
      "v_mfma_f32_16x16x32_bf16 a[148:151], v[60:63], v[84:87], a[148:151]\n\t"
      "buffer_load_dwordx4 v[20:23], v0, s[36:39], s48 offen offset:1024 sc1\n\t"
      "v_mfma_f32_16x16x32_bf16 a[92:95], v[64:67], v[72:75], a[92:95]\n\t"
      "v_mul_f32 v224, 0xbfb8aa3b, v204\n\t"
      "v_mul_f32 v225, 0xbfb8aa3b, v205\n\t"
      "v_mfma_f32_16x16x32_bf16 a[112:115], v[64:67], v[76:79], a[112:115]\n\t"
      "buffer_load_dwordx4 v[24:27], v0, s[36:39], s48 offen offset:2048 sc1\n\t"
      "v_mfma_f32_16x16x32_bf16 a[132:135], v[64:67], v[80:83], a[132:135]\n\t"
      "v_mul_f32 v226, 0xbfb8aa3b, v206\n\t"
      "v_mul_f32 v227, 0xbfb8aa3b, v207\n\t"
      "v_mfma_f32_16x16x32_bf16 a[152:155], v[64:67], v[84:87], a[152:155]\n\t"
      "buffer_load_dwordx4 v[28:31], v0, s[36:39], s48 offen offset:3072 sc1\n\t"
      "v_mfma_f32_16x16x32_bf16 a[96:99], v[68:71], v[72:75], a[96:99]\n\t"
      "v_mul_f32 v228, 0xbfb8aa3b, v208\n\t"
      "v_mul_f32 v229, 0xbfb8aa3b, v209\n\t"
      "v_mfma_f32_16x16x32_bf16 a[116:119], v[68:71], v[76:79], a[116:119]\n\t"
      "buffer_load_dwordx4 v[32:35], v1, s[36:39], s48 offen sc1\n\t"
      "v_mfma_f32_16x16x32_bf16 a[136:139], v[68:71], v[80:83], a[136:139]\n\t"
      "v_mul_f32 v230, 0xbfb8aa3b, v210\n\t"
      "v_mul_f32 v231, 0xbfb8aa3b, v211\n\t"
      "v_mfma_f32_16x16x32_bf16 a[156:159], v[68:71], v[84:87], a[156:159]\n\t"
      "v_mul_f32 v232, 0xbfb8aa3b, v212\n\t"
      "v_mul_f32 v233, 0xbfb8aa3b, v213\n\t"
      "v_mul_f32 v234, 0xbfb8aa3b, v214\n\t"
      "v_mul_f32 v235, 0xbfb8aa3b, v215\n\t"
      "s_add_u32 s50, s51, 0x44\n\t"
      "s_lshl_b32 s49, s50, 12\n\t"
      "s_add_u32 s49, s49, s53\n\t"
      "s_add_u32 s50, s50, 54\n\t"
      "s_mul_i32 s48, s50, 0x1400\n\t"
      "s_add_u32 s48, s48, s54\n\t"
      "s_waitcnt vmcnt(27)\n\t"
      "v_mfma_f32_16x16x32_bf16 a[80:83], v[88:91], v[108:111], a[80:83]\n\t"
      "v_exp_f32 v216, v216\n\t"
      "v_mfma_f32_16x16x32_bf16 a[100:103], v[88:91], v[112:115], a[100:103]\n\t"
      "buffer_load_dwordx4 v[72:75], v0, s[36:39], s49 offen sc1\n\t"
      "v_mfma_f32_16x16x32_bf16 a[120:123], v[88:91], v[116:119], a[120:123]\n\t"
      "v_exp_f32 v217, v217\n\t"
      "v_mfma_f32_16x16x32_bf16 a[140:143], v[88:91], v[120:123], a[140:143]\n\t"
      "buffer_load_dwordx4 v[76:79], v0, s[36:39], s49 offen offset:1024 sc1\n\t"
      "v_mfma_f32_16x16x32_bf16 a[84:87], v[92:95], v[108:111], a[84:87]\n\t"
      "v_exp_f32 v218, v218\n\t"
      "v_mfma_f32_16x16x32_bf16 a[104:107], v[92:95], v[112:115], a[104:107]\n\t"
      "buffer_load_dwordx4 v[80:83], v0, s[36:39], s49 offen offset:2048 sc1\n\t"
      "v_mfma_f32_16x16x32_bf16 a[124:127], v[92:95], v[116:119], a[124:127]\n\t"
      "v_exp_f32 v219, v219\n\t"
      "v_mfma_f32_16x16x32_bf16 a[144:147], v[92:95], v[120:123], a[144:147]\n\t"
      "buffer_load_dwordx4 v[84:87], v0, s[36:39], s49 offen offset:3072 sc1\n\t"
      "v_mfma_f32_16x16x32_bf16 a[88:91], v[96:99], v[108:111], a[88:91]\n\t"
      "v_exp_f32 v220, v220\n\t"
      "v_exp_f32 v221, v221\n\t"
      "v_mfma_f32_16x16x32_bf16 a[108:111], v[96:99], v[112:115], a[108:111]\n\t"
      "buffer_load_dwordx4 v[52:55], v0, s[36:39], s48 offen sc1\n\t"
      "v_mfma_f32_16x16x32_bf16 a[128:131], v[96:99], v[116:119], a[128:131]\n\t"
      "v_exp_f32 v222, v222\n\t"
      "v_exp_f32 v223, v223\n\t"
      "v_mfma_f32_16x16x32_bf16 a[148:151], v[96:99], v[120:123], a[148:151]\n\t"
      "buffer_load_dwordx4 v[56:59], v0, s[36:39], s48 offen offset:1024 sc1\n\t"
      "v_mfma_f32_16x16x32_bf16 a[92:95], v[100:103], v[108:111], a[92:95]\n\t"
      "v_exp_f32 v224, v224\n\t"
      "v_exp_f32 v225, v225\n\t"
      "v_mfma_f32_16x16x32_bf16 a[112:115], v[100:103], v[112:115], a[112:115]\n\t"
      "buffer_load_dwordx4 v[60:63], v0, s[36:39], s48 offen offset:2048 sc1\n\t"
      "v_mfma_f32_16x16x32_bf16 a[132:135], v[100:103], v[116:119], a[132:135]\n\t"
      "v_exp_f32 v226, v226\n\t"
      "v_exp_f32 v227, v227\n\t"
      "v_mfma_f32_16x16x32_bf16 a[152:155], v[100:103], v[120:123], a[152:155]\n\t"
      "buffer_load_dwordx4 v[64:67], v0, s[36:39], s48 offen offset:3072 sc1\n\t"
      "v_mfma_f32_16x16x32_bf16 a[96:99], v[104:107], v[108:111], a[96:99]\n\t"
      "v_exp_f32 v228, v228\n\t"
      "v_exp_f32 v229, v229\n\t"
      "v_mfma_f32_16x16x32_bf16 a[116:119], v[104:107], v[112:115], a[116:119]\n\t"
      "buffer_load_dwordx4 v[68:71], v1, s[36:39], s48 offen sc1\n\t"
      "v_mfma_f32_16x16x32_bf16 a[136:139], v[104:107], v[116:119], a[136:139]\n\t"
      "v_exp_f32 v230, v230\n\t"
      "v_exp_f32 v231, v231\n\t"
      "v_mfma_f32_16x16x32_bf16 a[156:159], v[104:107], v[120:123], a[156:159]\n\t"
      "v_exp_f32 v232, v232\n\t"
      "v_exp_f32 v233, v233\n\t"
      "v_exp_f32 v234, v234\n\t"
      "v_exp_f32 v235, v235\n\t"
      "s_add_u32 s50, s51, 0x48\n\t"
      "s_lshl_b32 s49, s50, 12\n\t"
      "s_add_u32 s49, s49, s53\n\t"
      "s_add_u32 s50, s50, 54\n\t"
      "s_mul_i32 s48, s50, 0x1400\n\t"
      "s_add_u32 s48, s48, s54\n\t"
      "s_waitcnt vmcnt(27)\n\t"
      "v_mfma_f32_16x16x32_bf16 a[80:83], v[124:127], v[144:147], a[80:83]\n\t"
      "v_add_f32 v216, 0x3f800000, v216\n\t"
      "v_mfma_f32_16x16x32_bf16 a[100:103], v[124:127], v[148:151], a[100:103]\n\t"
      "buffer_load_dwordx4 v[108:111], v0, s[36:39], s49 offen sc1\n\t"
      "v_mfma_f32_16x16x32_bf16 a[120:123], v[124:127], v[152:155], a[120:123]\n\t"
      "v_add_f32 v217, 0x3f800000, v217\n\t"
      "v_mfma_f32_16x16x32_bf16 a[140:143], v[124:127], v[156:159], a[140:143]\n\t"
      "buffer_load_dwordx4 v[112:115], v0, s[36:39], s49 offen offset:1024 sc1\n\t"
      "v_mfma_f32_16x16x32_bf16 a[84:87], v[128:131], v[144:147], a[84:87]\n\t"
      "v_add_f32 v218, 0x3f800000, v218\n\t"
      "v_mfma_f32_16x16x32_bf16 a[104:107], v[128:131], v[148:151], a[104:107]\n\t"
      "buffer_load_dwordx4 v[116:119], v0, s[36:39], s49 offen offset:2048 sc1\n\t"
      "v_mfma_f32_16x16x32_bf16 a[124:127], v[128:131], v[152:155], a[124:127]\n\t"
      "v_add_f32 v219, 0x3f800000, v219\n\t"
      "v_mfma_f32_16x16x32_bf16 a[144:147], v[128:131], v[156:159], a[144:147]\n\t"
      "buffer_load_dwordx4 v[120:123], v0, s[36:39], s49 offen offset:3072 sc1\n\t"
      "v_mfma_f32_16x16x32_bf16 a[88:91], v[132:135], v[144:147], a[88:91]\n\t"
      "v_add_f32 v220, 0x3f800000, v220\n\t"
      "v_add_f32 v221, 0x3f800000, v221\n\t"
      "v_mfma_f32_16x16x32_bf16 a[108:111], v[132:135], v[148:151], a[108:111]\n\t"
      "buffer_load_dwordx4 v[88:91], v0, s[36:39], s48 offen sc1\n\t"
      "v_mfma_f32_16x16x32_bf16 a[128:131], v[132:135], v[152:155], a[128:131]\n\t"
      "v_add_f32 v222, 0x3f800000, v222\n\t"
      "v_add_f32 v223, 0x3f800000, v223\n\t"
      "v_mfma_f32_16x16x32_bf16 a[148:151], v[132:135], v[156:159], a[148:151]\n\t"
      "buffer_load_dwordx4 v[92:95], v0, s[36:39], s48 offen offset:1024 sc1\n\t"
      "v_mfma_f32_16x16x32_bf16 a[92:95], v[136:139], v[144:147], a[92:95]\n\t"
      "v_add_f32 v224, 0x3f800000, v224\n\t"
      "v_add_f32 v225, 0x3f800000, v225\n\t"
      "v_mfma_f32_16x16x32_bf16 a[112:115], v[136:139], v[148:151], a[112:115]\n\t"
      "buffer_load_dwordx4 v[96:99], v0, s[36:39], s48 offen offset:2048 sc1\n\t"
      "v_mfma_f32_16x16x32_bf16 a[132:135], v[136:139], v[152:155], a[132:135]\n\t"
      "v_add_f32 v226, 0x3f800000, v226\n\t"
      "v_add_f32 v227, 0x3f800000, v227\n\t"
      "v_mfma_f32_16x16x32_bf16 a[152:155], v[136:139], v[156:159], a[152:155]\n\t"
      "buffer_load_dwordx4 v[100:103], v0, s[36:39], s48 offen offset:3072 sc1\n\t"
      "v_mfma_f32_16x16x32_bf16 a[96:99], v[140:143], v[144:147], a[96:99]\n\t"
      "v_add_f32 v228, 0x3f800000, v228\n\t"
      "v_add_f32 v229, 0x3f800000, v229\n\t"
      "v_mfma_f32_16x16x32_bf16 a[116:119], v[140:143], v[148:151], a[116:119]\n\t"
      "buffer_load_dwordx4 v[104:107], v1, s[36:39], s48 offen sc1\n\t"
      "v_mfma_f32_16x16x32_bf16 a[136:139], v[140:143], v[152:155], a[136:139]\n\t"
      "v_add_f32 v230, 0x3f800000, v230\n\t"
      "v_add_f32 v231, 0x3f800000, v231\n\t"
      "v_mfma_f32_16x16x32_bf16 a[156:159], v[140:143], v[156:159], a[156:159]\n\t"
      "v_add_f32 v232, 0x3f800000, v232\n\t"
      "v_add_f32 v233, 0x3f800000, v233\n\t"
      "v_add_f32 v234, 0x3f800000, v234\n\t"
      "v_add_f32 v235, 0x3f800000, v235\n\t"
      "s_add_u32 s50, s51, 0x4c\n\t"
      "s_lshl_b32 s49, s50, 12\n\t"
      "s_add_u32 s49, s49, s53\n\t"
      "s_add_u32 s50, s50, 54\n\t"
      "s_mul_i32 s48, s50, 0x1400\n\t"
      "s_add_u32 s48, s48, s54\n\t"
      "s_waitcnt vmcnt(27)\n\t"
      "v_mfma_f32_16x16x32_bf16 a[80:83], v[160:163], v[180:183], a[80:83]\n\t"
      "v_rcp_f32 v216, v216\n\t"
      "v_mfma_f32_16x16x32_bf16 a[100:103], v[160:163], v[184:187], a[100:103]\n\t"
      "buffer_load_dwordx4 v[144:147], v0, s[36:39], s49 offen sc1\n\t"
      "v_mfma_f32_16x16x32_bf16 a[120:123], v[160:163], v[188:191], a[120:123]\n\t"
      "v_rcp_f32 v217, v217\n\t"
      "v_mfma_f32_16x16x32_bf16 a[140:143], v[160:163], v[192:195], a[140:143]\n\t"
      "buffer_load_dwordx4 v[148:151], v0, s[36:39], s49 offen offset:1024 sc1\n\t"
      "v_mfma_f32_16x16x32_bf16 a[84:87], v[164:167], v[180:183], a[84:87]\n\t"
      "v_rcp_f32 v218, v218\n\t"
      "v_mfma_f32_16x16x32_bf16 a[104:107], v[164:167], v[184:187], a[104:107]\n\t"
      "buffer_load_dwordx4 v[152:155], v0, s[36:39], s49 offen offset:2048 sc1\n\t"
      "v_mfma_f32_16x16x32_bf16 a[124:127], v[164:167], v[188:191], a[124:127]\n\t"
      "v_rcp_f32 v219, v219\n\t"
      "v_mfma_f32_16x16x32_bf16 a[144:147], v[164:167], v[192:195], a[144:147]\n\t"
      "buffer_load_dwordx4 v[156:159], v0, s[36:39], s49 offen offset:3072 sc1\n\t"
      "v_mfma_f32_16x16x32_bf16 a[88:91], v[168:171], v[180:183], a[88:91]\n\t"
      "v_rcp_f32 v220, v220\n\t"
      "v_rcp_f32 v221, v221\n\t"
      "v_mfma_f32_16x16x32_bf16 a[108:111], v[168:171], v[184:187], a[108:111]\n\t"
      "buffer_load_dwordx4 v[124:127], v0, s[36:39], s48 offen sc1\n\t"
      "v_mfma_f32_16x16x32_bf16 a[128:131], v[168:171], v[188:191], a[128:131]\n\t"
      "v_rcp_f32 v222, v222\n\t"
      "v_rcp_f32 v223, v223\n\t"
      "v_mfma_f32_16x16x32_bf16 a[148:151], v[168:171], v[192:195], a[148:151]\n\t"
      "buffer_load_dwordx4 v[128:131], v0, s[36:39], s48 offen offset:1024 sc1\n\t"
      "v_mfma_f32_16x16x32_bf16 a[92:95], v[172:175], v[180:183], a[92:95]\n\t"
      "v_rcp_f32 v224, v224\n\t"
      "v_rcp_f32 v225, v225\n\t"
      "v_mfma_f32_16x16x32_bf16 a[112:115], v[172:175], v[184:187], a[112:115]\n\t"
      "buffer_load_dwordx4 v[132:135], v0, s[36:39], s48 offen offset:2048 sc1\n\t"
      "v_mfma_f32_16x16x32_bf16 a[132:135], v[172:175], v[188:191], a[132:135]\n\t"
      "v_rcp_f32 v226, v226\n\t"
      "v_rcp_f32 v227, v227\n\t"
      "v_mfma_f32_16x16x32_bf16 a[152:155], v[172:175], v[192:195], a[152:155]\n\t"
      "buffer_load_dwordx4 v[136:139], v0, s[36:39], s48 offen offset:3072 sc1\n\t"
      "v_mfma_f32_16x16x32_bf16 a[96:99], v[176:179], v[180:183], a[96:99]\n\t"
      "v_rcp_f32 v228, v228\n\t"
      "v_rcp_f32 v229, v229\n\t"
      "v_mfma_f32_16x16x32_bf16 a[116:119], v[176:179], v[184:187], a[116:119]\n\t"
      "buffer_load_dwordx4 v[140:143], v1, s[36:39], s48 offen sc1\n\t"
      "v_mfma_f32_16x16x32_bf16 a[136:139], v[176:179], v[188:191], a[136:139]\n\t"
      "v_rcp_f32 v230, v230\n\t"
      "v_rcp_f32 v231, v231\n\t"
      "v_mfma_f32_16x16x32_bf16 a[156:159], v[176:179], v[192:195], a[156:159]\n\t"
      "v_rcp_f32 v232, v232\n\t"
      "v_rcp_f32 v233, v233\n\t"
      "v_rcp_f32 v234, v234\n\t"
      "v_rcp_f32 v235, v235\n\t"
      "s_add_u32 s50, s51, 0x50\n\t"
      "s_lshl_b32 s49, s50, 12\n\t"
      "s_add_u32 s49, s49, s53\n\t"
      "s_add_u32 s50, s50, 54\n\t"
      "s_mul_i32 s48, s50, 0x1400\n\t"
      "s_add_u32 s48, s48, s54\n\t"
      "s_waitcnt vmcnt(27)\n\t"
      "v_mfma_f32_16x16x32_bf16 a[80:83], v[16:19], v[36:39], a[80:83]\n\t"
      "v_mul_f32 v196, v196, v216\n\t"
      "v_mfma_f32_16x16x32_bf16 a[100:103], v[16:19], v[40:43], a[100:103]\n\t"
      "buffer_load_dwordx4 v[180:183], v0, s[36:39], s49 offen sc1\n\t"
      "v_mfma_f32_16x16x32_bf16 a[120:123], v[16:19], v[44:47], a[120:123]\n\t"
      "v_mul_f32 v197, v197, v217\n\t"
      "v_mfma_f32_16x16x32_bf16 a[140:143], v[16:19], v[48:51], a[140:143]\n\t"
      "buffer_load_dwordx4 v[184:187], v0, s[36:39], s49 offen offset:1024 sc1\n\t"
      "v_mfma_f32_16x16x32_bf16 a[84:87], v[20:23], v[36:39], a[84:87]\n\t"
      "v_mul_f32 v198, v198, v218\n\t"
      "v_mfma_f32_16x16x32_bf16 a[104:107], v[20:23], v[40:43], a[104:107]\n\t"
      "buffer_load_dwordx4 v[188:191], v0, s[36:39], s49 offen offset:2048 sc1\n\t"
      "v_mfma_f32_16x16x32_bf16 a[124:127], v[20:23], v[44:47], a[124:127]\n\t"
      "v_mul_f32 v199, v199, v219\n\t"
      "v_mfma_f32_16x16x32_bf16 a[144:147], v[20:23], v[48:51], a[144:147]\n\t"
      "buffer_load_dwordx4 v[192:195], v0, s[36:39], s49 offen offset:3072 sc1\n\t"
      "v_mfma_f32_16x16x32_bf16 a[88:91], v[24:27], v[36:39], a[88:91]\n\t"
      "v_mul_f32 v200, v200, v220\n\t"
      "v_mul_f32 v201, v201, v221\n\t"
      "v_mfma_f32_16x16x32_bf16 a[108:111], v[24:27], v[40:43], a[108:111]\n\t"
      "buffer_load_dwordx4 v[160:163], v0, s[36:39], s48 offen sc1\n\t"
      "v_mfma_f32_16x16x32_bf16 a[128:131], v[24:27], v[44:47], a[128:131]\n\t"
      "v_mul_f32 v202, v202, v222\n\t"
      "v_mul_f32 v203, v203, v223\n\t"
      "v_mfma_f32_16x16x32_bf16 a[148:151], v[24:27], v[48:51], a[148:151]\n\t"
      "buffer_load_dwordx4 v[164:167], v0, s[36:39], s48 offen offset:1024 sc1\n\t"
      "v_mfma_f32_16x16x32_bf16 a[92:95], v[28:31], v[36:39], a[92:95]\n\t"
      "v_mul_f32 v204, v204, v224\n\t"
      "v_mul_f32 v205, v205, v225\n\t"
      "v_mfma_f32_16x16x32_bf16 a[112:115], v[28:31], v[40:43], a[112:115]\n\t"
      "buffer_load_dwordx4 v[168:171], v0, s[36:39], s48 offen offset:2048 sc1\n\t"
      "v_mfma_f32_16x16x32_bf16 a[132:135], v[28:31], v[44:47], a[132:135]\n\t"
      "v_mul_f32 v206, v206, v226\n\t"
      "v_mul_f32 v207, v207, v227\n\t"
      "v_mfma_f32_16x16x32_bf16 a[152:155], v[28:31], v[48:51], a[152:155]\n\t"
      "buffer_load_dwordx4 v[172:175], v0, s[36:39], s48 offen offset:3072 sc1\n\t"
      "v_mfma_f32_16x16x32_bf16 a[96:99], v[32:35], v[36:39], a[96:99]\n\t"
      "v_mul_f32 v208, v208, v228\n\t"
      "v_mul_f32 v209, v209, v229\n\t"
      "v_mfma_f32_16x16x32_bf16 a[116:119], v[32:35], v[40:43], a[116:119]\n\t"
      "buffer_load_dwordx4 v[176:179], v1, s[36:39], s48 offen sc1\n\t"
      "v_mfma_f32_16x16x32_bf16 a[136:139], v[32:35], v[44:47], a[136:139]\n\t"
      "v_mul_f32 v210, v210, v230\n\t"
      "v_mul_f32 v211, v211, v231\n\t"
      "v_mfma_f32_16x16x32_bf16 a[156:159], v[32:35], v[48:51], a[156:159]\n\t"
      "v_mul_f32 v212, v212, v232\n\t"
      "v_mul_f32 v213, v213, v233\n\t"
      "v_mul_f32 v214, v214, v234\n\t"
      "v_mul_f32 v215, v215, v235\n\t"
      "s_add_u32 s50, s51, 0x54\n\t"
      "s_lshl_b32 s49, s50, 12\n\t"
      "s_add_u32 s49, s49, s53\n\t"
      "s_add_u32 s50, s50, 54\n\t"
      "s_mul_i32 s48, s50, 0x1400\n\t"
      "s_add_u32 s48, s48, s54\n\t"
      "s_waitcnt vmcnt(27)\n\t"
      "v_mfma_f32_16x16x32_bf16 a[80:83], v[52:55], v[72:75], a[80:83]\n\t"
      "v_lshlrev_b32 v216, 16, v236\n\t"
      "v_mfma_f32_16x16x32_bf16 a[100:103], v[52:55], v[76:79], a[100:103]\n\t"
      "buffer_load_dwordx4 v[36:39], v0, s[36:39], s49 offen sc1\n\t"
      "v_mfma_f32_16x16x32_bf16 a[120:123], v[52:55], v[80:83], a[120:123]\n\t"
      "v_and_b32 v217, 0xffff0000, v236\n\t"
      "v_mfma_f32_16x16x32_bf16 a[140:143], v[52:55], v[84:87], a[140:143]\n\t"
      "buffer_load_dwordx4 v[40:43], v0, s[36:39], s49 offen offset:1024 sc1\n\t"
      "v_mfma_f32_16x16x32_bf16 a[84:87], v[56:59], v[72:75], a[84:87]\n\t"
      "v_lshlrev_b32 v218, 16, v237\n\t"
      "v_mfma_f32_16x16x32_bf16 a[104:107], v[56:59], v[76:79], a[104:107]\n\t"
      "buffer_load_dwordx4 v[44:47], v0, s[36:39], s49 offen offset:2048 sc1\n\t"
      "v_mfma_f32_16x16x32_bf16 a[124:127], v[56:59], v[80:83], a[124:127]\n\t"
      "v_and_b32 v219, 0xffff0000, v237\n\t"
      "v_mfma_f32_16x16x32_bf16 a[144:147], v[56:59], v[84:87], a[144:147]\n\t"
      "buffer_load_dwordx4 v[48:51], v0, s[36:39], s49 offen offset:3072 sc1\n\t"
      "v_mfma_f32_16x16x32_bf16 a[88:91], v[60:63], v[72:75], a[88:91]\n\t"
      "v_lshlrev_b32 v220, 16, v238\n\t"
      "v_and_b32 v221, 0xffff0000, v238\n\t"
      "v_mfma_f32_16x16x32_bf16 a[108:111], v[60:63], v[76:79], a[108:111]\n\t"
      "buffer_load_dwordx4 v[16:19], v0, s[36:39], s48 offen sc1\n\t"
      "v_mfma_f32_16x16x32_bf16 a[128:131], v[60:63], v[80:83], a[128:131]\n\t"
      "v_lshlrev_b32 v222, 16, v239\n\t"
      "v_and_b32 v223, 0xffff0000, v239\n\t"
      "v_mfma_f32_16x16x32_bf16 a[148:151], v[60:63], v[84:87], a[148:151]\n\t"
      "buffer_load_dwordx4 v[20:23], v0, s[36:39], s48 offen offset:1024 sc1\n\t"
      "v_mfma_f32_16x16x32_bf16 a[92:95], v[64:67], v[72:75], a[92:95]\n\t"
      "v_lshlrev_b32 v224, 16, v240\n\t"
      "v_and_b32 v225, 0xffff0000, v240\n\t"
      "v_mfma_f32_16x16x32_bf16 a[112:115], v[64:67], v[76:79], a[112:115]\n\t"
      "buffer_load_dwordx4 v[24:27], v0, s[36:39], s48 offen offset:2048 sc1\n\t"
      "v_mfma_f32_16x16x32_bf16 a[132:135], v[64:67], v[80:83], a[132:135]\n\t"
      "v_lshlrev_b32 v226, 16, v241\n\t"
      "v_and_b32 v227, 0xffff0000, v241\n\t"
      "v_mfma_f32_16x16x32_bf16 a[152:155], v[64:67], v[84:87], a[152:155]\n\t"
      "buffer_load_dwordx4 v[28:31], v0, s[36:39], s48 offen offset:3072 sc1\n\t"
      "v_mfma_f32_16x16x32_bf16 a[96:99], v[68:71], v[72:75], a[96:99]\n\t"
      "v_lshlrev_b32 v228, 16, v242\n\t"
      "v_and_b32 v229, 0xffff0000, v242\n\t"
      "v_mfma_f32_16x16x32_bf16 a[116:119], v[68:71], v[76:79], a[116:119]\n\t"
      "buffer_load_dwordx4 v[32:35], v1, s[36:39], s48 offen sc1\n\t"
      "v_mfma_f32_16x16x32_bf16 a[136:139], v[68:71], v[80:83], a[136:139]\n\t"
      "v_lshlrev_b32 v230, 16, v243\n\t"
      "v_and_b32 v231, 0xffff0000, v243\n\t"
      "v_mfma_f32_16x16x32_bf16 a[156:159], v[68:71], v[84:87], a[156:159]\n\t"
      "v_lshlrev_b32 v232, 16, v244\n\t"
      "v_and_b32 v233, 0xffff0000, v244\n\t"
      "v_lshlrev_b32 v234, 16, v245\n\t"
      "v_and_b32 v235, 0xffff0000, v245\n\t"
      "s_add_u32 s50, s51, 0x58\n\t"
      "s_lshl_b32 s49, s50, 12\n\t"
      "s_add_u32 s49, s49, s53\n\t"
      "s_add_u32 s50, s50, 54\n\t"
      "s_mul_i32 s48, s50, 0x1400\n\t"
      "s_add_u32 s48, s48, s54\n\t"
      "s_waitcnt vmcnt(27)\n\t"
      "v_mfma_f32_16x16x32_bf16 a[80:83], v[88:91], v[108:111], a[80:83]\n\t"
      "v_mfma_f32_16x16x32_bf16 a[100:103], v[88:91], v[112:115], a[100:103]\n\t"
      "buffer_load_dwordx4 v[72:75], v0, s[36:39], s49 offen sc1\n\t"
      "v_mfma_f32_16x16x32_bf16 a[120:123], v[88:91], v[116:119], a[120:123]\n\t"
      "v_mfma_f32_16x16x32_bf16 a[140:143], v[88:91], v[120:123], a[140:143]\n\t"
      "buffer_load_dwordx4 v[76:79], v0, s[36:39], s49 offen offset:1024 sc1\n\t"
      "v_mfma_f32_16x16x32_bf16 a[84:87], v[92:95], v[108:111], a[84:87]\n\t"
      "v_mfma_f32_16x16x32_bf16 a[104:107], v[92:95], v[112:115], a[104:107]\n\t"
      "buffer_load_dwordx4 v[80:83], v0, s[36:39], s49 offen offset:2048 sc1\n\t"
      "v_mfma_f32_16x16x32_bf16 a[124:127], v[92:95], v[116:119], a[124:127]\n\t"
      "v_mfma_f32_16x16x32_bf16 a[144:147], v[92:95], v[120:123], a[144:147]\n\t"
      "buffer_load_dwordx4 v[84:87], v0, s[36:39], s49 offen offset:3072 sc1\n\t"
      "v_mfma_f32_16x16x32_bf16 a[88:91], v[96:99], v[108:111], a[88:91]\n\t"
      "v_mfma_f32_16x16x32_bf16 a[108:111], v[96:99], v[112:115], a[108:111]\n\t"
      "buffer_load_dwordx4 v[52:55], v0, s[36:39], s48 offen sc1\n\t"
      "v_mfma_f32_16x16x32_bf16 a[128:131], v[96:99], v[116:119], a[128:131]\n\t"
      "v_mfma_f32_16x16x32_bf16 a[148:151], v[96:99], v[120:123], a[148:151]\n\t"
      "buffer_load_dwordx4 v[56:59], v0, s[36:39], s48 offen offset:1024 sc1\n\t"
      "v_mfma_f32_16x16x32_bf16 a[92:95], v[100:103], v[108:111], a[92:95]\n\t"
      "v_mfma_f32_16x16x32_bf16 a[112:115], v[100:103], v[112:115], a[112:115]\n\t"
      "buffer_load_dwordx4 v[60:63], v0, s[36:39], s48 offen offset:2048 sc1\n\t"
      "v_mfma_f32_16x16x32_bf16 a[132:135], v[100:103], v[116:119], a[132:135]\n\t"
      "v_mfma_f32_16x16x32_bf16 a[152:155], v[100:103], v[120:123], a[152:155]\n\t"
      "buffer_load_dwordx4 v[64:67], v0, s[36:39], s48 offen offset:3072 sc1\n\t"
      "v_mfma_f32_16x16x32_bf16 a[96:99], v[104:107], v[108:111], a[96:99]\n\t"
      "v_mfma_f32_16x16x32_bf16 a[116:119], v[104:107], v[112:115], a[116:119]\n\t"
      "buffer_load_dwordx4 v[68:71], v1, s[36:39], s48 offen sc1\n\t"
      "v_mfma_f32_16x16x32_bf16 a[136:139], v[104:107], v[116:119], a[136:139]\n\t"
      "v_mfma_f32_16x16x32_bf16 a[156:159], v[104:107], v[120:123], a[156:159]\n\t"
      "s_add_u32 s50, s51, 0x5c\n\t"
      "s_lshl_b32 s49, s50, 12\n\t"
      "s_add_u32 s49, s49, s53\n\t"
      "s_add_u32 s50, s50, 54\n\t"
      "s_mul_i32 s48, s50, 0x1400\n\t"
      "s_add_u32 s48, s48, s54\n\t"
      "s_waitcnt vmcnt(27)\n\t"
      "v_mfma_f32_16x16x32_bf16 a[80:83], v[124:127], v[144:147], a[80:83]\n\t"
      "v_mfma_f32_16x16x32_bf16 a[100:103], v[124:127], v[148:151], a[100:103]\n\t"
      "buffer_load_dwordx4 v[108:111], v0, s[36:39], s49 offen sc1\n\t"
      "v_mfma_f32_16x16x32_bf16 a[120:123], v[124:127], v[152:155], a[120:123]\n\t"
      "v_mfma_f32_16x16x32_bf16 a[140:143], v[124:127], v[156:159], a[140:143]\n\t"
      "buffer_load_dwordx4 v[112:115], v0, s[36:39], s49 offen offset:1024 sc1\n\t"
      "v_mfma_f32_16x16x32_bf16 a[84:87], v[128:131], v[144:147], a[84:87]\n\t"
      "v_mfma_f32_16x16x32_bf16 a[104:107], v[128:131], v[148:151], a[104:107]\n\t"
      "buffer_load_dwordx4 v[116:119], v0, s[36:39], s49 offen offset:2048 sc1\n\t"
      "v_mfma_f32_16x16x32_bf16 a[124:127], v[128:131], v[152:155], a[124:127]\n\t"
      "v_mfma_f32_16x16x32_bf16 a[144:147], v[128:131], v[156:159], a[144:147]\n\t"
      "buffer_load_dwordx4 v[120:123], v0, s[36:39], s49 offen offset:3072 sc1\n\t"
      "v_mfma_f32_16x16x32_bf16 a[88:91], v[132:135], v[144:147], a[88:91]\n\t"
      "v_mfma_f32_16x16x32_bf16 a[108:111], v[132:135], v[148:151], a[108:111]\n\t"
      "buffer_load_dwordx4 v[88:91], v0, s[36:39], s48 offen sc1\n\t"
      "v_mfma_f32_16x16x32_bf16 a[128:131], v[132:135], v[152:155], a[128:131]\n\t"
      "v_mfma_f32_16x16x32_bf16 a[148:151], v[132:135], v[156:159], a[148:151]\n\t"
      "buffer_load_dwordx4 v[92:95], v0, s[36:39], s48 offen offset:1024 sc1\n\t"
      "v_mfma_f32_16x16x32_bf16 a[92:95], v[136:139], v[144:147], a[92:95]\n\t"
      "v_mfma_f32_16x16x32_bf16 a[112:115], v[136:139], v[148:151], a[112:115]\n\t"
      "buffer_load_dwordx4 v[96:99], v0, s[36:39], s48 offen offset:2048 sc1\n\t"
      "v_mfma_f32_16x16x32_bf16 a[132:135], v[136:139], v[152:155], a[132:135]\n\t"
      "v_mfma_f32_16x16x32_bf16 a[152:155], v[136:139], v[156:159], a[152:155]\n\t"
      "buffer_load_dwordx4 v[100:103], v0, s[36:39], s48 offen offset:3072 sc1\n\t"
      "v_mfma_f32_16x16x32_bf16 a[96:99], v[140:143], v[144:147], a[96:99]\n\t"
      "v_mfma_f32_16x16x32_bf16 a[116:119], v[140:143], v[148:151], a[116:119]\n\t"
      "buffer_load_dwordx4 v[104:107], v1, s[36:39], s48 offen sc1\n\t"
      "v_mfma_f32_16x16x32_bf16 a[136:139], v[140:143], v[152:155], a[136:139]\n\t"
      "v_mfma_f32_16x16x32_bf16 a[156:159], v[140:143], v[156:159], a[156:159]\n\t"
      "s_add_u32 s50, s51, 0x60\n\t"
      "s_lshl_b32 s49, s50, 12\n\t"
      "s_add_u32 s49, s49, s53\n\t"
      "s_add_u32 s50, s50, 54\n\t"
      "s_mul_i32 s48, s50, 0x1400\n\t"
      "s_add_u32 s48, s48, s54\n\t"
      "s_waitcnt vmcnt(27)\n\t"
      "v_mfma_f32_16x16x32_bf16 a[80:83], v[160:163], v[180:183], a[80:83]\n\t"
      "v_mfma_f32_16x16x32_bf16 a[100:103], v[160:163], v[184:187], a[100:103]\n\t"
      "buffer_load_dwordx4 v[144:147], v0, s[36:39], s49 offen sc1\n\t"
      "v_mfma_f32_16x16x32_bf16 a[120:123], v[160:163], v[188:191], a[120:123]\n\t"
      "v_mfma_f32_16x16x32_bf16 a[140:143], v[160:163], v[192:195], a[140:143]\n\t"
      "buffer_load_dwordx4 v[148:151], v0, s[36:39], s49 offen offset:1024 sc1\n\t"
      "v_mfma_f32_16x16x32_bf16 a[84:87], v[164:167], v[180:183], a[84:87]\n\t"
      "v_mfma_f32_16x16x32_bf16 a[104:107], v[164:167], v[184:187], a[104:107]\n\t"
      "buffer_load_dwordx4 v[152:155], v0, s[36:39], s49 offen offset:2048 sc1\n\t"
      "v_mfma_f32_16x16x32_bf16 a[124:127], v[164:167], v[188:191], a[124:127]\n\t"
      "v_mfma_f32_16x16x32_bf16 a[144:147], v[164:167], v[192:195], a[144:147]\n\t"
      "buffer_load_dwordx4 v[156:159], v0, s[36:39], s49 offen offset:3072 sc1\n\t"
      "v_mfma_f32_16x16x32_bf16 a[88:91], v[168:171], v[180:183], a[88:91]\n\t"
      "v_mfma_f32_16x16x32_bf16 a[108:111], v[168:171], v[184:187], a[108:111]\n\t"
      "buffer_load_dwordx4 v[124:127], v0, s[36:39], s48 offen sc1\n\t"
      "v_mfma_f32_16x16x32_bf16 a[128:131], v[168:171], v[188:191], a[128:131]\n\t"
      "v_mfma_f32_16x16x32_bf16 a[148:151], v[168:171], v[192:195], a[148:151]\n\t"
      "buffer_load_dwordx4 v[128:131], v0, s[36:39], s48 offen offset:1024 sc1\n\t"
      "v_mfma_f32_16x16x32_bf16 a[92:95], v[172:175], v[180:183], a[92:95]\n\t"
      "v_mfma_f32_16x16x32_bf16 a[112:115], v[172:175], v[184:187], a[112:115]\n\t"
      "buffer_load_dwordx4 v[132:135], v0, s[36:39], s48 offen offset:2048 sc1\n\t"
      "v_mfma_f32_16x16x32_bf16 a[132:135], v[172:175], v[188:191], a[132:135]\n\t"
      "v_mfma_f32_16x16x32_bf16 a[152:155], v[172:175], v[192:195], a[152:155]\n\t"
      "buffer_load_dwordx4 v[136:139], v0, s[36:39], s48 offen offset:3072 sc1\n\t"
      "v_mfma_f32_16x16x32_bf16 a[96:99], v[176:179], v[180:183], a[96:99]\n\t"
      "v_mfma_f32_16x16x32_bf16 a[116:119], v[176:179], v[184:187], a[116:119]\n\t"
      "buffer_load_dwordx4 v[140:143], v1, s[36:39], s48 offen sc1\n\t"
      "v_mfma_f32_16x16x32_bf16 a[136:139], v[176:179], v[188:191], a[136:139]\n\t"
      "v_mfma_f32_16x16x32_bf16 a[156:159], v[176:179], v[192:195], a[156:159]\n\t"
      "s_add_u32 s50, s51, 0x64\n\t"
      "s_lshl_b32 s49, s50, 12\n\t"
      "s_add_u32 s49, s49, s53\n\t"
      "s_add_u32 s50, s50, 54\n\t"
      "s_mul_i32 s48, s50, 0x1400\n\t"
      "s_add_u32 s48, s48, s54\n\t"
      "s_waitcnt vmcnt(27)\n\t"
      "v_mfma_f32_16x16x32_bf16 a[80:83], v[16:19], v[36:39], a[80:83]\n\t"
      "v_mfma_f32_16x16x32_bf16 a[100:103], v[16:19], v[40:43], a[100:103]\n\t"
      "buffer_load_dwordx4 v[180:183], v0, s[36:39], s49 offen sc1\n\t"
      "v_mfma_f32_16x16x32_bf16 a[120:123], v[16:19], v[44:47], a[120:123]\n\t"
      "v_mfma_f32_16x16x32_bf16 a[140:143], v[16:19], v[48:51], a[140:143]\n\t"
      "buffer_load_dwordx4 v[184:187], v0, s[36:39], s49 offen offset:1024 sc1\n\t"
      "v_mfma_f32_16x16x32_bf16 a[84:87], v[20:23], v[36:39], a[84:87]\n\t"
      "v_mfma_f32_16x16x32_bf16 a[104:107], v[20:23], v[40:43], a[104:107]\n\t"
      "buffer_load_dwordx4 v[188:191], v0, s[36:39], s49 offen offset:2048 sc1\n\t"
      "v_mfma_f32_16x16x32_bf16 a[124:127], v[20:23], v[44:47], a[124:127]\n\t"
      "v_mfma_f32_16x16x32_bf16 a[144:147], v[20:23], v[48:51], a[144:147]\n\t"
      "buffer_load_dwordx4 v[192:195], v0, s[36:39], s49 offen offset:3072 sc1\n\t"
      "v_mfma_f32_16x16x32_bf16 a[88:91], v[24:27], v[36:39], a[88:91]\n\t"
      "v_mfma_f32_16x16x32_bf16 a[108:111], v[24:27], v[40:43], a[108:111]\n\t"
      "buffer_load_dwordx4 v[160:163], v0, s[36:39], s48 offen sc1\n\t"
      "v_mfma_f32_16x16x32_bf16 a[128:131], v[24:27], v[44:47], a[128:131]\n\t"
      "v_mfma_f32_16x16x32_bf16 a[148:151], v[24:27], v[48:51], a[148:151]\n\t"
      "buffer_load_dwordx4 v[164:167], v0, s[36:39], s48 offen offset:1024 sc1\n\t"
      "v_mfma_f32_16x16x32_bf16 a[92:95], v[28:31], v[36:39], a[92:95]\n\t"
      "v_mfma_f32_16x16x32_bf16 a[112:115], v[28:31], v[40:43], a[112:115]\n\t"
      "buffer_load_dwordx4 v[168:171], v0, s[36:39], s48 offen offset:2048 sc1\n\t"
      "v_mfma_f32_16x16x32_bf16 a[132:135], v[28:31], v[44:47], a[132:135]\n\t"
      "v_mfma_f32_16x16x32_bf16 a[152:155], v[28:31], v[48:51], a[152:155]\n\t"
      "buffer_load_dwordx4 v[172:175], v0, s[36:39], s48 offen offset:3072 sc1\n\t"
      "v_mfma_f32_16x16x32_bf16 a[96:99], v[32:35], v[36:39], a[96:99]\n\t"
      "v_mfma_f32_16x16x32_bf16 a[116:119], v[32:35], v[40:43], a[116:119]\n\t"
      "buffer_load_dwordx4 v[176:179], v1, s[36:39], s48 offen sc1\n\t"
      "v_mfma_f32_16x16x32_bf16 a[136:139], v[32:35], v[44:47], a[136:139]\n\t"
      "v_mfma_f32_16x16x32_bf16 a[156:159], v[32:35], v[48:51], a[156:159]\n\t"
      "s_add_u32 s50, s51, 0x68\n\t"
      "s_lshl_b32 s49, s50, 12\n\t"
      "s_add_u32 s49, s49, s53\n\t"
      "s_add_u32 s50, s50, 54\n\t"
      "s_mul_i32 s48, s50, 0x1400\n\t"
      "s_add_u32 s48, s48, s54\n\t"
      "s_waitcnt vmcnt(27)\n\t"
      "v_mfma_f32_16x16x32_bf16 a[80:83], v[52:55], v[72:75], a[80:83]\n\t"
      "v_mfma_f32_16x16x32_bf16 a[100:103], v[52:55], v[76:79], a[100:103]\n\t"
      "buffer_load_dwordx4 v[36:39], v0, s[36:39], s49 offen sc1\n\t"
      "v_mfma_f32_16x16x32_bf16 a[120:123], v[52:55], v[80:83], a[120:123]\n\t"
      "v_mfma_f32_16x16x32_bf16 a[140:143], v[52:55], v[84:87], a[140:143]\n\t"
      "buffer_load_dwordx4 v[40:43], v0, s[36:39], s49 offen offset:1024 sc1\n\t"
      "v_mfma_f32_16x16x32_bf16 a[84:87], v[56:59], v[72:75], a[84:87]\n\t"
      "v_mfma_f32_16x16x32_bf16 a[104:107], v[56:59], v[76:79], a[104:107]\n\t"
      "buffer_load_dwordx4 v[44:47], v0, s[36:39], s49 offen offset:2048 sc1\n\t"
      "v_mfma_f32_16x16x32_bf16 a[124:127], v[56:59], v[80:83], a[124:127]\n\t"
      "v_mfma_f32_16x16x32_bf16 a[144:147], v[56:59], v[84:87], a[144:147]\n\t"
      "buffer_load_dwordx4 v[48:51], v0, s[36:39], s49 offen offset:3072 sc1\n\t"
      "v_mfma_f32_16x16x32_bf16 a[88:91], v[60:63], v[72:75], a[88:91]\n\t"
      "v_mfma_f32_16x16x32_bf16 a[108:111], v[60:63], v[76:79], a[108:111]\n\t"
      "buffer_load_dwordx4 v[16:19], v0, s[36:39], s48 offen sc1\n\t"
      "v_mfma_f32_16x16x32_bf16 a[128:131], v[60:63], v[80:83], a[128:131]\n\t"
      "v_mfma_f32_16x16x32_bf16 a[148:151], v[60:63], v[84:87], a[148:151]\n\t"
      "buffer_load_dwordx4 v[20:23], v0, s[36:39], s48 offen offset:1024 sc1\n\t"
      "v_mfma_f32_16x16x32_bf16 a[92:95], v[64:67], v[72:75], a[92:95]\n\t"
      "v_mfma_f32_16x16x32_bf16 a[112:115], v[64:67], v[76:79], a[112:115]\n\t"
      "buffer_load_dwordx4 v[24:27], v0, s[36:39], s48 offen offset:2048 sc1\n\t"
      "v_mfma_f32_16x16x32_bf16 a[132:135], v[64:67], v[80:83], a[132:135]\n\t"
      "v_mfma_f32_16x16x32_bf16 a[152:155], v[64:67], v[84:87], a[152:155]\n\t"
      "buffer_load_dwordx4 v[28:31], v0, s[36:39], s48 offen offset:3072 sc1\n\t"
      "v_mfma_f32_16x16x32_bf16 a[96:99], v[68:71], v[72:75], a[96:99]\n\t"
      "v_mfma_f32_16x16x32_bf16 a[116:119], v[68:71], v[76:79], a[116:119]\n\t"
      "buffer_load_dwordx4 v[32:35], v1, s[36:39], s48 offen sc1\n\t"
      "v_mfma_f32_16x16x32_bf16 a[136:139], v[68:71], v[80:83], a[136:139]\n\t"
      "v_mfma_f32_16x16x32_bf16 a[156:159], v[68:71], v[84:87], a[156:159]\n\t"
      "s_add_u32 s50, s51, 0x6c\n\t"
      "s_lshl_b32 s49, s50, 12\n\t"
      "s_add_u32 s49, s49, s53\n\t"
      "s_add_u32 s50, s50, 54\n\t"
      "s_mul_i32 s48, s50, 0x1400\n\t"
      "s_add_u32 s48, s48, s54\n\t"
      "s_waitcnt vmcnt(27)\n\t"
      "v_mfma_f32_16x16x32_bf16 a[80:83], v[88:91], v[108:111], a[80:83]\n\t"
      "v_mfma_f32_16x16x32_bf16 a[100:103], v[88:91], v[112:115], a[100:103]\n\t"
      "buffer_load_dwordx4 v[72:75], v0, s[36:39], s49 offen sc1\n\t"
      "v_mfma_f32_16x16x32_bf16 a[120:123], v[88:91], v[116:119], a[120:123]\n\t"
      "v_mfma_f32_16x16x32_bf16 a[140:143], v[88:91], v[120:123], a[140:143]\n\t"
      "buffer_load_dwordx4 v[76:79], v0, s[36:39], s49 offen offset:1024 sc1\n\t"
      "v_mfma_f32_16x16x32_bf16 a[84:87], v[92:95], v[108:111], a[84:87]\n\t"
      "v_mfma_f32_16x16x32_bf16 a[104:107], v[92:95], v[112:115], a[104:107]\n\t"
      "buffer_load_dwordx4 v[80:83], v0, s[36:39], s49 offen offset:2048 sc1\n\t"
      "v_mfma_f32_16x16x32_bf16 a[124:127], v[92:95], v[116:119], a[124:127]\n\t"
      "v_mfma_f32_16x16x32_bf16 a[144:147], v[92:95], v[120:123], a[144:147]\n\t"
      "buffer_load_dwordx4 v[84:87], v0, s[36:39], s49 offen offset:3072 sc1\n\t"
      "v_mfma_f32_16x16x32_bf16 a[88:91], v[96:99], v[108:111], a[88:91]\n\t"
      "v_mfma_f32_16x16x32_bf16 a[108:111], v[96:99], v[112:115], a[108:111]\n\t"
      "buffer_load_dwordx4 v[52:55], v0, s[36:39], s48 offen sc1\n\t"
      "v_mfma_f32_16x16x32_bf16 a[128:131], v[96:99], v[116:119], a[128:131]\n\t"
      "v_mfma_f32_16x16x32_bf16 a[148:151], v[96:99], v[120:123], a[148:151]\n\t"
      "buffer_load_dwordx4 v[56:59], v0, s[36:39], s48 offen offset:1024 sc1\n\t"
      "v_mfma_f32_16x16x32_bf16 a[92:95], v[100:103], v[108:111], a[92:95]\n\t"
      "v_mfma_f32_16x16x32_bf16 a[112:115], v[100:103], v[112:115], a[112:115]\n\t"
      "buffer_load_dwordx4 v[60:63], v0, s[36:39], s48 offen offset:2048 sc1\n\t"
      "v_mfma_f32_16x16x32_bf16 a[132:135], v[100:103], v[116:119], a[132:135]\n\t"
      "v_mfma_f32_16x16x32_bf16 a[152:155], v[100:103], v[120:123], a[152:155]\n\t"
      "buffer_load_dwordx4 v[64:67], v0, s[36:39], s48 offen offset:3072 sc1\n\t"
      "v_mfma_f32_16x16x32_bf16 a[96:99], v[104:107], v[108:111], a[96:99]\n\t"
      "v_mfma_f32_16x16x32_bf16 a[116:119], v[104:107], v[112:115], a[116:119]\n\t"
      "buffer_load_dwordx4 v[68:71], v1, s[36:39], s48 offen sc1\n\t"
      "v_mfma_f32_16x16x32_bf16 a[136:139], v[104:107], v[116:119], a[136:139]\n\t"
      "v_mfma_f32_16x16x32_bf16 a[156:159], v[104:107], v[120:123], a[156:159]\n\t"
      "s_add_u32 s50, s51, 0x70\n\t"
      "s_lshl_b32 s49, s50, 12\n\t"
      "s_add_u32 s49, s49, s53\n\t"
      "s_add_u32 s50, s50, 54\n\t"
      "s_mul_i32 s48, s50, 0x1400\n\t"
      "s_add_u32 s48, s48, s54\n\t"
      "s_waitcnt vmcnt(27)\n\t"
      "v_mfma_f32_16x16x32_bf16 a[80:83], v[124:127], v[144:147], a[80:83]\n\t"
      "v_mfma_f32_16x16x32_bf16 a[100:103], v[124:127], v[148:151], a[100:103]\n\t"
      "buffer_load_dwordx4 v[108:111], v0, s[36:39], s49 offen sc1\n\t"
      "v_mfma_f32_16x16x32_bf16 a[120:123], v[124:127], v[152:155], a[120:123]\n\t"
      "v_mfma_f32_16x16x32_bf16 a[140:143], v[124:127], v[156:159], a[140:143]\n\t"
      "buffer_load_dwordx4 v[112:115], v0, s[36:39], s49 offen offset:1024 sc1\n\t"
      "v_mfma_f32_16x16x32_bf16 a[84:87], v[128:131], v[144:147], a[84:87]\n\t"
      "v_mfma_f32_16x16x32_bf16 a[104:107], v[128:131], v[148:151], a[104:107]\n\t"
      "buffer_load_dwordx4 v[116:119], v0, s[36:39], s49 offen offset:2048 sc1\n\t"
      "v_mfma_f32_16x16x32_bf16 a[124:127], v[128:131], v[152:155], a[124:127]\n\t"
      "v_mfma_f32_16x16x32_bf16 a[144:147], v[128:131], v[156:159], a[144:147]\n\t"
      "buffer_load_dwordx4 v[120:123], v0, s[36:39], s49 offen offset:3072 sc1\n\t"
      "v_mfma_f32_16x16x32_bf16 a[88:91], v[132:135], v[144:147], a[88:91]\n\t"
      "v_mfma_f32_16x16x32_bf16 a[108:111], v[132:135], v[148:151], a[108:111]\n\t"
      "buffer_load_dwordx4 v[88:91], v0, s[36:39], s48 offen sc1\n\t"
      "v_mfma_f32_16x16x32_bf16 a[128:131], v[132:135], v[152:155], a[128:131]\n\t"
      "v_mfma_f32_16x16x32_bf16 a[148:151], v[132:135], v[156:159], a[148:151]\n\t"
      "buffer_load_dwordx4 v[92:95], v0, s[36:39], s48 offen offset:1024 sc1\n\t"
      "v_mfma_f32_16x16x32_bf16 a[92:95], v[136:139], v[144:147], a[92:95]\n\t"
      "v_mfma_f32_16x16x32_bf16 a[112:115], v[136:139], v[148:151], a[112:115]\n\t"
      "buffer_load_dwordx4 v[96:99], v0, s[36:39], s48 offen offset:2048 sc1\n\t"
      "v_mfma_f32_16x16x32_bf16 a[132:135], v[136:139], v[152:155], a[132:135]\n\t"
      "v_mfma_f32_16x16x32_bf16 a[152:155], v[136:139], v[156:159], a[152:155]\n\t"
      "buffer_load_dwordx4 v[100:103], v0, s[36:39], s48 offen offset:3072 sc1\n\t"
      "v_mfma_f32_16x16x32_bf16 a[96:99], v[140:143], v[144:147], a[96:99]\n\t"
      "v_mfma_f32_16x16x32_bf16 a[116:119], v[140:143], v[148:151], a[116:119]\n\t"
      "buffer_load_dwordx4 v[104:107], v1, s[36:39], s48 offen sc1\n\t"
      "v_mfma_f32_16x16x32_bf16 a[136:139], v[140:143], v[152:155], a[136:139]\n\t"
      "v_mfma_f32_16x16x32_bf16 a[156:159], v[140:143], v[156:159], a[156:159]\n\t"
      "s_add_u32 s50, s51, 0x74\n\t"
      "s_lshl_b32 s49, s50, 12\n\t"
      "s_add_u32 s49, s49, s53\n\t"
      "s_add_u32 s50, s50, 54\n\t"
      "s_mul_i32 s48, s50, 0x1400\n\t"
      "s_add_u32 s48, s48, s54\n\t"
      "s_waitcnt vmcnt(27)\n\t"
      "v_mfma_f32_16x16x32_bf16 a[80:83], v[160:163], v[180:183], a[80:83]\n\t"
      "v_mfma_f32_16x16x32_bf16 a[100:103], v[160:163], v[184:187], a[100:103]\n\t"
      "buffer_load_dwordx4 v[144:147], v0, s[36:39], s49 offen sc1\n\t"
      "v_mfma_f32_16x16x32_bf16 a[120:123], v[160:163], v[188:191], a[120:123]\n\t"
      "v_mfma_f32_16x16x32_bf16 a[140:143], v[160:163], v[192:195], a[140:143]\n\t"
      "buffer_load_dwordx4 v[148:151], v0, s[36:39], s49 offen offset:1024 sc1\n\t"
      "v_mfma_f32_16x16x32_bf16 a[84:87], v[164:167], v[180:183], a[84:87]\n\t"
      "v_mfma_f32_16x16x32_bf16 a[104:107], v[164:167], v[184:187], a[104:107]\n\t"
      "buffer_load_dwordx4 v[152:155], v0, s[36:39], s49 offen offset:2048 sc1\n\t"
      "v_mfma_f32_16x16x32_bf16 a[124:127], v[164:167], v[188:191], a[124:127]\n\t"
      "v_mfma_f32_16x16x32_bf16 a[144:147], v[164:167], v[192:195], a[144:147]\n\t"
      "buffer_load_dwordx4 v[156:159], v0, s[36:39], s49 offen offset:3072 sc1\n\t"
      "v_mfma_f32_16x16x32_bf16 a[88:91], v[168:171], v[180:183], a[88:91]\n\t"
      "v_mfma_f32_16x16x32_bf16 a[108:111], v[168:171], v[184:187], a[108:111]\n\t"
      "buffer_load_dwordx4 v[124:127], v0, s[36:39], s48 offen sc1\n\t"
      "v_mfma_f32_16x16x32_bf16 a[128:131], v[168:171], v[188:191], a[128:131]\n\t"
      "v_mfma_f32_16x16x32_bf16 a[148:151], v[168:171], v[192:195], a[148:151]\n\t"
      "buffer_load_dwordx4 v[128:131], v0, s[36:39], s48 offen offset:1024 sc1\n\t"
      "v_mfma_f32_16x16x32_bf16 a[92:95], v[172:175], v[180:183], a[92:95]\n\t"
      "v_mfma_f32_16x16x32_bf16 a[112:115], v[172:175], v[184:187], a[112:115]\n\t"
      "buffer_load_dwordx4 v[132:135], v0, s[36:39], s48 offen offset:2048 sc1\n\t"
      "v_mfma_f32_16x16x32_bf16 a[132:135], v[172:175], v[188:191], a[132:135]\n\t"
      "v_mfma_f32_16x16x32_bf16 a[152:155], v[172:175], v[192:195], a[152:155]\n\t"
      "buffer_load_dwordx4 v[136:139], v0, s[36:39], s48 offen offset:3072 sc1\n\t"
      "v_mfma_f32_16x16x32_bf16 a[96:99], v[176:179], v[180:183], a[96:99]\n\t"
      "v_mfma_f32_16x16x32_bf16 a[116:119], v[176:179], v[184:187], a[116:119]\n\t"
      "buffer_load_dwordx4 v[140:143], v1, s[36:39], s48 offen sc1\n\t"
      "v_mfma_f32_16x16x32_bf16 a[136:139], v[176:179], v[188:191], a[136:139]\n\t"
      "v_mfma_f32_16x16x32_bf16 a[156:159], v[176:179], v[192:195], a[156:159]\n\t"
      "s_add_u32 s50, s51, 0x78\n\t"
      "s_lshl_b32 s49, s50, 12\n\t"
      "s_add_u32 s49, s49, s53\n\t"
      "s_add_u32 s50, s50, 54\n\t"
      "s_mul_i32 s48, s50, 0x1400\n\t"
      "s_add_u32 s48, s48, s54\n\t"
      "s_waitcnt vmcnt(27)\n\t"
      "v_mfma_f32_16x16x32_bf16 a[80:83], v[16:19], v[36:39], a[80:83]\n\t"
      "v_mfma_f32_16x16x32_bf16 a[100:103], v[16:19], v[40:43], a[100:103]\n\t"
      "buffer_load_dwordx4 v[180:183], v0, s[36:39], s49 offen sc1\n\t"
      "v_mfma_f32_16x16x32_bf16 a[120:123], v[16:19], v[44:47], a[120:123]\n\t"
      "v_mfma_f32_16x16x32_bf16 a[140:143], v[16:19], v[48:51], a[140:143]\n\t"
      "buffer_load_dwordx4 v[184:187], v0, s[36:39], s49 offen offset:1024 sc1\n\t"
      "v_mfma_f32_16x16x32_bf16 a[84:87], v[20:23], v[36:39], a[84:87]\n\t"
      "v_mfma_f32_16x16x32_bf16 a[104:107], v[20:23], v[40:43], a[104:107]\n\t"
      "buffer_load_dwordx4 v[188:191], v0, s[36:39], s49 offen offset:2048 sc1\n\t"
      "v_mfma_f32_16x16x32_bf16 a[124:127], v[20:23], v[44:47], a[124:127]\n\t"
      "v_mfma_f32_16x16x32_bf16 a[144:147], v[20:23], v[48:51], a[144:147]\n\t"
      "buffer_load_dwordx4 v[192:195], v0, s[36:39], s49 offen offset:3072 sc1\n\t"
      "v_mfma_f32_16x16x32_bf16 a[88:91], v[24:27], v[36:39], a[88:91]\n\t"
      "v_mfma_f32_16x16x32_bf16 a[108:111], v[24:27], v[40:43], a[108:111]\n\t"
      "buffer_load_dwordx4 v[160:163], v0, s[36:39], s48 offen sc1\n\t"
      "v_mfma_f32_16x16x32_bf16 a[128:131], v[24:27], v[44:47], a[128:131]\n\t"
      "v_mfma_f32_16x16x32_bf16 a[148:151], v[24:27], v[48:51], a[148:151]\n\t"
      "buffer_load_dwordx4 v[164:167], v0, s[36:39], s48 offen offset:1024 sc1\n\t"
      "v_mfma_f32_16x16x32_bf16 a[92:95], v[28:31], v[36:39], a[92:95]\n\t"
      "v_mfma_f32_16x16x32_bf16 a[112:115], v[28:31], v[40:43], a[112:115]\n\t"
      "buffer_load_dwordx4 v[168:171], v0, s[36:39], s48 offen offset:2048 sc1\n\t"
      "v_mfma_f32_16x16x32_bf16 a[132:135], v[28:31], v[44:47], a[132:135]\n\t"
      "v_mfma_f32_16x16x32_bf16 a[152:155], v[28:31], v[48:51], a[152:155]\n\t"
      "buffer_load_dwordx4 v[172:175], v0, s[36:39], s48 offen offset:3072 sc1\n\t"
      "v_mfma_f32_16x16x32_bf16 a[96:99], v[32:35], v[36:39], a[96:99]\n\t"
      "v_mfma_f32_16x16x32_bf16 a[116:119], v[32:35], v[40:43], a[116:119]\n\t"
      "buffer_load_dwordx4 v[176:179], v1, s[36:39], s48 offen sc1\n\t"
      "v_mfma_f32_16x16x32_bf16 a[136:139], v[32:35], v[44:47], a[136:139]\n\t"
      "v_mfma_f32_16x16x32_bf16 a[156:159], v[32:35], v[48:51], a[156:159]\n\t"
      "s_add_u32 s50, s51, 0x7c\n\t"
      "s_lshl_b32 s49, s50, 12\n\t"
      "s_add_u32 s49, s49, s53\n\t"
      "s_add_u32 s50, s50, 54\n\t"
      "s_mul_i32 s48, s50, 0x1400\n\t"
      "s_add_u32 s48, s48, s54\n\t"
      "s_waitcnt vmcnt(27)\n\t"
      "v_mfma_f32_16x16x32_bf16 a[80:83], v[52:55], v[72:75], a[80:83]\n\t"
      "v_mfma_f32_16x16x32_bf16 a[100:103], v[52:55], v[76:79], a[100:103]\n\t"
      "buffer_load_dwordx4 v[36:39], v0, s[36:39], s49 offen sc1\n\t"
      "v_mfma_f32_16x16x32_bf16 a[120:123], v[52:55], v[80:83], a[120:123]\n\t"
      "v_mfma_f32_16x16x32_bf16 a[140:143], v[52:55], v[84:87], a[140:143]\n\t"
      "buffer_load_dwordx4 v[40:43], v0, s[36:39], s49 offen offset:1024 sc1\n\t"
      "v_mfma_f32_16x16x32_bf16 a[84:87], v[56:59], v[72:75], a[84:87]\n\t"
      "v_mfma_f32_16x16x32_bf16 a[104:107], v[56:59], v[76:79], a[104:107]\n\t"
      "buffer_load_dwordx4 v[44:47], v0, s[36:39], s49 offen offset:2048 sc1\n\t"
      "v_mfma_f32_16x16x32_bf16 a[124:127], v[56:59], v[80:83], a[124:127]\n\t"
      "v_mfma_f32_16x16x32_bf16 a[144:147], v[56:59], v[84:87], a[144:147]\n\t"
      "buffer_load_dwordx4 v[48:51], v0, s[36:39], s49 offen offset:3072 sc1\n\t"
      "v_mfma_f32_16x16x32_bf16 a[88:91], v[60:63], v[72:75], a[88:91]\n\t"
      "v_mfma_f32_16x16x32_bf16 a[108:111], v[60:63], v[76:79], a[108:111]\n\t"
      "buffer_load_dwordx4 v[16:19], v0, s[36:39], s48 offen sc1\n\t"
      "v_mfma_f32_16x16x32_bf16 a[128:131], v[60:63], v[80:83], a[128:131]\n\t"
      "v_mfma_f32_16x16x32_bf16 a[148:151], v[60:63], v[84:87], a[148:151]\n\t"
      "buffer_load_dwordx4 v[20:23], v0, s[36:39], s48 offen offset:1024 sc1\n\t"
      "v_mfma_f32_16x16x32_bf16 a[92:95], v[64:67], v[72:75], a[92:95]\n\t"
      "v_mfma_f32_16x16x32_bf16 a[112:115], v[64:67], v[76:79], a[112:115]\n\t"
      "buffer_load_dwordx4 v[24:27], v0, s[36:39], s48 offen offset:2048 sc1\n\t"
      "v_mfma_f32_16x16x32_bf16 a[132:135], v[64:67], v[80:83], a[132:135]\n\t"
      "v_mfma_f32_16x16x32_bf16 a[152:155], v[64:67], v[84:87], a[152:155]\n\t"
      "buffer_load_dwordx4 v[28:31], v0, s[36:39], s48 offen offset:3072 sc1\n\t"
      "v_mfma_f32_16x16x32_bf16 a[96:99], v[68:71], v[72:75], a[96:99]\n\t"
      "v_mfma_f32_16x16x32_bf16 a[116:119], v[68:71], v[76:79], a[116:119]\n\t"
      "buffer_load_dwordx4 v[32:35], v1, s[36:39], s48 offen sc1\n\t"
      "v_mfma_f32_16x16x32_bf16 a[136:139], v[68:71], v[80:83], a[136:139]\n\t"
      "v_mfma_f32_16x16x32_bf16 a[156:159], v[68:71], v[84:87], a[156:159]\n\t"
      "s_waitcnt vmcnt(27)\n\t"
      "v_mfma_f32_16x16x32_bf16 a[80:83], v[88:91], v[108:111], a[80:83]\n\t"
      "v_mfma_f32_16x16x32_bf16 a[100:103], v[88:91], v[112:115], a[100:103]\n\t"
      "v_mfma_f32_16x16x32_bf16 a[120:123], v[88:91], v[116:119], a[120:123]\n\t"
      "v_mfma_f32_16x16x32_bf16 a[140:143], v[88:91], v[120:123], a[140:143]\n\t"
      "v_mfma_f32_16x16x32_bf16 a[84:87], v[92:95], v[108:111], a[84:87]\n\t"
      "v_mfma_f32_16x16x32_bf16 a[104:107], v[92:95], v[112:115], a[104:107]\n\t"
      "v_mfma_f32_16x16x32_bf16 a[124:127], v[92:95], v[116:119], a[124:127]\n\t"
      "v_mfma_f32_16x16x32_bf16 a[144:147], v[92:95], v[120:123], a[144:147]\n\t"
      "v_mfma_f32_16x16x32_bf16 a[88:91], v[96:99], v[108:111], a[88:91]\n\t"
      "v_mfma_f32_16x16x32_bf16 a[108:111], v[96:99], v[112:115], a[108:111]\n\t"
      "v_mfma_f32_16x16x32_bf16 a[128:131], v[96:99], v[116:119], a[128:131]\n\t"
      "v_mfma_f32_16x16x32_bf16 a[148:151], v[96:99], v[120:123], a[148:151]\n\t"
      "v_mfma_f32_16x16x32_bf16 a[92:95], v[100:103], v[108:111], a[92:95]\n\t"
      "v_mfma_f32_16x16x32_bf16 a[112:115], v[100:103], v[112:115], a[112:115]\n\t"
      "v_mfma_f32_16x16x32_bf16 a[132:135], v[100:103], v[116:119], a[132:135]\n\t"
      "v_mfma_f32_16x16x32_bf16 a[152:155], v[100:103], v[120:123], a[152:155]\n\t"
      "v_mfma_f32_16x16x32_bf16 a[96:99], v[104:107], v[108:111], a[96:99]\n\t"
      "v_mfma_f32_16x16x32_bf16 a[116:119], v[104:107], v[112:115], a[116:119]\n\t"
      "v_mfma_f32_16x16x32_bf16 a[136:139], v[104:107], v[116:119], a[136:139]\n\t"
      "v_mfma_f32_16x16x32_bf16 a[156:159], v[104:107], v[120:123], a[156:159]\n\t"
      "s_waitcnt vmcnt(18)\n\t"
      "v_mfma_f32_16x16x32_bf16 a[80:83], v[124:127], v[144:147], a[80:83]\n\t"
      "v_mfma_f32_16x16x32_bf16 a[100:103], v[124:127], v[148:151], a[100:103]\n\t"
      "v_mfma_f32_16x16x32_bf16 a[120:123], v[124:127], v[152:155], a[120:123]\n\t"
      "v_mfma_f32_16x16x32_bf16 a[140:143], v[124:127], v[156:159], a[140:143]\n\t"
      "v_mfma_f32_16x16x32_bf16 a[84:87], v[128:131], v[144:147], a[84:87]\n\t"
      "v_mfma_f32_16x16x32_bf16 a[104:107], v[128:131], v[148:151], a[104:107]\n\t"
      "v_mfma_f32_16x16x32_bf16 a[124:127], v[128:131], v[152:155], a[124:127]\n\t"
      "v_mfma_f32_16x16x32_bf16 a[144:147], v[128:131], v[156:159], a[144:147]\n\t"
      "v_mfma_f32_16x16x32_bf16 a[88:91], v[132:135], v[144:147], a[88:91]\n\t"
      "v_mfma_f32_16x16x32_bf16 a[108:111], v[132:135], v[148:151], a[108:111]\n\t"
      "v_mfma_f32_16x16x32_bf16 a[128:131], v[132:135], v[152:155], a[128:131]\n\t"
      "v_mfma_f32_16x16x32_bf16 a[148:151], v[132:135], v[156:159], a[148:151]\n\t"
      "v_mfma_f32_16x16x32_bf16 a[92:95], v[136:139], v[144:147], a[92:95]\n\t"
      "v_mfma_f32_16x16x32_bf16 a[112:115], v[136:139], v[148:151], a[112:115]\n\t"
      "v_mfma_f32_16x16x32_bf16 a[132:135], v[136:139], v[152:155], a[132:135]\n\t"
      "v_mfma_f32_16x16x32_bf16 a[152:155], v[136:139], v[156:159], a[152:155]\n\t"
      "v_mfma_f32_16x16x32_bf16 a[96:99], v[140:143], v[144:147], a[96:99]\n\t"
      "v_mfma_f32_16x16x32_bf16 a[116:119], v[140:143], v[148:151], a[116:119]\n\t"
      "v_mfma_f32_16x16x32_bf16 a[136:139], v[140:143], v[152:155], a[136:139]\n\t"
      "v_mfma_f32_16x16x32_bf16 a[156:159], v[140:143], v[156:159], a[156:159]\n\t"
      "s_waitcnt vmcnt(9)\n\t"
      "v_mfma_f32_16x16x32_bf16 a[80:83], v[160:163], v[180:183], a[80:83]\n\t"
      "v_mfma_f32_16x16x32_bf16 a[100:103], v[160:163], v[184:187], a[100:103]\n\t"
      "v_mfma_f32_16x16x32_bf16 a[120:123], v[160:163], v[188:191], a[120:123]\n\t"
      "v_mfma_f32_16x16x32_bf16 a[140:143], v[160:163], v[192:195], a[140:143]\n\t"
      "v_mfma_f32_16x16x32_bf16 a[84:87], v[164:167], v[180:183], a[84:87]\n\t"
      "v_mfma_f32_16x16x32_bf16 a[104:107], v[164:167], v[184:187], a[104:107]\n\t"
      "v_mfma_f32_16x16x32_bf16 a[124:127], v[164:167], v[188:191], a[124:127]\n\t"
      "v_mfma_f32_16x16x32_bf16 a[144:147], v[164:167], v[192:195], a[144:147]\n\t"
      "v_mfma_f32_16x16x32_bf16 a[88:91], v[168:171], v[180:183], a[88:91]\n\t"
      "v_mfma_f32_16x16x32_bf16 a[108:111], v[168:171], v[184:187], a[108:111]\n\t"
      "v_mfma_f32_16x16x32_bf16 a[128:131], v[168:171], v[188:191], a[128:131]\n\t"
      "v_mfma_f32_16x16x32_bf16 a[148:151], v[168:171], v[192:195], a[148:151]\n\t"
      "v_mfma_f32_16x16x32_bf16 a[92:95], v[172:175], v[180:183], a[92:95]\n\t"
      "v_mfma_f32_16x16x32_bf16 a[112:115], v[172:175], v[184:187], a[112:115]\n\t"
      "v_mfma_f32_16x16x32_bf16 a[132:135], v[172:175], v[188:191], a[132:135]\n\t"
      "v_mfma_f32_16x16x32_bf16 a[152:155], v[172:175], v[192:195], a[152:155]\n\t"
      "v_mfma_f32_16x16x32_bf16 a[96:99], v[176:179], v[180:183], a[96:99]\n\t"
      "v_mfma_f32_16x16x32_bf16 a[116:119], v[176:179], v[184:187], a[116:119]\n\t"
      "v_mfma_f32_16x16x32_bf16 a[136:139], v[176:179], v[188:191], a[136:139]\n\t"
      "v_mfma_f32_16x16x32_bf16 a[156:159], v[176:179], v[192:195], a[156:159]\n\t"
      "s_waitcnt vmcnt(0)\n\t"
      "v_mfma_f32_16x16x32_bf16 a[80:83], v[16:19], v[36:39], a[80:83]\n\t"
      "v_mfma_f32_16x16x32_bf16 a[100:103], v[16:19], v[40:43], a[100:103]\n\t"
      "v_mfma_f32_16x16x32_bf16 a[120:123], v[16:19], v[44:47], a[120:123]\n\t"
      "v_mfma_f32_16x16x32_bf16 a[140:143], v[16:19], v[48:51], a[140:143]\n\t"
      "v_mfma_f32_16x16x32_bf16 a[84:87], v[20:23], v[36:39], a[84:87]\n\t"
      "v_mfma_f32_16x16x32_bf16 a[104:107], v[20:23], v[40:43], a[104:107]\n\t"
      "v_mfma_f32_16x16x32_bf16 a[124:127], v[20:23], v[44:47], a[124:127]\n\t"
      "v_mfma_f32_16x16x32_bf16 a[144:147], v[20:23], v[48:51], a[144:147]\n\t"
      "v_mfma_f32_16x16x32_bf16 a[88:91], v[24:27], v[36:39], a[88:91]\n\t"
      "v_mfma_f32_16x16x32_bf16 a[108:111], v[24:27], v[40:43], a[108:111]\n\t"
      "v_mfma_f32_16x16x32_bf16 a[128:131], v[24:27], v[44:47], a[128:131]\n\t"
      "v_mfma_f32_16x16x32_bf16 a[148:151], v[24:27], v[48:51], a[148:151]\n\t"
      "v_mfma_f32_16x16x32_bf16 a[92:95], v[28:31], v[36:39], a[92:95]\n\t"
      "v_mfma_f32_16x16x32_bf16 a[112:115], v[28:31], v[40:43], a[112:115]\n\t"
      "v_mfma_f32_16x16x32_bf16 a[132:135], v[28:31], v[44:47], a[132:135]\n\t"
      "v_mfma_f32_16x16x32_bf16 a[152:155], v[28:31], v[48:51], a[152:155]\n\t"
      "v_mfma_f32_16x16x32_bf16 a[96:99], v[32:35], v[36:39], a[96:99]\n\t"
      "v_mfma_f32_16x16x32_bf16 a[116:119], v[32:35], v[40:43], a[116:119]\n\t"
      "v_mfma_f32_16x16x32_bf16 a[136:139], v[32:35], v[44:47], a[136:139]\n\t"
      "v_mfma_f32_16x16x32_bf16 a[156:159], v[32:35], v[48:51], a[156:159]\n\t"
      "s_nop 7\n\t"
      "s_nop 7\n\t"
      "v_add_u32 v10, 0x14000, v8\n\t"
      "v_add_u32 v11, 0x14000, v9\n\t"
      "v_add_u32 v12, 0x14000, v14\n\t"
      "ds_write_b128 v10, a[80:83]\n\t"
      "ds_write_b128 v10, a[84:87] offset:1024\n\t"
      "ds_write_b128 v10, a[88:91] offset:2048\n\t"
      "ds_write_b128 v10, a[92:95] offset:3072\n\t"
      "ds_write_b128 v10, a[96:99] offset:4096\n\t"
      "ds_write_b128 v10, a[100:103] offset:20480\n\t"
      "ds_write_b128 v10, a[104:107] offset:21504\n\t"
      "ds_write_b128 v10, a[108:111] offset:22528\n\t"
      "ds_write_b128 v10, a[112:115] offset:23552\n\t"
      "ds_write_b128 v10, a[116:119] offset:24576\n\t"
      "ds_write_b128 v12, a[120:123]\n\t"
      "ds_write_b128 v12, a[124:127] offset:1024\n\t"
      "ds_write_b128 v12, a[128:131] offset:2048\n\t"
      "ds_write_b128 v12, a[132:135] offset:3072\n\t"
      "ds_write_b128 v12, a[136:139] offset:4096\n\t"
      "ds_write_b128 v12, a[140:143] offset:20480\n\t"
      "ds_write_b128 v12, a[144:147] offset:21504\n\t"
      "ds_write_b128 v12, a[148:151] offset:22528\n\t"
      "ds_write_b128 v12, a[152:155] offset:23552\n\t"
      "ds_write_b128 v12, a[156:159] offset:24576\n\t"
      "s_waitcnt lgkmcnt(0)\n\t"
      "s_barrier\n\t"
      "ds_read_b128 v[16:19], v11\n\t"
      "ds_read_b128 v[20:23], v11 offset:1024\n\t"
      "ds_read_b128 v[24:27], v11 offset:2048\n\t"
      "ds_read_b128 v[28:31], v11 offset:3072\n\t"
      "ds_read_b128 v[32:35], v11 offset:4096\n\t"
      "ds_read_b128 v[36:39], v11 offset:5120\n\t"
      "ds_read_b128 v[40:43], v11 offset:6144\n\t"
      "ds_read_b128 v[44:47], v11 offset:7168\n\t"
      "ds_read_b128 v[48:51], v11 offset:8192\n\t"
      "ds_read_b128 v[52:55], v11 offset:9216\n\t"
      "ds_read_b128 v[56:59], v11 offset:10240\n\t"
      "ds_read_b128 v[60:63], v11 offset:11264\n\t"
      "ds_read_b128 v[64:67], v11 offset:12288\n\t"
      "ds_read_b128 v[68:71], v11 offset:13312\n\t"
      "ds_read_b128 v[72:75], v11 offset:14336\n\t"
      "ds_read_b128 v[76:79], v11 offset:15360\n\t"
      "ds_read_b128 v[80:83], v11 offset:16384\n\t"
      "ds_read_b128 v[84:87], v11 offset:17408\n\t"
      "ds_read_b128 v[88:91], v11 offset:18432\n\t"
      "ds_read_b128 v[92:95], v11 offset:19456\n\t"
      "s_waitcnt lgkmcnt(10)\n\t"
      "v_pk_add_f32 v[16:17], v[16:17], v[36:37]\n\t"
      "v_pk_add_f32 v[18:19], v[18:19], v[38:39]\n\t"
      "v_pk_add_f32 v[20:21], v[20:21], v[40:41]\n\t"
      "v_pk_add_f32 v[22:23], v[22:23], v[42:43]\n\t"
      "v_pk_add_f32 v[24:25], v[24:25], v[44:45]\n\t"
      "v_pk_add_f32 v[26:27], v[26:27], v[46:47]\n\t"
      "v_pk_add_f32 v[28:29], v[28:29], v[48:49]\n\t"
      "v_pk_add_f32 v[30:31], v[30:31], v[50:51]\n\t"
      "v_pk_add_f32 v[32:33], v[32:33], v[52:53]\n\t"
      "v_pk_add_f32 v[34:35], v[34:35], v[54:55]\n\t"
      "s_waitcnt lgkmcnt(0)\n\t"
      "v_pk_add_f32 v[56:57], v[56:57], v[76:77]\n\t"
      "v_pk_add_f32 v[58:59], v[58:59], v[78:79]\n\t"
      "v_pk_add_f32 v[60:61], v[60:61], v[80:81]\n\t"
      "v_pk_add_f32 v[62:63], v[62:63], v[82:83]\n\t"
      "v_pk_add_f32 v[64:65], v[64:65], v[84:85]\n\t"
      "v_pk_add_f32 v[66:67], v[66:67], v[86:87]\n\t"
      "v_pk_add_f32 v[68:69], v[68:69], v[88:89]\n\t"
      "v_pk_add_f32 v[70:71], v[70:71], v[90:91]\n\t"
      "v_pk_add_f32 v[72:73], v[72:73], v[92:93]\n\t"
      "v_pk_add_f32 v[74:75], v[74:75], v[94:95]\n\t"
      "v_pk_add_f32 v[16:17], v[16:17], v[56:57]\n\t"
      "v_pk_add_f32 v[18:19], v[18:19], v[58:59]\n\t"
      "v_pk_add_f32 v[20:21], v[20:21], v[60:61]\n\t"
      "v_pk_add_f32 v[22:23], v[22:23], v[62:63]\n\t"
      "v_pk_add_f32 v[24:25], v[24:25], v[64:65]\n\t"
      "v_pk_add_f32 v[26:27], v[26:27], v[66:67]\n\t"
      "v_pk_add_f32 v[28:29], v[28:29], v[68:69]\n\t"
      "v_pk_add_f32 v[30:31], v[30:31], v[70:71]\n\t"
      "v_pk_add_f32 v[32:33], v[32:33], v[72:73]\n\t"
      "v_pk_add_f32 v[34:35], v[34:35], v[74:75]\n\t"
      "v_pk_fma_f32 v[216:217], v[16:17], v[196:197], v[216:217]\n\t"
      "v_pk_fma_f32 v[218:219], v[18:19], v[198:199], v[218:219]\n\t"
      "v_pk_fma_f32 v[220:221], v[20:21], v[200:201], v[220:221]\n\t"
      "v_pk_fma_f32 v[222:223], v[22:23], v[202:203], v[222:223]\n\t"
      "v_pk_fma_f32 v[224:225], v[24:25], v[204:205], v[224:225]\n\t"
      "v_pk_fma_f32 v[226:227], v[26:27], v[206:207], v[226:227]\n\t"
      "v_pk_fma_f32 v[228:229], v[28:29], v[208:209], v[228:229]\n\t"
      "v_pk_fma_f32 v[230:231], v[30:31], v[210:211], v[230:231]\n\t"
      "v_pk_fma_f32 v[232:233], v[32:33], v[212:213], v[232:233]\n\t"
      "v_pk_fma_f32 v[234:235], v[34:35], v[214:215], v[234:235]\n\t"
      "v_cvt_pk_bf16_f32 v236, v216, v217\n\t"
      "v_cvt_pk_bf16_f32 v237, v218, v219\n\t"
      "v_cvt_pk_bf16_f32 v238, v220, v221\n\t"
      "v_cvt_pk_bf16_f32 v239, v222, v223\n\t"
      "v_cvt_pk_bf16_f32 v240, v224, v225\n\t"
      "v_cvt_pk_bf16_f32 v241, v226, v227\n\t"
      "v_cvt_pk_bf16_f32 v242, v228, v229\n\t"
      "v_cvt_pk_bf16_f32 v243, v230, v231\n\t"
      "v_cvt_pk_bf16_f32 v244, v232, v233\n\t"
      "v_cvt_pk_bf16_f32 v245, v234, v235\n\t"
      "buffer_store_dwordx2 v[236:237], v3, s[44:47], s55 offen\n\t"
      "buffer_store_dwordx2 v[238:239], v3, s[44:47], s55 offen offset:32\n\t"
      "buffer_store_dwordx2 v[240:241], v3, s[44:47], s55 offen offset:64\n\t"
      "buffer_store_dwordx2 v[242:243], v3, s[44:47], s55 offen offset:96\n\t"
      "buffer_store_dwordx2 v[244:245], v3, s[44:47], s55 offen offset:128\n\t"
      "K1exit:\n\t"
      : "=&s"(bad)
      : "s"(kargs()), "s"(h), "s"(c), "s"(w), "s"(tb64), "s"(skip), "s"(WS_END + 65536u + (blockIdx.x * 4 + w) * 64u), "s"(nonce), "s"(rb),
        "s"(8 * (xcd >> 1) + (idx >> 2)), "s"(4 * (xcd & 1) + (idx & 3))
      :
      "v0","v1","v2","v3","v4","v5","v6","v7","v8","v9","v10","v11","v12","v13","v14","v15","v16","v17","v18","v19","v20","v21","v22","v23",
      "v24","v25","v26","v27","v28","v29","v30","v31","v32","v33","v34","v35","v36","v37","v38","v39","v40","v41","v42","v43","v44","v45","v46","v47",
      "v48","v49","v50","v51","v52","v53","v54","v55","v56","v57","v58","v59","v60","v61","v62","v63","v64","v65","v66","v67","v68","v69","v70","v71",
      "v72","v73","v74","v75","v76","v77","v78","v79","v80","v81","v82","v83","v84","v85","v86","v87","v88","v89","v90","v91","v92","v93","v94","v95",
      "v96","v97","v98","v99","v100","v101","v102","v103","v104","v105","v106","v107","v108","v109","v110","v111","v112","v113","v114","v115","v116","v117","v118","v119",
      "v120","v121","v122","v123","v124","v125","v126","v127","v128","v129","v130","v131","v132","v133","v134","v135","v136","v137","v138","v139","v140","v141","v142","v143",
      "v144","v145","v146","v147","v148","v149","v150","v151","v152","v153","v154","v155","v156","v157","v158","v159","v160","v161","v162","v163","v164","v165","v166","v167",
      "v168","v169","v170","v171","v172","v173","v174","v175","v176","v177","v178","v179","v180","v181","v182","v183","v184","v185","v186","v187","v188","v189","v190","v191",
      "v192","v193","v194","v195","v196","v197","v198","v199","v200","v201","v202","v203","v204","v205","v206","v207","v208","v209","v210","v211","v212","v213","v214","v215",
      "v216","v217","v218","v219","v220","v221","v222","v223","v224","v225","v226","v227","v228","v229","v230","v231","v232","v233","v234","v235","v236","v237","v238","v239",
      "v240","v241","v242","v243","v244","v245","v246","v247","v248","v249","v250","v251","v252","v253","v254","v255","a0","a1","a2","a3","a4","a5","a6","a7",
      "a8","a9","a10","a11","a12","a13","a14","a15","a16","a17","a18","a19","a20","a21","a22","a23","a24","a25","a26","a27","a28","a29","a30","a31",
      "a32","a33","a34","a35","a36","a37","a38","a39","a40","a41","a42","a43","a44","a45","a46","a47","a48","a49","a50","a51","a52","a53","a54","a55",
      "a56","a57","a58","a59","a60","a61","a62","a63","a64","a65","a66","a67","a68","a69","a70","a71","a72","a73","a74","a75","a76","a77","a78","a79",
      "a80","a81","a82","a83","a84","a85","a86","a87","a88","a89","a90","a91","a92","a93","a94","a95","a96","a97","a98","a99","a100","a101","a102","a103",
      "a104","a105","a106","a107","a108","a109","a110","a111","a112","a113","a114","a115","a116","a117","a118","a119","a120","a121","a122","a123","a124","a125","a126","a127",
      "a128","a129","a130","a131","a132","a133","a134","a135","a136","a137","a138","a139","a140","a141","a142","a143","a144","a145","a146","a147","a148","a149","a150","a151",
      "a152","a153","a154","a155","a156","a157","a158","a159","a160","a161","a162","a163","a164","a165","a166","a167","a168","a169","a170","a171","a172","a173","a174","a175",
      "a176","a177","a178","a179","a180","a181","a182","a183","a184","a185","a186","a187","a188","a189","a190","a191","a192","a193","a194","a195","a196","a197","a198","a199",
      "a200","a201","a202","a203","a204","a205","a206","a207","a208","a209","a210","a211","a212","a213","a214","a215","a216","a217","a218","a219","a220","a221","a222","a223",
      "a224","a225","a226","a227","a228","a229","a230","a231","a232","a233","a234","a235","a236","a237","a238","a239","a240","a241","a242","a243","a244","a245","a246","a247",
      "a248","a249","a250","a251","a252","a253","a254","a255","s36","s37","s38","s39","s40","s41","s42","s43","s44","s45","s46","s47","s48","s49","s50","s51",
      "s52","s53","s54","s55","s56","s57","s58","s59","s60","s61","s62","s63","s64","s65","s66","s67","s68","s69","s70","s71","s72","s73","s74","s75",
      "s76","s77","s78","s79","s80","s81","s82","s83","s84","s85","s86","s87","s88","s89","s90","s91","s92","s93","s94","s95","s96","s97","s98","s99",
      "vcc","scc","memory");
  if (bad & 2) {
  int b_ = b, w_ = w;
  asm volatile("" : "+s"(b_), "+s"(w_));
  const int b = b_, w = w_, xcd = b & 7, idx = b >> 3, rb = idx & 7, h = 4 * xcd + (idx >> 3);
  const unsigned tb64 = 4 * rb + w;
  {
    KArgs a = kargs();
    char* ws = a->ws;
    const int l = __builtin_amdgcn_mbcnt_hi(~0u, __builtin_amdgcn_mbcnt_lo(~0u, 0u));
    if (bad & 1) {
      const u16* kc = a->kc; const u16* vc = a->vc; const u16* wq = a->wq;
      float* sq = (float*)(lds) + w * 1024;
      float* sp = sq + 128;
      for (int t = 0; t < 64; ++t) {
        gu32* xb = (gu32*)(ws + WS_XN + (size_t)(tb64 * 20) * 4096 + (t >> 4) * 1024 + (t & 15) * 16);
        float q0 = 0.f, q1 = 0.f;
        for (int k = 0; k < 640; k += 2) {
          const unsigned xv = __hip_atomic_load(xb + ((k >> 5) * 4096 + ((k >> 3) & 3) * 256 + (k & 7) * 2) / 4, __ATOMIC_RELAXED, __HIP_MEMORY_SCOPE_AGENT);
          const unsigned w0 = *(const unsigned*)(wq + (size_t)k * 4096 + h * 128 + 2 * l), w1 = *(const unsigned*)(wq + (size_t)(k + 1) * 4096 + h * 128 + 2 * l);
          const float x0 = bf2f(xv & 0xffffu), x1 = __uint_as_float(xv & 0xffff0000u);
          q0 = fmaf(x0, bf2f(w0 & 0xffffu), q0); q1 = fmaf(x0, __uint_as_float(w0 & 0xffff0000u), q1);
          q0 = fmaf(x1, bf2f(w1 & 0xffffu), q0); q1 = fmaf(x1, __uint_as_float(w1 & 0xffff0000u), q1);
        }
        const unsigned qp = pk2(q0, q1);
        sq[2 * l] = bf2f(qp & 0xffffu); sq[2 * l + 1] = __uint_as_float(qp & 0xffff0000u);
        __builtin_amdgcn_wave_barrier(); __builtin_amdgcn_s_waitcnt(0);
        float mx = -INFINITY;
        for (int i = 0; i < 8; ++i) {
          const int key = i * 64 + l;
          const u16* kr = kc + ((size_t)h * 2048 + 1536 + key) * 128;
          float s = 0.f;
          for (int d = 0; d < 128; ++d) s = fmaf(sq[d], bf2f(kr[d]), s);
          sp[key] = s; mx = fmaxf(mx, s);
        }
        for (int m = 1; m < 64; m <<= 1) mx = fmaxf(mx, __shfl_xor(mx, m));
        float sum = 0.f;
        for (int i = 0; i < 8; ++i) {
          const float e = exp2f(sp[i * 64 + l] - mx);
          const float eb = bf2f(pk2(e, e) & 0xffffu);
          sp[i * 64 + l] = eb; sum += eb;
        }
        for (int m = 1; m < 64; m <<= 1) sum += __shfl_xor(sum, m);
        __builtin_amdgcn_wave_barrier(); __builtin_amdgcn_s_waitcnt(0);
        float o0 = 0.f, o1 = 0.f;
        for (int key = 0; key < 512; ++key) {
          const unsigned vv = *(const unsigned*)(vc + ((size_t)h * 2048 + 1536 + key) * 128 + 2 * l);
          const float pp = sp[key];
          o0 = fmaf(pp, bf2f(vv & 0xffffu), o0); o1 = fmaf(pp, __uint_as_float(vv & 0xffff0000u), o1);
        }
        const float inv = 1.f / sum;
        const int d = 2 * l, u = d >> 5, dd = d & 31, g = (dd >> 2) & 3, j = (dd & 3) + ((dd >> 4) << 2);
        __hip_atomic_store((gu32*)(ws + WS_O + ((size_t)(tb64 * 128 + h * 4 + u) * 4 + (t >> 4)) * 1024 + (g * 16 + (t & 15)) * 16 + j * 2), pk2(o0 * inv, o1 * inv),
                           __ATOMIC_RELAXED, __HIP_MEMORY_SCOPE_AGENT);
        __builtin_amdgcn_wave_barrier(); __builtin_amdgcn_s_waitcnt(0);
      }
    }
    __syncthreads();
  }
  {
    const unsigned t2 = 8 * (xcd >> 1) + (idx >> 2), cb = 4 * (xcd & 1) + (idx & 3);
    asm volatile(
      "s_load_dwordx2 s[36:37], %0, 64\n\t"
      "s_mov_b32 s38, 0x209c000\n\t"
      "s_mov_b32 s39, 0x20000\n\t"
      "s_load_dwordx2 s[40:41], %0, 0\n\t"
      "s_mov_b32 s42, 0x280000\n\t"
      "s_mov_b32 s43, 0x20000\n\t"
      "s_load_dwordx2 s[44:45], %0, 72\n\t"
      "s_mov_b32 s46, 0x280000\n\t"
      "s_mov_b32 s47, 0x20000\n\t"
      "s_mov_b32 s54, %1\n\t"
      "s_mov_b32 s52, %2\n\t"
      "s_mov_b32 s53, %3\n\t"
      "s_mov_b32 s51, %4\n\t"
      "s_mov_b32 s55, %5\n\t"
      "s_mov_b32 s56, %7\n\t"
      "s_mov_b32 s57, %10\n\t"
      "s_mov_b32 s62, %9\n\t"
      "s_waitcnt lgkmcnt(0)\n\t"
      "v_mbcnt_lo_u32_b32 v0, -1, 0\n\t"
      "v_mbcnt_hi_u32_b32 v0, -1, v0\n\t"
      "v_lshlrev_b32 v0, 4, v0\n\t"
      "v_add_u32 v1, 0x1000, v0\n\t"
      "v_lshrrev_b32 v11, 2, v0\n\t"
      "v_and_b32 v11, 0x7c, v11\n\t"
      "s_lshl_b32 s59, %8, 7\n\t"
      "s_add_u32 s59, s59, 0x205e000\n\t"
      "v_lshrrev_b32 v4, 4, v0\n\t"
      "v_and_b32 v5, 15, v4\n\t"
      "v_lshrrev_b32 v6, 4, v4\n\t"
      "v_mul_u32_u24 v5, 0x500, v5\n\t"
      "v_lshlrev_b32 v6, 3, v6\n\t"
      "v_add_u32 v3, v5, v6\n\t"
      "s_mul_i32 s50, s51, 0x5000\n\t"
      "s_add_u32 s55, s55, s50\n\t"
      "v_lshrrev_b32 v6, 1, v0\n\t"
      "s_mul_i32 s50, s51, 0x1400\n\t"
      "v_add_u32 v8, s50, v0\n\t"
      "v_add_u32 v14, 0xa000, v8\n\t"
      "s_mul_i32 s50, s51, 0x5000\n\t"
      "v_add_u32 v9, s50, v0\n\t"
      "s_and_b32 s50, s51, 1\n\t"
      "s_mul_i32 s48, s50, 0x1400\n\t"
      "s_add_u32 s48, s48, s54\n\t"
      "s_lshl_b32 s49, s50, 12\n\t"
      "s_add_u32 s49, s49, s52\n\t"
      "buffer_load_dwordx4 v[36:39], v0, s[36:39], s49 offen sc1\n\t"
      "buffer_load_dwordx4 v[40:43], v0, s[36:39], s49 offen offset:1024 sc1\n\t"
      "buffer_load_dwordx4 v[44:47], v0, s[36:39], s49 offen offset:2048 sc1\n\t"
      "buffer_load_dwordx4 v[48:51], v0, s[36:39], s49 offen offset:3072 sc1\n\t"
      "buffer_load_dwordx4 v[16:19], v0, s[36:39], s48 offen sc1\n\t"
      "buffer_load_dwordx4 v[20:23], v0, s[36:39], s48 offen offset:1024 sc1\n\t"
      "buffer_load_dwordx4 v[24:27], v0, s[36:39], s48 offen offset:2048 sc1\n\t"
      "buffer_load_dwordx4 v[28:31], v0, s[36:39], s48 offen offset:3072 sc1\n\t"
      "buffer_load_dwordx4 v[32:35], v1, s[36:39], s48 offen sc1\n\t"
      "buffer_load_dwordx2 v[236:237], v3, s[40:43], s55 offen\n\t"
      "buffer_load_dwordx2 v[238:239], v3, s[40:43], s55 offen offset:32\n\t"
      "buffer_load_dwordx2 v[240:241], v3, s[40:43], s55 offen offset:64\n\t"
      "buffer_load_dwordx2 v[242:243], v3, s[40:43], s55 offen offset:96\n\t"
      "buffer_load_dwordx2 v[244:245], v3, s[40:43], s55 offen offset:128\n\t"
      "s_add_u32 s50, s51, 2\n\t"
      "s_mul_i32 s48, s50, 0x1400\n\t"
      "s_add_u32 s48, s48, s54\n\t"
      "s_lshl_b32 s49, s50, 12\n\t"
      "s_add_u32 s49, s49, s52\n\t"
      "buffer_load_dwordx4 v[72:75], v0, s[36:39], s49 offen sc1\n\t"
      "buffer_load_dwordx4 v[76:79], v0, s[36:39], s49 offen offset:1024 sc1\n\t"
      "buffer_load_dwordx4 v[80:83], v0, s[36:39], s49 offen offset:2048 sc1\n\t"
      "buffer_load_dwordx4 v[84:87], v0, s[36:39], s49 offen offset:3072 sc1\n\t"
      "buffer_load_dwordx4 v[52:55], v0, s[36:39], s48 offen sc1\n\t"
      "buffer_load_dwordx4 v[56:59], v0, s[36:39], s48 offen offset:1024 sc1\n\t"
      "buffer_load_dwordx4 v[60:63], v0, s[36:39], s48 offen offset:2048 sc1\n\t"
      "buffer_load_dwordx4 v[64:67], v0, s[36:39], s48 offen offset:3072 sc1\n\t"
      "buffer_load_dwordx4 v[68:71], v1, s[36:39], s48 offen sc1\n\t"
      "s_add_u32 s50, s51, 6\n\t"
      "s_mul_i32 s48, s50, 0x1400\n\t"
      "s_add_u32 s48, s48, s54\n\t"
      "s_lshl_b32 s49, s50, 12\n\t"
      "s_add_u32 s49, s49, s52\n\t"
      "buffer_load_dwordx4 v[108:111], v0, s[36:39], s49 offen sc1\n\t"
      "buffer_load_dwordx4 v[112:115], v0, s[36:39], s49 offen offset:1024 sc1\n\t"
      "buffer_load_dwordx4 v[116:119], v0, s[36:39], s49 offen offset:2048 sc1\n\t"
      "buffer_load_dwordx4 v[120:123], v0, s[36:39], s49 offen offset:3072 sc1\n\t"
      "buffer_load_dwordx4 v[88:91], v0, s[36:39], s48 offen sc1\n\t"
      "buffer_load_dwordx4 v[92:95], v0, s[36:39], s48 offen offset:1024 sc1\n\t"
      "buffer_load_dwordx4 v[96:99], v0, s[36:39], s48 offen offset:2048 sc1\n\t"
      "buffer_load_dwordx4 v[100:103], v0, s[36:39], s48 offen offset:3072 sc1\n\t"
      "buffer_load_dwordx4 v[104:107], v1, s[36:39], s48 offen sc1\n\t"
      "s_add_u32 s50, s51, 10\n\t"
      "s_mul_i32 s48, s50, 0x1400\n\t"
      "s_add_u32 s48, s48, s54\n\t"
      "s_lshl_b32 s49, s50, 12\n\t"
      "s_add_u32 s49, s49, s52\n\t"
      "buffer_load_dwordx4 v[144:147], v0, s[36:39], s49 offen sc1\n\t"
      "buffer_load_dwordx4 v[148:151], v0, s[36:39], s49 offen offset:1024 sc1\n\t"
      "buffer_load_dwordx4 v[152:155], v0, s[36:39], s49 offen offset:2048 sc1\n\t"
      "buffer_load_dwordx4 v[156:159], v0, s[36:39], s49 offen offset:3072 sc1\n\t"
      "buffer_load_dwordx4 v[124:127], v0, s[36:39], s48 offen sc1\n\t"
      "buffer_load_dwordx4 v[128:131], v0, s[36:39], s48 offen offset:1024 sc1\n\t"
      "buffer_load_dwordx4 v[132:135], v0, s[36:39], s48 offen offset:2048 sc1\n\t"
      "buffer_load_dwordx4 v[136:139], v0, s[36:39], s48 offen offset:3072 sc1\n\t"
      "buffer_load_dwordx4 v[140:143], v1, s[36:39], s48 offen sc1\n\t"
      "v_accvgpr_write_b32 a0, 0\n\t"
      "v_accvgpr_write_b32 a1, 0\n\t"
      "v_accvgpr_write_b32 a2, 0\n\t"
      "v_accvgpr_write_b32 a3, 0\n\t"
      "v_accvgpr_write_b32 a4, 0\n\t"
      "v_accvgpr_write_b32 a5, 0\n\t"
      "v_accvgpr_write_b32 a6, 0\n\t"
      "v_accvgpr_write_b32 a7, 0\n\t"
      "v_accvgpr_write_b32 a8, 0\n\t"
      "v_accvgpr_write_b32 a9, 0\n\t"
      "v_accvgpr_write_b32 a10, 0\n\t"
      "v_accvgpr_write_b32 a11, 0\n\t"
      "v_accvgpr_write_b32 a12, 0\n\t"
      "v_accvgpr_write_b32 a13, 0\n\t"
      "v_accvgpr_write_b32 a14, 0\n\t"
      "v_accvgpr_write_b32 a15, 0\n\t"
      "v_accvgpr_write_b32 a16, 0\n\t"
      "v_accvgpr_write_b32 a17, 0\n\t"
      "v_accvgpr_write_b32 a18, 0\n\t"
      "v_accvgpr_write_b32 a19, 0\n\t"
      "v_accvgpr_write_b32 a20, 0\n\t"
      "v_accvgpr_write_b32 a21, 0\n\t"
      "v_accvgpr_write_b32 a22, 0\n\t"
      "v_accvgpr_write_b32 a23, 0\n\t"
      "v_accvgpr_write_b32 a24, 0\n\t"
      "v_accvgpr_write_b32 a25, 0\n\t"
      "v_accvgpr_write_b32 a26, 0\n\t"
      "v_accvgpr_write_b32 a27, 0\n\t"
      "v_accvgpr_write_b32 a28, 0\n\t"
      "v_accvgpr_write_b32 a29, 0\n\t"
      "v_accvgpr_write_b32 a30, 0\n\t"
      "v_accvgpr_write_b32 a31, 0\n\t"
      "v_accvgpr_write_b32 a32, 0\n\t"
      "v_accvgpr_write_b32 a33, 0\n\t"
      "v_accvgpr_write_b32 a34, 0\n\t"
      "v_accvgpr_write_b32 a35, 0\n\t"
      "v_accvgpr_write_b32 a36, 0\n\t"
      "v_accvgpr_write_b32 a37, 0\n\t"
      "v_accvgpr_write_b32 a38, 0\n\t"
      "v_accvgpr_write_b32 a39, 0\n\t"
      "v_accvgpr_write_b32 a40, 0\n\t"
      "v_accvgpr_write_b32 a41, 0\n\t"
      "v_accvgpr_write_b32 a42, 0\n\t"
      "v_accvgpr_write_b32 a43, 0\n\t"
      "v_accvgpr_write_b32 a44, 0\n\t"
      "v_accvgpr_write_b32 a45, 0\n\t"
      "v_accvgpr_write_b32 a46, 0\n\t"
      "v_accvgpr_write_b32 a47, 0\n\t"
      "v_accvgpr_write_b32 a48, 0\n\t"
      "v_accvgpr_write_b32 a49, 0\n\t"
      "v_accvgpr_write_b32 a50, 0\n\t"
      "v_accvgpr_write_b32 a51, 0\n\t"
      "v_accvgpr_write_b32 a52, 0\n\t"
      "v_accvgpr_write_b32 a53, 0\n\t"
      "v_accvgpr_write_b32 a54, 0\n\t"
      "v_accvgpr_write_b32 a55, 0\n\t"
      "v_accvgpr_write_b32 a56, 0\n\t"
      "v_accvgpr_write_b32 a57, 0\n\t"
      "v_accvgpr_write_b32 a58, 0\n\t"
      "v_accvgpr_write_b32 a59, 0\n\t"
      "v_accvgpr_write_b32 a60, 0\n\t"
      "v_accvgpr_write_b32 a61, 0\n\t"
      "v_accvgpr_write_b32 a62, 0\n\t"
      "v_accvgpr_write_b32 a63, 0\n\t"
      "v_accvgpr_write_b32 a64, 0\n\t"
      "v_accvgpr_write_b32 a65, 0\n\t"
      "v_accvgpr_write_b32 a66, 0\n\t"
      "v_accvgpr_write_b32 a67, 0\n\t"
      "v_accvgpr_write_b32 a68, 0\n\t"
      "v_accvgpr_write_b32 a69, 0\n\t"
      "v_accvgpr_write_b32 a70, 0\n\t"
      "v_accvgpr_write_b32 a71, 0\n\t"
      "v_accvgpr_write_b32 a72, 0\n\t"
      "v_accvgpr_write_b32 a73, 0\n\t"
      "v_accvgpr_write_b32 a74, 0\n\t"
      "v_accvgpr_write_b32 a75, 0\n\t"
      "v_accvgpr_write_b32 a76, 0\n\t"
      "v_accvgpr_write_b32 a77, 0\n\t"
      "v_accvgpr_write_b32 a78, 0\n\t"
      "v_accvgpr_write_b32 a79, 0\n\t"
      "v_accvgpr_write_b32 a80, 0\n\t"
      "v_accvgpr_write_b32 a81, 0\n\t"
      "v_accvgpr_write_b32 a82, 0\n\t"
      "v_accvgpr_write_b32 a83, 0\n\t"
      "v_accvgpr_write_b32 a84, 0\n\t"
      "v_accvgpr_write_b32 a85, 0\n\t"
      "v_accvgpr_write_b32 a86, 0\n\t"
      "v_accvgpr_write_b32 a87, 0\n\t"
      "v_accvgpr_write_b32 a88, 0\n\t"
      "v_accvgpr_write_b32 a89, 0\n\t"
      "v_accvgpr_write_b32 a90, 0\n\t"
      "v_accvgpr_write_b32 a91, 0\n\t"
      "v_accvgpr_write_b32 a92, 0\n\t"
      "v_accvgpr_write_b32 a93, 0\n\t"
      "v_accvgpr_write_b32 a94, 0\n\t"
      "v_accvgpr_write_b32 a95, 0\n\t"
      "v_accvgpr_write_b32 a96, 0\n\t"
      "v_accvgpr_write_b32 a97, 0\n\t"
      "v_accvgpr_write_b32 a98, 0\n\t"
      "v_accvgpr_write_b32 a99, 0\n\t"
      "v_accvgpr_write_b32 a100, 0\n\t"
      "v_accvgpr_write_b32 a101, 0\n\t"
      "v_accvgpr_write_b32 a102, 0\n\t"
      "v_accvgpr_write_b32 a103, 0\n\t"
      "v_accvgpr_write_b32 a104, 0\n\t"
      "v_accvgpr_write_b32 a105, 0\n\t"
      "v_accvgpr_write_b32 a106, 0\n\t"
      "v_accvgpr_write_b32 a107, 0\n\t"
      "v_accvgpr_write_b32 a108, 0\n\t"
      "v_accvgpr_write_b32 a109, 0\n\t"
      "v_accvgpr_write_b32 a110, 0\n\t"
      "v_accvgpr_write_b32 a111, 0\n\t"
      "v_accvgpr_write_b32 a112, 0\n\t"
      "v_accvgpr_write_b32 a113, 0\n\t"
      "v_accvgpr_write_b32 a114, 0\n\t"
      "v_accvgpr_write_b32 a115, 0\n\t"
      "v_accvgpr_write_b32 a116, 0\n\t"
      "v_accvgpr_write_b32 a117, 0\n\t"
      "v_accvgpr_write_b32 a118, 0\n\t"
      "v_accvgpr_write_b32 a119, 0\n\t"
      "v_accvgpr_write_b32 a120, 0\n\t"
      "v_accvgpr_write_b32 a121, 0\n\t"
      "v_accvgpr_write_b32 a122, 0\n\t"
      "v_accvgpr_write_b32 a123, 0\n\t"
      "v_accvgpr_write_b32 a124, 0\n\t"
      "v_accvgpr_write_b32 a125, 0\n\t"
      "v_accvgpr_write_b32 a126, 0\n\t"
      "v_accvgpr_write_b32 a127, 0\n\t"
      "v_accvgpr_write_b32 a128, 0\n\t"
      "v_accvgpr_write_b32 a129, 0\n\t"
      "v_accvgpr_write_b32 a130, 0\n\t"
      "v_accvgpr_write_b32 a131, 0\n\t"
      "v_accvgpr_write_b32 a132, 0\n\t"
      "v_accvgpr_write_b32 a133, 0\n\t"
      "v_accvgpr_write_b32 a134, 0\n\t"
      "v_accvgpr_write_b32 a135, 0\n\t"
      "v_accvgpr_write_b32 a136, 0\n\t"
      "v_accvgpr_write_b32 a137, 0\n\t"
      "v_accvgpr_write_b32 a138, 0\n\t"
      "v_accvgpr_write_b32 a139, 0\n\t"
      "v_accvgpr_write_b32 a140, 0\n\t"
      "v_accvgpr_write_b32 a141, 0\n\t"
      "v_accvgpr_write_b32 a142, 0\n\t"
      "v_accvgpr_write_b32 a143, 0\n\t"
      "v_accvgpr_write_b32 a144, 0\n\t"
      "v_accvgpr_write_b32 a145, 0\n\t"
      "v_accvgpr_write_b32 a146, 0\n\t"
      "v_accvgpr_write_b32 a147, 0\n\t"
      "v_accvgpr_write_b32 a148, 0\n\t"
      "v_accvgpr_write_b32 a149, 0\n\t"
      "v_accvgpr_write_b32 a150, 0\n\t"
      "v_accvgpr_write_b32 a151, 0\n\t"
      "v_accvgpr_write_b32 a152, 0\n\t"
      "v_accvgpr_write_b32 a153, 0\n\t"
      "v_accvgpr_write_b32 a154, 0\n\t"
      "v_accvgpr_write_b32 a155, 0\n\t"
      "v_accvgpr_write_b32 a156, 0\n\t"
      "v_accvgpr_write_b32 a157, 0\n\t"
      "v_accvgpr_write_b32 a158, 0\n\t"
      "v_accvgpr_write_b32 a159, 0\n\t"
      "s_add_u32 s50, s51, 14\n\t"
      "s_mul_i32 s48, s50, 0x1400\n\t"
      "s_add_u32 s48, s48, s54\n\t"
      "s_lshl_b32 s49, s50, 12\n\t"
      "s_add_u32 s49, s49, s52\n\t"
      "s_waitcnt vmcnt(27)\n\t"
      "v_mov_b32 v15, s56\n\t"
      "v_mov_b32 v10, 0\n\t"
      "s_mov_b64 exec, 1\n\t"
      "s_nop 1\n\t"
      "buffer_store_dword v15, v10, s[36:39], s57 offen sc1\n\t"
      "s_mov_b64 exec, -1\n\t"
      "s_cmp_ge_u32 s51, 2\n\t"
      "s_cbranch_scc1 K2skip0\n\t"
      "v_mfma_f32_16x16x32_bf16 a[0:3], v[16:19], v[36:39], a[0:3]\n\t"
      "v_mfma_f32_16x16x32_bf16 a[20:23], v[16:19], v[40:43], a[20:23]\n\t"
      "v_mfma_f32_16x16x32_bf16 a[40:43], v[16:19], v[44:47], a[40:43]\n\t"
      "v_mfma_f32_16x16x32_bf16 a[60:63], v[16:19], v[48:51], a[60:63]\n\t"
      "v_mfma_f32_16x16x32_bf16 a[4:7], v[20:23], v[36:39], a[4:7]\n\t"
      "v_mfma_f32_16x16x32_bf16 a[24:27], v[20:23], v[40:43], a[24:27]\n\t"
      "v_mfma_f32_16x16x32_bf16 a[44:47], v[20:23], v[44:47], a[44:47]\n\t"
      "v_mfma_f32_16x16x32_bf16 a[64:67], v[20:23], v[48:51], a[64:67]\n\t"
      "v_mfma_f32_16x16x32_bf16 a[8:11], v[24:27], v[36:39], a[8:11]\n\t"
      "v_mfma_f32_16x16x32_bf16 a[28:31], v[24:27], v[40:43], a[28:31]\n\t"
      "v_mfma_f32_16x16x32_bf16 a[48:51], v[24:27], v[44:47], a[48:51]\n\t"
      "v_mfma_f32_16x16x32_bf16 a[68:71], v[24:27], v[48:51], a[68:71]\n\t"
      "v_mfma_f32_16x16x32_bf16 a[12:15], v[28:31], v[36:39], a[12:15]\n\t"
      "v_mfma_f32_16x16x32_bf16 a[32:35], v[28:31], v[40:43], a[32:35]\n\t"
      "v_mfma_f32_16x16x32_bf16 a[52:55], v[28:31], v[44:47], a[52:55]\n\t"
      "v_mfma_f32_16x16x32_bf16 a[72:75], v[28:31], v[48:51], a[72:75]\n\t"
      "v_mfma_f32_16x16x32_bf16 a[16:19], v[32:35], v[36:39], a[16:19]\n\t"
      "v_mfma_f32_16x16x32_bf16 a[36:39], v[32:35], v[40:43], a[36:39]\n\t"
      "v_mfma_f32_16x16x32_bf16 a[56:59], v[32:35], v[44:47], a[56:59]\n\t"
      "v_mfma_f32_16x16x32_bf16 a[76:79], v[32:35], v[48:51], a[76:79]\n\t"
      "K2skip0:\n\t"
      "buffer_load_dwordx4 v[180:183], v0, s[36:39], s49 offen sc1\n\t"
      "buffer_load_dwordx4 v[184:187], v0, s[36:39], s49 offen offset:1024 sc1\n\t"
      "buffer_load_dwordx4 v[188:191], v0, s[36:39], s49 offen offset:2048 sc1\n\t"
      "buffer_load_dwordx4 v[192:195], v0, s[36:39], s49 offen offset:3072 sc1\n\t"
      "buffer_load_dwordx4 v[160:163], v0, s[36:39], s48 offen sc1\n\t"
      "buffer_load_dwordx4 v[164:167], v0, s[36:39], s48 offen offset:1024 sc1\n\t"
      "buffer_load_dwordx4 v[168:171], v0, s[36:39], s48 offen offset:2048 sc1\n\t"
      "buffer_load_dwordx4 v[172:175], v0, s[36:39], s48 offen offset:3072 sc1\n\t"
      "buffer_load_dwordx4 v[176:179], v1, s[36:39], s48 offen sc1\n\t"
      "s_add_u32 s50, s51, 18\n\t"
      "s_mul_i32 s48, s50, 0x1400\n\t"
      "s_add_u32 s48, s48, s54\n\t"
      "s_lshl_b32 s49, s50, 12\n\t"
      "s_add_u32 s49, s49, s52\n\t"
      "s_waitcnt vmcnt(27)\n\t"
      "v_mfma_f32_16x16x32_bf16 a[0:3], v[52:55], v[72:75], a[0:3]\n\t"
      "v_mfma_f32_16x16x32_bf16 a[20:23], v[52:55], v[76:79], a[20:23]\n\t"
      "buffer_load_dwordx4 v[36:39], v0, s[36:39], s49 offen sc1\n\t"
      "v_mfma_f32_16x16x32_bf16 a[40:43], v[52:55], v[80:83], a[40:43]\n\t"
      "v_mfma_f32_16x16x32_bf16 a[60:63], v[52:55], v[84:87], a[60:63]\n\t"
      "buffer_load_dwordx4 v[40:43], v0, s[36:39], s49 offen offset:1024 sc1\n\t"
      "v_mfma_f32_16x16x32_bf16 a[4:7], v[56:59], v[72:75], a[4:7]\n\t"
      "v_mfma_f32_16x16x32_bf16 a[24:27], v[56:59], v[76:79], a[24:27]\n\t"
      "buffer_load_dwordx4 v[44:47], v0, s[36:39], s49 offen offset:2048 sc1\n\t"
      "v_mfma_f32_16x16x32_bf16 a[44:47], v[56:59], v[80:83], a[44:47]\n\t"
      "v_mfma_f32_16x16x32_bf16 a[64:67], v[56:59], v[84:87], a[64:67]\n\t"
      "buffer_load_dwordx4 v[48:51], v0, s[36:39], s49 offen offset:3072 sc1\n\t"
      "v_mfma_f32_16x16x32_bf16 a[8:11], v[60:63], v[72:75], a[8:11]\n\t"
      "v_mfma_f32_16x16x32_bf16 a[28:31], v[60:63], v[76:79], a[28:31]\n\t"
      "buffer_load_dwordx4 v[16:19], v0, s[36:39], s48 offen sc1\n\t"
      "v_mfma_f32_16x16x32_bf16 a[48:51], v[60:63], v[80:83], a[48:51]\n\t"
      "v_mfma_f32_16x16x32_bf16 a[68:71], v[60:63], v[84:87], a[68:71]\n\t"
      "buffer_load_dwordx4 v[20:23], v0, s[36:39], s48 offen offset:1024 sc1\n\t"
      "v_mfma_f32_16x16x32_bf16 a[12:15], v[64:67], v[72:75], a[12:15]\n\t"
      "v_mfma_f32_16x16x32_bf16 a[32:35], v[64:67], v[76:79], a[32:35]\n\t"
      "buffer_load_dwordx4 v[24:27], v0, s[36:39], s48 offen offset:2048 sc1\n\t"
      "v_mfma_f32_16x16x32_bf16 a[52:55], v[64:67], v[80:83], a[52:55]\n\t"
      "v_mfma_f32_16x16x32_bf16 a[72:75], v[64:67], v[84:87], a[72:75]\n\t"
      "buffer_load_dwordx4 v[28:31], v0, s[36:39], s48 offen offset:3072 sc1\n\t"
      "v_mfma_f32_16x16x32_bf16 a[16:19], v[68:71], v[72:75], a[16:19]\n\t"
      "v_mfma_f32_16x16x32_bf16 a[36:39], v[68:71], v[76:79], a[36:39]\n\t"
      "buffer_load_dwordx4 v[32:35], v1, s[36:39], s48 offen sc1\n\t"
      "v_mfma_f32_16x16x32_bf16 a[56:59], v[68:71], v[80:83], a[56:59]\n\t"
      "v_mfma_f32_16x16x32_bf16 a[76:79], v[68:71], v[84:87], a[76:79]\n\t"
      "s_add_u32 s50, s51, 22\n\t"
      "s_mul_i32 s48, s50, 0x1400\n\t"
      "s_add_u32 s48, s48, s54\n\t"
      "s_lshl_b32 s49, s50, 12\n\t"
      "s_add_u32 s49, s49, s52\n\t"
      "s_waitcnt vmcnt(27)\n\t"
      "v_mfma_f32_16x16x32_bf16 a[0:3], v[88:91], v[108:111], a[0:3]\n\t"
      "v_mfma_f32_16x16x32_bf16 a[20:23], v[88:91], v[112:115], a[20:23]\n\t"
      "buffer_load_dwordx4 v[72:75], v0, s[36:39], s49 offen sc1\n\t"
      "v_mfma_f32_16x16x32_bf16 a[40:43], v[88:91], v[116:119], a[40:43]\n\t"
      "v_mfma_f32_16x16x32_bf16 a[60:63], v[88:91], v[120:123], a[60:63]\n\t"
      "buffer_load_dwordx4 v[76:79], v0, s[36:39], s49 offen offset:1024 sc1\n\t"
      "v_mfma_f32_16x16x32_bf16 a[4:7], v[92:95], v[108:111], a[4:7]\n\t"
      "v_mfma_f32_16x16x32_bf16 a[24:27], v[92:95], v[112:115], a[24:27]\n\t"
      "buffer_load_dwordx4 v[80:83], v0, s[36:39], s49 offen offset:2048 sc1\n\t"
      "v_mfma_f32_16x16x32_bf16 a[44:47], v[92:95], v[116:119], a[44:47]\n\t"
      "v_mfma_f32_16x16x32_bf16 a[64:67], v[92:95], v[120:123], a[64:67]\n\t"
      "buffer_load_dwordx4 v[84:87], v0, s[36:39], s49 offen offset:3072 sc1\n\t"
      "v_mfma_f32_16x16x32_bf16 a[8:11], v[96:99], v[108:111], a[8:11]\n\t"
      "v_mfma_f32_16x16x32_bf16 a[28:31], v[96:99], v[112:115], a[28:31]\n\t"
      "buffer_load_dwordx4 v[52:55], v0, s[36:39], s48 offen sc1\n\t"
      "v_mfma_f32_16x16x32_bf16 a[48:51], v[96:99], v[116:119], a[48:51]\n\t"
      "v_mfma_f32_16x16x32_bf16 a[68:71], v[96:99], v[120:123], a[68:71]\n\t"
      "buffer_load_dwordx4 v[56:59], v0, s[36:39], s48 offen offset:1024 sc1\n\t"
      "v_mfma_f32_16x16x32_bf16 a[12:15], v[100:103], v[108:111], a[12:15]\n\t"
      "v_mfma_f32_16x16x32_bf16 a[32:35], v[100:103], v[112:115], a[32:35]\n\t"
      "buffer_load_dwordx4 v[60:63], v0, s[36:39], s48 offen offset:2048 sc1\n\t"
      "v_mfma_f32_16x16x32_bf16 a[52:55], v[100:103], v[116:119], a[52:55]\n\t"
      "v_mfma_f32_16x16x32_bf16 a[72:75], v[100:103], v[120:123], a[72:75]\n\t"
      "buffer_load_dwordx4 v[64:67], v0, s[36:39], s48 offen offset:3072 sc1\n\t"
      "v_mfma_f32_16x16x32_bf16 a[16:19], v[104:107], v[108:111], a[16:19]\n\t"
      "v_mfma_f32_16x16x32_bf16 a[36:39], v[104:107], v[112:115], a[36:39]\n\t"
      "buffer_load_dwordx4 v[68:71], v1, s[36:39], s48 offen sc1\n\t"
      "v_mfma_f32_16x16x32_bf16 a[56:59], v[104:107], v[116:119], a[56:59]\n\t"
      "v_mfma_f32_16x16x32_bf16 a[76:79], v[104:107], v[120:123], a[76:79]\n\t"
      "s_add_u32 s50, s51, 26\n\t"
      "s_mul_i32 s48, s50, 0x1400\n\t"
      "s_add_u32 s48, s48, s54\n\t"
      "s_lshl_b32 s49, s50, 12\n\t"
      "s_add_u32 s49, s49, s52\n\t"
      "s_waitcnt vmcnt(27)\n\t"
      "v_mfma_f32_16x16x32_bf16 a[0:3], v[124:127], v[144:147], a[0:3]\n\t"
      "v_mfma_f32_16x16x32_bf16 a[20:23], v[124:127], v[148:151], a[20:23]\n\t"
      "buffer_load_dwordx4 v[108:111], v0, s[36:39], s49 offen sc1\n\t"
      "v_mfma_f32_16x16x32_bf16 a[40:43], v[124:127], v[152:155], a[40:43]\n\t"
      "v_mfma_f32_16x16x32_bf16 a[60:63], v[124:127], v[156:159], a[60:63]\n\t"
      "buffer_load_dwordx4 v[112:115], v0, s[36:39], s49 offen offset:1024 sc1\n\t"
      "v_mfma_f32_16x16x32_bf16 a[4:7], v[128:131], v[144:147], a[4:7]\n\t"
      "v_mfma_f32_16x16x32_bf16 a[24:27], v[128:131], v[148:151], a[24:27]\n\t"
      "buffer_load_dwordx4 v[116:119], v0, s[36:39], s49 offen offset:2048 sc1\n\t"
      "v_mfma_f32_16x16x32_bf16 a[44:47], v[128:131], v[152:155], a[44:47]\n\t"
      "v_mfma_f32_16x16x32_bf16 a[64:67], v[128:131], v[156:159], a[64:67]\n\t"
      "buffer_load_dwordx4 v[120:123], v0, s[36:39], s49 offen offset:3072 sc1\n\t"
      "v_mfma_f32_16x16x32_bf16 a[8:11], v[132:135], v[144:147], a[8:11]\n\t"
      "v_mfma_f32_16x16x32_bf16 a[28:31], v[132:135], v[148:151], a[28:31]\n\t"
      "buffer_load_dwordx4 v[88:91], v0, s[36:39], s48 offen sc1\n\t"
      "v_mfma_f32_16x16x32_bf16 a[48:51], v[132:135], v[152:155], a[48:51]\n\t"
      "v_mfma_f32_16x16x32_bf16 a[68:71], v[132:135], v[156:159], a[68:71]\n\t"
      "buffer_load_dwordx4 v[92:95], v0, s[36:39], s48 offen offset:1024 sc1\n\t"
      "v_mfma_f32_16x16x32_bf16 a[12:15], v[136:139], v[144:147], a[12:15]\n\t"
      "v_mfma_f32_16x16x32_bf16 a[32:35], v[136:139], v[148:151], a[32:35]\n\t"
      "buffer_load_dwordx4 v[96:99], v0, s[36:39], s48 offen offset:2048 sc1\n\t"
      "v_mfma_f32_16x16x32_bf16 a[52:55], v[136:139], v[152:155], a[52:55]\n\t"
      "v_mfma_f32_16x16x32_bf16 a[72:75], v[136:139], v[156:159], a[72:75]\n\t"
      "buffer_load_dwordx4 v[100:103], v0, s[36:39], s48 offen offset:3072 sc1\n\t"
      "v_mfma_f32_16x16x32_bf16 a[16:19], v[140:143], v[144:147], a[16:19]\n\t"
      "v_mfma_f32_16x16x32_bf16 a[36:39], v[140:143], v[148:151], a[36:39]\n\t"
      "buffer_load_dwordx4 v[104:107], v1, s[36:39], s48 offen sc1\n\t"
      "v_mfma_f32_16x16x32_bf16 a[56:59], v[140:143], v[152:155], a[56:59]\n\t"
      "v_mfma_f32_16x16x32_bf16 a[76:79], v[140:143], v[156:159], a[76:79]\n\t"
      "s_add_u32 s50, s51, 30\n\t"
      "s_mul_i32 s48, s50, 0x1400\n\t"
      "s_add_u32 s48, s48, s54\n\t"
      "s_lshl_b32 s49, s50, 12\n\t"
      "s_add_u32 s49, s49, s52\n\t"
      "s_waitcnt vmcnt(27)\n\t"
      "v_mfma_f32_16x16x32_bf16 a[0:3], v[160:163], v[180:183], a[0:3]\n\t"
      "v_mfma_f32_16x16x32_bf16 a[20:23], v[160:163], v[184:187], a[20:23]\n\t"
      "buffer_load_dwordx4 v[144:147], v0, s[36:39], s49 offen sc1\n\t"
      "v_mfma_f32_16x16x32_bf16 a[40:43], v[160:163], v[188:191], a[40:43]\n\t"
      "v_mfma_f32_16x16x32_bf16 a[60:63], v[160:163], v[192:195], a[60:63]\n\t"
      "buffer_load_dwordx4 v[148:151], v0, s[36:39], s49 offen offset:1024 sc1\n\t"
      "v_mfma_f32_16x16x32_bf16 a[4:7], v[164:167], v[180:183], a[4:7]\n\t"
      "v_mfma_f32_16x16x32_bf16 a[24:27], v[164:167], v[184:187], a[24:27]\n\t"
      "buffer_load_dwordx4 v[152:155], v0, s[36:39], s49 offen offset:2048 sc1\n\t"
      "v_mfma_f32_16x16x32_bf16 a[44:47], v[164:167], v[188:191], a[44:47]\n\t"
      "v_mfma_f32_16x16x32_bf16 a[64:67], v[164:167], v[192:195], a[64:67]\n\t"
      "buffer_load_dwordx4 v[156:159], v0, s[36:39], s49 offen offset:3072 sc1\n\t"
      "v_mfma_f32_16x16x32_bf16 a[8:11], v[168:171], v[180:183], a[8:11]\n\t"
      "v_mfma_f32_16x16x32_bf16 a[28:31], v[168:171], v[184:187], a[28:31]\n\t"
      "buffer_load_dwordx4 v[124:127], v0, s[36:39], s48 offen sc1\n\t"
      "v_mfma_f32_16x16x32_bf16 a[48:51], v[168:171], v[188:191], a[48:51]\n\t"
      "v_mfma_f32_16x16x32_bf16 a[68:71], v[168:171], v[192:195], a[68:71]\n\t"
      "buffer_load_dwordx4 v[128:131], v0, s[36:39], s48 offen offset:1024 sc1\n\t"
      "v_mfma_f32_16x16x32_bf16 a[12:15], v[172:175], v[180:183], a[12:15]\n\t"
      "v_mfma_f32_16x16x32_bf16 a[32:35], v[172:175], v[184:187], a[32:35]\n\t"
      "buffer_load_dwordx4 v[132:135], v0, s[36:39], s48 offen offset:2048 sc1\n\t"
      "v_mfma_f32_16x16x32_bf16 a[52:55], v[172:175], v[188:191], a[52:55]\n\t"
      "v_mfma_f32_16x16x32_bf16 a[72:75], v[172:175], v[192:195], a[72:75]\n\t"
      "buffer_load_dwordx4 v[136:139], v0, s[36:39], s48 offen offset:3072 sc1\n\t"
      "v_mfma_f32_16x16x32_bf16 a[16:19], v[176:179], v[180:183], a[16:19]\n\t"
      "v_mfma_f32_16x16x32_bf16 a[36:39], v[176:179], v[184:187], a[36:39]\n\t"
      "buffer_load_dwordx4 v[140:143], v1, s[36:39], s48 offen sc1\n\t"
      "v_mfma_f32_16x16x32_bf16 a[56:59], v[176:179], v[188:191], a[56:59]\n\t"
      "v_mfma_f32_16x16x32_bf16 a[76:79], v[176:179], v[192:195], a[76:79]\n\t"
      "s_add_u32 s50, s51, 34\n\t"
      "s_mul_i32 s48, s50, 0x1400\n\t"
      "s_add_u32 s48, s48, s54\n\t"
      "s_lshl_b32 s49, s50, 12\n\t"
      "s_add_u32 s49, s49, s52\n\t"
      "s_waitcnt vmcnt(27)\n\t"
      "v_mfma_f32_16x16x32_bf16 a[0:3], v[16:19], v[36:39], a[0:3]\n\t"
      "v_mfma_f32_16x16x32_bf16 a[20:23], v[16:19], v[40:43], a[20:23]\n\t"
      "buffer_load_dwordx4 v[180:183], v0, s[36:39], s49 offen sc1\n\t"
      "v_mfma_f32_16x16x32_bf16 a[40:43], v[16:19], v[44:47], a[40:43]\n\t"
      "v_mfma_f32_16x16x32_bf16 a[60:63], v[16:19], v[48:51], a[60:63]\n\t"
      "buffer_load_dwordx4 v[184:187], v0, s[36:39], s49 offen offset:1024 sc1\n\t"
      "v_mfma_f32_16x16x32_bf16 a[4:7], v[20:23], v[36:39], a[4:7]\n\t"
      "v_mfma_f32_16x16x32_bf16 a[24:27], v[20:23], v[40:43], a[24:27]\n\t"
      "buffer_load_dwordx4 v[188:191], v0, s[36:39], s49 offen offset:2048 sc1\n\t"
      "v_mfma_f32_16x16x32_bf16 a[44:47], v[20:23], v[44:47], a[44:47]\n\t"
      "v_mfma_f32_16x16x32_bf16 a[64:67], v[20:23], v[48:51], a[64:67]\n\t"
      "buffer_load_dwordx4 v[192:195], v0, s[36:39], s49 offen offset:3072 sc1\n\t"
      "v_mfma_f32_16x16x32_bf16 a[8:11], v[24:27], v[36:39], a[8:11]\n\t"
      "v_mfma_f32_16x16x32_bf16 a[28:31], v[24:27], v[40:43], a[28:31]\n\t"
      "buffer_load_dwordx4 v[160:163], v0, s[36:39], s48 offen sc1\n\t"
      "v_mfma_f32_16x16x32_bf16 a[48:51], v[24:27], v[44:47], a[48:51]\n\t"
      "v_mfma_f32_16x16x32_bf16 a[68:71], v[24:27], v[48:51], a[68:71]\n\t"
      "buffer_load_dwordx4 v[164:167], v0, s[36:39], s48 offen offset:1024 sc1\n\t"
      "v_mfma_f32_16x16x32_bf16 a[12:15], v[28:31], v[36:39], a[12:15]\n\t"
      "v_mfma_f32_16x16x32_bf16 a[32:35], v[28:31], v[40:43], a[32:35]\n\t"
      "buffer_load_dwordx4 v[168:171], v0, s[36:39], s48 offen offset:2048 sc1\n\t"
      "v_mfma_f32_16x16x32_bf16 a[52:55], v[28:31], v[44:47], a[52:55]\n\t"
      "v_mfma_f32_16x16x32_bf16 a[72:75], v[28:31], v[48:51], a[72:75]\n\t"
      "buffer_load_dwordx4 v[172:175], v0, s[36:39], s48 offen offset:3072 sc1\n\t"
      "v_mfma_f32_16x16x32_bf16 a[16:19], v[32:35], v[36:39], a[16:19]\n\t"
      "v_mfma_f32_16x16x32_bf16 a[36:39], v[32:35], v[40:43], a[36:39]\n\t"
      "buffer_load_dwordx4 v[176:179], v1, s[36:39], s48 offen sc1\n\t"
      "v_mfma_f32_16x16x32_bf16 a[56:59], v[32:35], v[44:47], a[56:59]\n\t"
      "v_mfma_f32_16x16x32_bf16 a[76:79], v[32:35], v[48:51], a[76:79]\n\t"
      "s_add_u32 s50, s51, 38\n\t"
      "s_mul_i32 s48, s50, 0x1400\n\t"
      "s_add_u32 s48, s48, s54\n\t"
      "s_lshl_b32 s49, s50, 12\n\t"
      "s_add_u32 s49, s49, s52\n\t"
      "s_waitcnt vmcnt(27)\n\t"
      "v_mfma_f32_16x16x32_bf16 a[0:3], v[52:55], v[72:75], a[0:3]\n\t"
      "v_mfma_f32_16x16x32_bf16 a[20:23], v[52:55], v[76:79], a[20:23]\n\t"
      "buffer_load_dwordx4 v[36:39], v0, s[36:39], s49 offen sc1\n\t"
      "v_mfma_f32_16x16x32_bf16 a[40:43], v[52:55], v[80:83], a[40:43]\n\t"
      "v_mfma_f32_16x16x32_bf16 a[60:63], v[52:55], v[84:87], a[60:63]\n\t"
      "buffer_load_dwordx4 v[40:43], v0, s[36:39], s49 offen offset:1024 sc1\n\t"
      "v_mfma_f32_16x16x32_bf16 a[4:7], v[56:59], v[72:75], a[4:7]\n\t"
      "v_mfma_f32_16x16x32_bf16 a[24:27], v[56:59], v[76:79], a[24:27]\n\t"
      "buffer_load_dwordx4 v[44:47], v0, s[36:39], s49 offen offset:2048 sc1\n\t"
      "v_mfma_f32_16x16x32_bf16 a[44:47], v[56:59], v[80:83], a[44:47]\n\t"
      "v_mfma_f32_16x16x32_bf16 a[64:67], v[56:59], v[84:87], a[64:67]\n\t"
      "buffer_load_dwordx4 v[48:51], v0, s[36:39], s49 offen offset:3072 sc1\n\t"
      "v_mfma_f32_16x16x32_bf16 a[8:11], v[60:63], v[72:75], a[8:11]\n\t"
      "v_mfma_f32_16x16x32_bf16 a[28:31], v[60:63], v[76:79], a[28:31]\n\t"
      "buffer_load_dwordx4 v[16:19], v0, s[36:39], s48 offen sc1\n\t"
      "v_mfma_f32_16x16x32_bf16 a[48:51], v[60:63], v[80:83], a[48:51]\n\t"
      "v_mfma_f32_16x16x32_bf16 a[68:71], v[60:63], v[84:87], a[68:71]\n\t"
      "buffer_load_dwordx4 v[20:23], v0, s[36:39], s48 offen offset:1024 sc1\n\t"
      "v_mfma_f32_16x16x32_bf16 a[12:15], v[64:67], v[72:75], a[12:15]\n\t"
      "v_mfma_f32_16x16x32_bf16 a[32:35], v[64:67], v[76:79], a[32:35]\n\t"
      "buffer_load_dwordx4 v[24:27], v0, s[36:39], s48 offen offset:2048 sc1\n\t"
      "v_mfma_f32_16x16x32_bf16 a[52:55], v[64:67], v[80:83], a[52:55]\n\t"
      "v_mfma_f32_16x16x32_bf16 a[72:75], v[64:67], v[84:87], a[72:75]\n\t"
      "buffer_load_dwordx4 v[28:31], v0, s[36:39], s48 offen offset:3072 sc1\n\t"
      "v_mfma_f32_16x16x32_bf16 a[16:19], v[68:71], v[72:75], a[16:19]\n\t"
      "v_mfma_f32_16x16x32_bf16 a[36:39], v[68:71], v[76:79], a[36:39]\n\t"
      "buffer_load_dwordx4 v[32:35], v1, s[36:39], s48 offen sc1\n\t"
      "v_mfma_f32_16x16x32_bf16 a[56:59], v[68:71], v[80:83], a[56:59]\n\t"
      "v_mfma_f32_16x16x32_bf16 a[76:79], v[68:71], v[84:87], a[76:79]\n\t"
      "buffer_load_dword v12, v11, s[36:39], s59 offen sc1\n\t"
      "buffer_load_dwordx2 v[4:5], v6, s[36:39], s62 offen sc1\n\t"
      "s_add_u32 s50, s51, 42\n\t"
      "s_mul_i32 s48, s50, 0x1400\n\t"
      "s_add_u32 s48, s48, s54\n\t"
      "s_lshl_b32 s49, s50, 12\n\t"
      "s_add_u32 s49, s49, s52\n\t"
      "s_waitcnt vmcnt(27)\n\t"
      "v_mfma_f32_16x16x32_bf16 a[0:3], v[88:91], v[108:111], a[0:3]\n\t"
      "v_mfma_f32_16x16x32_bf16 a[20:23], v[88:91], v[112:115], a[20:23]\n\t"
      "buffer_load_dwordx4 v[72:75], v0, s[36:39], s49 offen sc1\n\t"
      "v_mfma_f32_16x16x32_bf16 a[40:43], v[88:91], v[116:119], a[40:43]\n\t"
      "v_mfma_f32_16x16x32_bf16 a[60:63], v[88:91], v[120:123], a[60:63]\n\t"
      "buffer_load_dwordx4 v[76:79], v0, s[36:39], s49 offen offset:1024 sc1\n\t"
      "v_mfma_f32_16x16x32_bf16 a[4:7], v[92:95], v[108:111], a[4:7]\n\t"
      "v_mfma_f32_16x16x32_bf16 a[24:27], v[92:95], v[112:115], a[24:27]\n\t"
      "buffer_load_dwordx4 v[80:83], v0, s[36:39], s49 offen offset:2048 sc1\n\t"
      "v_mfma_f32_16x16x32_bf16 a[44:47], v[92:95], v[116:119], a[44:47]\n\t"
      "v_mfma_f32_16x16x32_bf16 a[64:67], v[92:95], v[120:123], a[64:67]\n\t"
      "buffer_load_dwordx4 v[84:87], v0, s[36:39], s49 offen offset:3072 sc1\n\t"
      "v_mfma_f32_16x16x32_bf16 a[8:11], v[96:99], v[108:111], a[8:11]\n\t"
      "v_mfma_f32_16x16x32_bf16 a[28:31], v[96:99], v[112:115], a[28:31]\n\t"
      "buffer_load_dwordx4 v[52:55], v0, s[36:39], s48 offen sc1\n\t"
      "v_mfma_f32_16x16x32_bf16 a[48:51], v[96:99], v[116:119], a[48:51]\n\t"
      "v_mfma_f32_16x16x32_bf16 a[68:71], v[96:99], v[120:123], a[68:71]\n\t"
      "buffer_load_dwordx4 v[56:59], v0, s[36:39], s48 offen offset:1024 sc1\n\t"
      "v_mfma_f32_16x16x32_bf16 a[12:15], v[100:103], v[108:111], a[12:15]\n\t"
      "v_mfma_f32_16x16x32_bf16 a[32:35], v[100:103], v[112:115], a[32:35]\n\t"
      "buffer_load_dwordx4 v[60:63], v0, s[36:39], s48 offen offset:2048 sc1\n\t"
      "v_mfma_f32_16x16x32_bf16 a[52:55], v[100:103], v[116:119], a[52:55]\n\t"
      "v_mfma_f32_16x16x32_bf16 a[72:75], v[100:103], v[120:123], a[72:75]\n\t"
      "buffer_load_dwordx4 v[64:67], v0, s[36:39], s48 offen offset:3072 sc1\n\t"
      "v_mfma_f32_16x16x32_bf16 a[16:19], v[104:107], v[108:111], a[16:19]\n\t"
      "v_mfma_f32_16x16x32_bf16 a[36:39], v[104:107], v[112:115], a[36:39]\n\t"
      "buffer_load_dwordx4 v[68:71], v1, s[36:39], s48 offen sc1\n\t"
      "v_mfma_f32_16x16x32_bf16 a[56:59], v[104:107], v[116:119], a[56:59]\n\t"
      "v_mfma_f32_16x16x32_bf16 a[76:79], v[104:107], v[120:123], a[76:79]\n\t"
      "s_add_u32 s50, s51, 46\n\t"
      "s_mul_i32 s48, s50, 0x1400\n\t"
      "s_add_u32 s48, s48, s54\n\t"
      "s_lshl_b32 s49, s50, 12\n\t"
      "s_add_u32 s49, s49, s52\n\t"
      "s_waitcnt vmcnt(27)\n\t"
      "v_mfma_f32_16x16x32_bf16 a[0:3], v[124:127], v[144:147], a[0:3]\n\t"
      "v_mfma_f32_16x16x32_bf16 a[20:23], v[124:127], v[148:151], a[20:23]\n\t"
      "buffer_load_dwordx4 v[108:111], v0, s[36:39], s49 offen sc1\n\t"
      "v_mfma_f32_16x16x32_bf16 a[40:43], v[124:127], v[152:155], a[40:43]\n\t"
      "v_mfma_f32_16x16x32_bf16 a[60:63], v[124:127], v[156:159], a[60:63]\n\t"
      "buffer_load_dwordx4 v[112:115], v0, s[36:39], s49 offen offset:1024 sc1\n\t"
      "v_mfma_f32_16x16x32_bf16 a[4:7], v[128:131], v[144:147], a[4:7]\n\t"
      "v_mfma_f32_16x16x32_bf16 a[24:27], v[128:131], v[148:151], a[24:27]\n\t"
      "buffer_load_dwordx4 v[116:119], v0, s[36:39], s49 offen offset:2048 sc1\n\t"
      "v_mfma_f32_16x16x32_bf16 a[44:47], v[128:131], v[152:155], a[44:47]\n\t"
      "v_mfma_f32_16x16x32_bf16 a[64:67], v[128:131], v[156:159], a[64:67]\n\t"
      "buffer_load_dwordx4 v[120:123], v0, s[36:39], s49 offen offset:3072 sc1\n\t"
      "v_mfma_f32_16x16x32_bf16 a[8:11], v[132:135], v[144:147], a[8:11]\n\t"
      "v_mfma_f32_16x16x32_bf16 a[28:31], v[132:135], v[148:151], a[28:31]\n\t"
      "buffer_load_dwordx4 v[88:91], v0, s[36:39], s48 offen sc1\n\t"
      "v_mfma_f32_16x16x32_bf16 a[48:51], v[132:135], v[152:155], a[48:51]\n\t"
      "v_mfma_f32_16x16x32_bf16 a[68:71], v[132:135], v[156:159], a[68:71]\n\t"
      "buffer_load_dwordx4 v[92:95], v0, s[36:39], s48 offen offset:1024 sc1\n\t"
      "v_mfma_f32_16x16x32_bf16 a[12:15], v[136:139], v[144:147], a[12:15]\n\t"
      "v_mfma_f32_16x16x32_bf16 a[32:35], v[136:139], v[148:151], a[32:35]\n\t"
      "buffer_load_dwordx4 v[96:99], v0, s[36:39], s48 offen offset:2048 sc1\n\t"
      "v_mfma_f32_16x16x32_bf16 a[52:55], v[136:139], v[152:155], a[52:55]\n\t"
      "v_mfma_f32_16x16x32_bf16 a[72:75], v[136:139], v[156:159], a[72:75]\n\t"
      "buffer_load_dwordx4 v[100:103], v0, s[36:39], s48 offen offset:3072 sc1\n\t"
      "v_mfma_f32_16x16x32_bf16 a[16:19], v[140:143], v[144:147], a[16:19]\n\t"
      "v_mfma_f32_16x16x32_bf16 a[36:39], v[140:143], v[148:151], a[36:39]\n\t"
      "buffer_load_dwordx4 v[104:107], v1, s[36:39], s48 offen sc1\n\t"
      "v_mfma_f32_16x16x32_bf16 a[56:59], v[140:143], v[152:155], a[56:59]\n\t"
      "v_mfma_f32_16x16x32_bf16 a[76:79], v[140:143], v[156:159], a[76:79]\n\t"
      "s_add_u32 s50, s51, 50\n\t"
      "s_mul_i32 s48, s50, 0x1400\n\t"
      "s_add_u32 s48, s48, s54\n\t"
      "s_lshl_b32 s49, s50, 12\n\t"
      "s_add_u32 s49, s49, s52\n\t"
      "s_waitcnt vmcnt(27)\n\t"
      "v_mfma_f32_16x16x32_bf16 a[0:3], v[160:163], v[180:183], a[0:3]\n\t"
      "v_mfma_f32_16x16x32_bf16 a[20:23], v[160:163], v[184:187], a[20:23]\n\t"
      "buffer_load_dwordx4 v[144:147], v0, s[36:39], s49 offen sc1\n\t"
      "v_mfma_f32_16x16x32_bf16 a[40:43], v[160:163], v[188:191], a[40:43]\n\t"
      "v_mfma_f32_16x16x32_bf16 a[60:63], v[160:163], v[192:195], a[60:63]\n\t"
      "buffer_load_dwordx4 v[148:151], v0, s[36:39], s49 offen offset:1024 sc1\n\t"
      "v_mfma_f32_16x16x32_bf16 a[4:7], v[164:167], v[180:183], a[4:7]\n\t"
      "v_mfma_f32_16x16x32_bf16 a[24:27], v[164:167], v[184:187], a[24:27]\n\t"
      "buffer_load_dwordx4 v[152:155], v0, s[36:39], s49 offen offset:2048 sc1\n\t"
      "v_mfma_f32_16x16x32_bf16 a[44:47], v[164:167], v[188:191], a[44:47]\n\t"
      "v_mfma_f32_16x16x32_bf16 a[64:67], v[164:167], v[192:195], a[64:67]\n\t"
      "buffer_load_dwordx4 v[156:159], v0, s[36:39], s49 offen offset:3072 sc1\n\t"
      "v_mfma_f32_16x16x32_bf16 a[8:11], v[168:171], v[180:183], a[8:11]\n\t"
      "v_mfma_f32_16x16x32_bf16 a[28:31], v[168:171], v[184:187], a[28:31]\n\t"
      "buffer_load_dwordx4 v[124:127], v0, s[36:39], s48 offen sc1\n\t"
      "v_mfma_f32_16x16x32_bf16 a[48:51], v[168:171], v[188:191], a[48:51]\n\t"
      "v_mfma_f32_16x16x32_bf16 a[68:71], v[168:171], v[192:195], a[68:71]\n\t"
      "buffer_load_dwordx4 v[128:131], v0, s[36:39], s48 offen offset:1024 sc1\n\t"
      "v_mfma_f32_16x16x32_bf16 a[12:15], v[172:175], v[180:183], a[12:15]\n\t"
      "v_mfma_f32_16x16x32_bf16 a[32:35], v[172:175], v[184:187], a[32:35]\n\t"
      "buffer_load_dwordx4 v[132:135], v0, s[36:39], s48 offen offset:2048 sc1\n\t"
      "v_mfma_f32_16x16x32_bf16 a[52:55], v[172:175], v[188:191], a[52:55]\n\t"
      "v_mfma_f32_16x16x32_bf16 a[72:75], v[172:175], v[192:195], a[72:75]\n\t"
      "buffer_load_dwordx4 v[136:139], v0, s[36:39], s48 offen offset:3072 sc1\n\t"
      "v_mfma_f32_16x16x32_bf16 a[16:19], v[176:179], v[180:183], a[16:19]\n\t"
      "v_mfma_f32_16x16x32_bf16 a[36:39], v[176:179], v[184:187], a[36:39]\n\t"
      "buffer_load_dwordx4 v[140:143], v1, s[36:39], s48 offen sc1\n\t"
      "v_mfma_f32_16x16x32_bf16 a[56:59], v[176:179], v[188:191], a[56:59]\n\t"
      "v_mfma_f32_16x16x32_bf16 a[76:79], v[176:179], v[192:195], a[76:79]\n\t"
      "s_waitcnt vmcnt(27)\n\t"
      "s_mov_b32 s58, 0\n\t"
      "v_cmp_ne_u32 vcc, s56, v12\n\t"
      "v_cmp_ne_u32 s[60:61], s56, v4\n\t"
      "s_nop 4\n\t"
      "s_or_b64 vcc, vcc, s[60:61]\n\t"
      "v_cmp_ne_u32 s[60:61], s56, v5\n\t"
      "s_nop 4\n\t"
      "s_or_b64 vcc, vcc, s[60:61]\n\t"
      "s_nop 1\n\t"
      "s_cbranch_vccz K2pbd\n\t"
      "s_sleep 2\n\t"
      "s_add_u32 s58, s58, 1\n\t"
      "s_cmp_lt_u32 s58, 0x40000\n\t"
      "s_cbranch_scc1 K2pc\n\t"
      "K2pbd:\n\t"
      "s_branch K2pe\n\t"
      "K2pc:\n\t"
      "buffer_load_dword v12, v11, s[36:39], s59 offen sc1\n\t"
      "buffer_load_dwordx2 v[4:5], v6, s[36:39], s62 offen sc1\n\t"
      "s_waitcnt vmcnt(0)\n\t"
      "v_cmp_ne_u32 vcc, s56, v12\n\t"
      "v_cmp_ne_u32 s[60:61], s56, v4\n\t"
      "s_nop 4\n\t"
      "s_or_b64 vcc, vcc, s[60:61]\n\t"
      "v_cmp_ne_u32 s[60:61], s56, v5\n\t"
      "s_nop 4\n\t"
      "s_or_b64 vcc, vcc, s[60:61]\n\t"
      "s_nop 1\n\t"
      "s_cbranch_vccz K2pfd\n\t"
      "s_sleep 2\n\t"
      "s_add_u32 s58, s58, 1\n\t"
      "s_cmp_lt_u32 s58, 0x40000\n\t"
      "s_cbranch_scc1 K2pc\n\t"
      "K2pfd:\n\t"
      "K2pe:\n\t"
      "s_add_u32 s50, s51, 0\n\t"
      "s_lshl_b32 s49, s50, 12\n\t"
      "s_add_u32 s49, s49, s53\n\t"
      "s_add_u32 s50, s50, 54\n\t"
      "s_mul_i32 s48, s50, 0x1400\n\t"
      "s_add_u32 s48, s48, s54\n\t"
      "s_waitcnt vmcnt(27)\n\t"
      "v_mfma_f32_16x16x32_bf16 a[0:3], v[16:19], v[36:39], a[0:3]\n\t"
      "v_mfma_f32_16x16x32_bf16 a[20:23], v[16:19], v[40:43], a[20:23]\n\t"
      "buffer_load_dwordx4 v[180:183], v0, s[36:39], s49 offen sc1\n\t"
      "v_mfma_f32_16x16x32_bf16 a[40:43], v[16:19], v[44:47], a[40:43]\n\t"
      "v_mfma_f32_16x16x32_bf16 a[60:63], v[16:19], v[48:51], a[60:63]\n\t"
      "buffer_load_dwordx4 v[184:187], v0, s[36:39], s49 offen offset:1024 sc1\n\t"
      "v_mfma_f32_16x16x32_bf16 a[4:7], v[20:23], v[36:39], a[4:7]\n\t"
      "v_mfma_f32_16x16x32_bf16 a[24:27], v[20:23], v[40:43], a[24:27]\n\t"
      "buffer_load_dwordx4 v[188:191], v0, s[36:39], s49 offen offset:2048 sc1\n\t"
      "v_mfma_f32_16x16x32_bf16 a[44:47], v[20:23], v[44:47], a[44:47]\n\t"
      "v_mfma_f32_16x16x32_bf16 a[64:67], v[20:23], v[48:51], a[64:67]\n\t"
      "buffer_load_dwordx4 v[192:195], v0, s[36:39], s49 offen offset:3072 sc1\n\t"
      "v_mfma_f32_16x16x32_bf16 a[8:11], v[24:27], v[36:39], a[8:11]\n\t"
      "v_mfma_f32_16x16x32_bf16 a[28:31], v[24:27], v[40:43], a[28:31]\n\t"
      "buffer_load_dwordx4 v[160:163], v0, s[36:39], s48 offen sc1\n\t"
      "v_mfma_f32_16x16x32_bf16 a[48:51], v[24:27], v[44:47], a[48:51]\n\t"
      "v_mfma_f32_16x16x32_bf16 a[68:71], v[24:27], v[48:51], a[68:71]\n\t"
      "buffer_load_dwordx4 v[164:167], v0, s[36:39], s48 offen offset:1024 sc1\n\t"
      "v_mfma_f32_16x16x32_bf16 a[12:15], v[28:31], v[36:39], a[12:15]\n\t"
      "v_mfma_f32_16x16x32_bf16 a[32:35], v[28:31], v[40:43], a[32:35]\n\t"
      "buffer_load_dwordx4 v[168:171], v0, s[36:39], s48 offen offset:2048 sc1\n\t"
      "v_mfma_f32_16x16x32_bf16 a[52:55], v[28:31], v[44:47], a[52:55]\n\t"
      "v_mfma_f32_16x16x32_bf16 a[72:75], v[28:31], v[48:51], a[72:75]\n\t"
      "buffer_load_dwordx4 v[172:175], v0, s[36:39], s48 offen offset:3072 sc1\n\t"
      "v_mfma_f32_16x16x32_bf16 a[16:19], v[32:35], v[36:39], a[16:19]\n\t"
      "v_mfma_f32_16x16x32_bf16 a[36:39], v[32:35], v[40:43], a[36:39]\n\t"
      "buffer_load_dwordx4 v[176:179], v1, s[36:39], s48 offen sc1\n\t"
      "v_mfma_f32_16x16x32_bf16 a[56:59], v[32:35], v[44:47], a[56:59]\n\t"
      "v_mfma_f32_16x16x32_bf16 a[76:79], v[32:35], v[48:51], a[76:79]\n\t"
      "s_add_u32 s50, s51, 4\n\t"
      "s_lshl_b32 s49, s50, 12\n\t"
      "s_add_u32 s49, s49, s53\n\t"
      "s_add_u32 s50, s50, 54\n\t"
      "s_mul_i32 s48, s50, 0x1400\n\t"
      "s_add_u32 s48, s48, s54\n\t"
      "s_waitcnt vmcnt(27)\n\t"
      "v_mfma_f32_16x16x32_bf16 a[0:3], v[52:55], v[72:75], a[0:3]\n\t"
      "v_mfma_f32_16x16x32_bf16 a[20:23], v[52:55], v[76:79], a[20:23]\n\t"
      "buffer_load_dwordx4 v[36:39], v0, s[36:39], s49 offen sc1\n\t"
      "v_mfma_f32_16x16x32_bf16 a[40:43], v[52:55], v[80:83], a[40:43]\n\t"
      "v_mfma_f32_16x16x32_bf16 a[60:63], v[52:55], v[84:87], a[60:63]\n\t"
      "buffer_load_dwordx4 v[40:43], v0, s[36:39], s49 offen offset:1024 sc1\n\t"
      "v_mfma_f32_16x16x32_bf16 a[4:7], v[56:59], v[72:75], a[4:7]\n\t"
      "v_mfma_f32_16x16x32_bf16 a[24:27], v[56:59], v[76:79], a[24:27]\n\t"
      "buffer_load_dwordx4 v[44:47], v0, s[36:39], s49 offen offset:2048 sc1\n\t"
      "v_mfma_f32_16x16x32_bf16 a[44:47], v[56:59], v[80:83], a[44:47]\n\t"
      "v_mfma_f32_16x16x32_bf16 a[64:67], v[56:59], v[84:87], a[64:67]\n\t"
      "buffer_load_dwordx4 v[48:51], v0, s[36:39], s49 offen offset:3072 sc1\n\t"
      "v_mfma_f32_16x16x32_bf16 a[8:11], v[60:63], v[72:75], a[8:11]\n\t"
      "v_mfma_f32_16x16x32_bf16 a[28:31], v[60:63], v[76:79], a[28:31]\n\t"
      "buffer_load_dwordx4 v[16:19], v0, s[36:39], s48 offen sc1\n\t"
      "v_mfma_f32_16x16x32_bf16 a[48:51], v[60:63], v[80:83], a[48:51]\n\t"
      "v_mfma_f32_16x16x32_bf16 a[68:71], v[60:63], v[84:87], a[68:71]\n\t"
      "buffer_load_dwordx4 v[20:23], v0, s[36:39], s48 offen offset:1024 sc1\n\t"
      "v_mfma_f32_16x16x32_bf16 a[12:15], v[64:67], v[72:75], a[12:15]\n\t"
      "v_mfma_f32_16x16x32_bf16 a[32:35], v[64:67], v[76:79], a[32:35]\n\t"
      "buffer_load_dwordx4 v[24:27], v0, s[36:39], s48 offen offset:2048 sc1\n\t"
      "v_mfma_f32_16x16x32_bf16 a[52:55], v[64:67], v[80:83], a[52:55]\n\t"
      "v_mfma_f32_16x16x32_bf16 a[72:75], v[64:67], v[84:87], a[72:75]\n\t"
      "buffer_load_dwordx4 v[28:31], v0, s[36:39], s48 offen offset:3072 sc1\n\t"
      "v_mfma_f32_16x16x32_bf16 a[16:19], v[68:71], v[72:75], a[16:19]\n\t"
      "v_mfma_f32_16x16x32_bf16 a[36:39], v[68:71], v[76:79], a[36:39]\n\t"
      "buffer_load_dwordx4 v[32:35], v1, s[36:39], s48 offen sc1\n\t"
      "v_mfma_f32_16x16x32_bf16 a[56:59], v[68:71], v[80:83], a[56:59]\n\t"
      "v_mfma_f32_16x16x32_bf16 a[76:79], v[68:71], v[84:87], a[76:79]\n\t"
      "s_add_u32 s50, s51, 8\n\t"
      "s_lshl_b32 s49, s50, 12\n\t"
      "s_add_u32 s49, s49, s53\n\t"
      "s_add_u32 s50, s50, 54\n\t"
      "s_mul_i32 s48, s50, 0x1400\n\t"
      "s_add_u32 s48, s48, s54\n\t"
      "s_waitcnt vmcnt(27)\n\t"
      "v_mfma_f32_16x16x32_bf16 a[0:3], v[88:91], v[108:111], a[0:3]\n\t"
      "v_mfma_f32_16x16x32_bf16 a[20:23], v[88:91], v[112:115], a[20:23]\n\t"
      "buffer_load_dwordx4 v[72:75], v0, s[36:39], s49 offen sc1\n\t"
      "v_mfma_f32_16x16x32_bf16 a[40:43], v[88:91], v[116:119], a[40:43]\n\t"
      "v_mfma_f32_16x16x32_bf16 a[60:63], v[88:91], v[120:123], a[60:63]\n\t"
      "buffer_load_dwordx4 v[76:79], v0, s[36:39], s49 offen offset:1024 sc1\n\t"
      "v_mfma_f32_16x16x32_bf16 a[4:7], v[92:95], v[108:111], a[4:7]\n\t"
      "v_mfma_f32_16x16x32_bf16 a[24:27], v[92:95], v[112:115], a[24:27]\n\t"
      "buffer_load_dwordx4 v[80:83], v0, s[36:39], s49 offen offset:2048 sc1\n\t"
      "v_mfma_f32_16x16x32_bf16 a[44:47], v[92:95], v[116:119], a[44:47]\n\t"
      "v_mfma_f32_16x16x32_bf16 a[64:67], v[92:95], v[120:123], a[64:67]\n\t"
      "buffer_load_dwordx4 v[84:87], v0, s[36:39], s49 offen offset:3072 sc1\n\t"
      "v_mfma_f32_16x16x32_bf16 a[8:11], v[96:99], v[108:111], a[8:11]\n\t"
      "v_mfma_f32_16x16x32_bf16 a[28:31], v[96:99], v[112:115], a[28:31]\n\t"
      "buffer_load_dwordx4 v[52:55], v0, s[36:39], s48 offen sc1\n\t"
      "v_mfma_f32_16x16x32_bf16 a[48:51], v[96:99], v[116:119], a[48:51]\n\t"
      "v_mfma_f32_16x16x32_bf16 a[68:71], v[96:99], v[120:123], a[68:71]\n\t"
      "buffer_load_dwordx4 v[56:59], v0, s[36:39], s48 offen offset:1024 sc1\n\t"
      "v_mfma_f32_16x16x32_bf16 a[12:15], v[100:103], v[108:111], a[12:15]\n\t"
      "v_mfma_f32_16x16x32_bf16 a[32:35], v[100:103], v[112:115], a[32:35]\n\t"
      "buffer_load_dwordx4 v[60:63], v0, s[36:39], s48 offen offset:2048 sc1\n\t"
      "v_mfma_f32_16x16x32_bf16 a[52:55], v[100:103], v[116:119], a[52:55]\n\t"
      "v_mfma_f32_16x16x32_bf16 a[72:75], v[100:103], v[120:123], a[72:75]\n\t"
      "buffer_load_dwordx4 v[64:67], v0, s[36:39], s48 offen offset:3072 sc1\n\t"
      "v_mfma_f32_16x16x32_bf16 a[16:19], v[104:107], v[108:111], a[16:19]\n\t"
      "v_mfma_f32_16x16x32_bf16 a[36:39], v[104:107], v[112:115], a[36:39]\n\t"
      "buffer_load_dwordx4 v[68:71], v1, s[36:39], s48 offen sc1\n\t"
      "v_mfma_f32_16x16x32_bf16 a[56:59], v[104:107], v[116:119], a[56:59]\n\t"
      "v_mfma_f32_16x16x32_bf16 a[76:79], v[104:107], v[120:123], a[76:79]\n\t"
      "s_add_u32 s50, s51, 12\n\t"
      "s_lshl_b32 s49, s50, 12\n\t"
      "s_add_u32 s49, s49, s53\n\t"
      "s_add_u32 s50, s50, 54\n\t"
      "s_mul_i32 s48, s50, 0x1400\n\t"
      "s_add_u32 s48, s48, s54\n\t"
      "s_waitcnt vmcnt(27)\n\t"
      "v_mfma_f32_16x16x32_bf16 a[0:3], v[124:127], v[144:147], a[0:3]\n\t"
      "v_mfma_f32_16x16x32_bf16 a[20:23], v[124:127], v[148:151], a[20:23]\n\t"
      "buffer_load_dwordx4 v[108:111], v0, s[36:39], s49 offen sc1\n\t"
      "v_mfma_f32_16x16x32_bf16 a[40:43], v[124:127], v[152:155], a[40:43]\n\t"
      "v_mfma_f32_16x16x32_bf16 a[60:63], v[124:127], v[156:159], a[60:63]\n\t"
      "buffer_load_dwordx4 v[112:115], v0, s[36:39], s49 offen offset:1024 sc1\n\t"
      "v_mfma_f32_16x16x32_bf16 a[4:7], v[128:131], v[144:147], a[4:7]\n\t"
      "v_mfma_f32_16x16x32_bf16 a[24:27], v[128:131], v[148:151], a[24:27]\n\t"
      "buffer_load_dwordx4 v[116:119], v0, s[36:39], s49 offen offset:2048 sc1\n\t"
      "v_mfma_f32_16x16x32_bf16 a[44:47], v[128:131], v[152:155], a[44:47]\n\t"
      "v_mfma_f32_16x16x32_bf16 a[64:67], v[128:131], v[156:159], a[64:67]\n\t"
      "buffer_load_dwordx4 v[120:123], v0, s[36:39], s49 offen offset:3072 sc1\n\t"
      "v_mfma_f32_16x16x32_bf16 a[8:11], v[132:135], v[144:147], a[8:11]\n\t"
      "v_mfma_f32_16x16x32_bf16 a[28:31], v[132:135], v[148:151], a[28:31]\n\t"
      "buffer_load_dwordx4 v[88:91], v0, s[36:39], s48 offen sc1\n\t"
      "v_mfma_f32_16x16x32_bf16 a[48:51], v[132:135], v[152:155], a[48:51]\n\t"
      "v_mfma_f32_16x16x32_bf16 a[68:71], v[132:135], v[156:159], a[68:71]\n\t"
      "buffer_load_dwordx4 v[92:95], v0, s[36:39], s48 offen offset:1024 sc1\n\t"
      "v_mfma_f32_16x16x32_bf16 a[12:15], v[136:139], v[144:147], a[12:15]\n\t"
      "v_mfma_f32_16x16x32_bf16 a[32:35], v[136:139], v[148:151], a[32:35]\n\t"
      "buffer_load_dwordx4 v[96:99], v0, s[36:39], s48 offen offset:2048 sc1\n\t"
      "v_mfma_f32_16x16x32_bf16 a[52:55], v[136:139], v[152:155], a[52:55]\n\t"
      "v_mfma_f32_16x16x32_bf16 a[72:75], v[136:139], v[156:159], a[72:75]\n\t"
      "buffer_load_dwordx4 v[100:103], v0, s[36:39], s48 offen offset:3072 sc1\n\t"
      "v_mfma_f32_16x16x32_bf16 a[16:19], v[140:143], v[144:147], a[16:19]\n\t"
      "v_mfma_f32_16x16x32_bf16 a[36:39], v[140:143], v[148:151], a[36:39]\n\t"
      "buffer_load_dwordx4 v[104:107], v1, s[36:39], s48 offen sc1\n\t"
      "v_mfma_f32_16x16x32_bf16 a[56:59], v[140:143], v[152:155], a[56:59]\n\t"
      "v_mfma_f32_16x16x32_bf16 a[76:79], v[140:143], v[156:159], a[76:79]\n\t"
      "s_add_u32 s50, s51, 16\n\t"
      "s_lshl_b32 s49, s50, 12\n\t"
      "s_add_u32 s49, s49, s53\n\t"
      "s_add_u32 s50, s50, 54\n\t"
      "s_mul_i32 s48, s50, 0x1400\n\t"
      "s_add_u32 s48, s48, s54\n\t"
      "s_waitcnt vmcnt(27)\n\t"
      "v_mfma_f32_16x16x32_bf16 a[80:83], v[160:163], v[180:183], a[80:83]\n\t"
      "v_mfma_f32_16x16x32_bf16 a[100:103], v[160:163], v[184:187], a[100:103]\n\t"
      "buffer_load_dwordx4 v[144:147], v0, s[36:39], s49 offen sc1\n\t"
      "v_mfma_f32_16x16x32_bf16 a[120:123], v[160:163], v[188:191], a[120:123]\n\t"
      "v_mfma_f32_16x16x32_bf16 a[140:143], v[160:163], v[192:195], a[140:143]\n\t"
      "buffer_load_dwordx4 v[148:151], v0, s[36:39], s49 offen offset:1024 sc1\n\t"
      "v_mfma_f32_16x16x32_bf16 a[84:87], v[164:167], v[180:183], a[84:87]\n\t"
      "v_mfma_f32_16x16x32_bf16 a[104:107], v[164:167], v[184:187], a[104:107]\n\t"
      "buffer_load_dwordx4 v[152:155], v0, s[36:39], s49 offen offset:2048 sc1\n\t"
      "v_mfma_f32_16x16x32_bf16 a[124:127], v[164:167], v[188:191], a[124:127]\n\t"
      "v_mfma_f32_16x16x32_bf16 a[144:147], v[164:167], v[192:195], a[144:147]\n\t"
      "buffer_load_dwordx4 v[156:159], v0, s[36:39], s49 offen offset:3072 sc1\n\t"
      "v_mfma_f32_16x16x32_bf16 a[88:91], v[168:171], v[180:183], a[88:91]\n\t"
      "v_mfma_f32_16x16x32_bf16 a[108:111], v[168:171], v[184:187], a[108:111]\n\t"
      "buffer_load_dwordx4 v[124:127], v0, s[36:39], s48 offen sc1\n\t"
      "v_mfma_f32_16x16x32_bf16 a[128:131], v[168:171], v[188:191], a[128:131]\n\t"
      "v_mfma_f32_16x16x32_bf16 a[148:151], v[168:171], v[192:195], a[148:151]\n\t"
      "buffer_load_dwordx4 v[128:131], v0, s[36:39], s48 offen offset:1024 sc1\n\t"
      "v_mfma_f32_16x16x32_bf16 a[92:95], v[172:175], v[180:183], a[92:95]\n\t"
      "v_mfma_f32_16x16x32_bf16 a[112:115], v[172:175], v[184:187], a[112:115]\n\t"
      "buffer_load_dwordx4 v[132:135], v0, s[36:39], s48 offen offset:2048 sc1\n\t"
      "v_mfma_f32_16x16x32_bf16 a[132:135], v[172:175], v[188:191], a[132:135]\n\t"
      "v_mfma_f32_16x16x32_bf16 a[152:155], v[172:175], v[192:195], a[152:155]\n\t"
      "buffer_load_dwordx4 v[136:139], v0, s[36:39], s48 offen offset:3072 sc1\n\t"
      "v_mfma_f32_16x16x32_bf16 a[96:99], v[176:179], v[180:183], a[96:99]\n\t"
      "v_mfma_f32_16x16x32_bf16 a[116:119], v[176:179], v[184:187], a[116:119]\n\t"
      "buffer_load_dwordx4 v[140:143], v1, s[36:39], s48 offen sc1\n\t"
      "v_mfma_f32_16x16x32_bf16 a[136:139], v[176:179], v[188:191], a[136:139]\n\t"
      "v_mfma_f32_16x16x32_bf16 a[156:159], v[176:179], v[192:195], a[156:159]\n\t"
      "s_add_u32 s50, s51, 20\n\t"
      "s_lshl_b32 s49, s50, 12\n\t"
      "s_add_u32 s49, s49, s53\n\t"
      "s_add_u32 s50, s50, 54\n\t"
      "s_mul_i32 s48, s50, 0x1400\n\t"
      "s_add_u32 s48, s48, s54\n\t"
      "s_waitcnt vmcnt(27)\n\t"
      "v_mfma_f32_16x16x32_bf16 a[80:83], v[16:19], v[36:39], a[80:83]\n\t"
      "ds_write_b128 v8, a[0:3]\n\t"
      "v_mfma_f32_16x16x32_bf16 a[100:103], v[16:19], v[40:43], a[100:103]\n\t"
      "buffer_load_dwordx4 v[180:183], v0, s[36:39], s49 offen sc1\n\t"
      "v_mfma_f32_16x16x32_bf16 a[120:123], v[16:19], v[44:47], a[120:123]\n\t"
      "ds_write_b128 v8, a[4:7] offset:1024\n\t"
      "v_mfma_f32_16x16x32_bf16 a[140:143], v[16:19], v[48:51], a[140:143]\n\t"
      "buffer_load_dwordx4 v[184:187], v0, s[36:39], s49 offen offset:1024 sc1\n\t"
      "v_mfma_f32_16x16x32_bf16 a[84:87], v[20:23], v[36:39], a[84:87]\n\t"
      "ds_write_b128 v8, a[8:11] offset:2048\n\t"
      "v_mfma_f32_16x16x32_bf16 a[104:107], v[20:23], v[40:43], a[104:107]\n\t"
      "buffer_load_dwordx4 v[188:191], v0, s[36:39], s49 offen offset:2048 sc1\n\t"
      "v_mfma_f32_16x16x32_bf16 a[124:127], v[20:23], v[44:47], a[124:127]\n\t"
      "ds_write_b128 v8, a[12:15] offset:3072\n\t"
      "v_mfma_f32_16x16x32_bf16 a[144:147], v[20:23], v[48:51], a[144:147]\n\t"
      "buffer_load_dwordx4 v[192:195], v0, s[36:39], s49 offen offset:3072 sc1\n\t"
      "v_mfma_f32_16x16x32_bf16 a[88:91], v[24:27], v[36:39], a[88:91]\n\t"
      "ds_write_b128 v8, a[16:19] offset:4096\n\t"
      "ds_write_b128 v8, a[20:23] offset:20480\n\t"
      "v_mfma_f32_16x16x32_bf16 a[108:111], v[24:27], v[40:43], a[108:111]\n\t"
      "buffer_load_dwordx4 v[160:163], v0, s[36:39], s48 offen sc1\n\t"
      "v_mfma_f32_16x16x32_bf16 a[128:131], v[24:27], v[44:47], a[128:131]\n\t"
      "ds_write_b128 v8, a[24:27] offset:21504\n\t"
      "ds_write_b128 v8, a[28:31] offset:22528\n\t"
      "v_mfma_f32_16x16x32_bf16 a[148:151], v[24:27], v[48:51], a[148:151]\n\t"
      "buffer_load_dwordx4 v[164:167], v0, s[36:39], s48 offen offset:1024 sc1\n\t"
      "v_mfma_f32_16x16x32_bf16 a[92:95], v[28:31], v[36:39], a[92:95]\n\t"
      "ds_write_b128 v8, a[32:35] offset:23552\n\t"
      "ds_write_b128 v8, a[36:39] offset:24576\n\t"
      "v_mfma_f32_16x16x32_bf16 a[112:115], v[28:31], v[40:43], a[112:115]\n\t"
      "buffer_load_dwordx4 v[168:171], v0, s[36:39], s48 offen offset:2048 sc1\n\t"
      "v_mfma_f32_16x16x32_bf16 a[132:135], v[28:31], v[44:47], a[132:135]\n\t"
      "ds_write_b128 v14, a[40:43]\n\t"
      "ds_write_b128 v14, a[44:47] offset:1024\n\t"
      "v_mfma_f32_16x16x32_bf16 a[152:155], v[28:31], v[48:51], a[152:155]\n\t"
      "buffer_load_dwordx4 v[172:175], v0, s[36:39], s48 offen offset:3072 sc1\n\t"
      "v_mfma_f32_16x16x32_bf16 a[96:99], v[32:35], v[36:39], a[96:99]\n\t"
      "ds_write_b128 v14, a[48:51] offset:2048\n\t"
      "ds_write_b128 v14, a[52:55] offset:3072\n\t"
      "v_mfma_f32_16x16x32_bf16 a[116:119], v[32:35], v[40:43], a[116:119]\n\t"
      "buffer_load_dwordx4 v[176:179], v1, s[36:39], s48 offen sc1\n\t"
      "v_mfma_f32_16x16x32_bf16 a[136:139], v[32:35], v[44:47], a[136:139]\n\t"
      "ds_write_b128 v14, a[56:59] offset:4096\n\t"
      "ds_write_b128 v14, a[60:63] offset:20480\n\t"
      "v_mfma_f32_16x16x32_bf16 a[156:159], v[32:35], v[48:51], a[156:159]\n\t"
      "ds_write_b128 v14, a[64:67] offset:21504\n\t"
      "ds_write_b128 v14, a[68:71] offset:22528\n\t"
      "ds_write_b128 v14, a[72:75] offset:23552\n\t"
      "ds_write_b128 v14, a[76:79] offset:24576\n\t"
      "s_add_u32 s50, s51, 24\n\t"
      "s_lshl_b32 s49, s50, 12\n\t"
      "s_add_u32 s49, s49, s53\n\t"
      "s_add_u32 s50, s50, 54\n\t"
      "s_mul_i32 s48, s50, 0x1400\n\t"
      "s_add_u32 s48, s48, s54\n\t"
      "s_waitcnt vmcnt(27)\n\t"
      "v_mfma_f32_16x16x32_bf16 a[80:83], v[52:55], v[72:75], a[80:83]\n\t"
      "v_mfma_f32_16x16x32_bf16 a[100:103], v[52:55], v[76:79], a[100:103]\n\t"
      "buffer_load_dwordx4 v[36:39], v0, s[36:39], s49 offen sc1\n\t"
      "v_mfma_f32_16x16x32_bf16 a[120:123], v[52:55], v[80:83], a[120:123]\n\t"
      "v_mfma_f32_16x16x32_bf16 a[140:143], v[52:55], v[84:87], a[140:143]\n\t"
      "buffer_load_dwordx4 v[40:43], v0, s[36:39], s49 offen offset:1024 sc1\n\t"
      "v_mfma_f32_16x16x32_bf16 a[84:87], v[56:59], v[72:75], a[84:87]\n\t"
      "v_mfma_f32_16x16x32_bf16 a[104:107], v[56:59], v[76:79], a[104:107]\n\t"
      "buffer_load_dwordx4 v[44:47], v0, s[36:39], s49 offen offset:2048 sc1\n\t"
      "v_mfma_f32_16x16x32_bf16 a[124:127], v[56:59], v[80:83], a[124:127]\n\t"
      "v_mfma_f32_16x16x32_bf16 a[144:147], v[56:59], v[84:87], a[144:147]\n\t"
      "buffer_load_dwordx4 v[48:51], v0, s[36:39], s49 offen offset:3072 sc1\n\t"
      "v_mfma_f32_16x16x32_bf16 a[88:91], v[60:63], v[72:75], a[88:91]\n\t"
      "v_mfma_f32_16x16x32_bf16 a[108:111], v[60:63], v[76:79], a[108:111]\n\t"
      "buffer_load_dwordx4 v[16:19], v0, s[36:39], s48 offen sc1\n\t"
      "v_mfma_f32_16x16x32_bf16 a[128:131], v[60:63], v[80:83], a[128:131]\n\t"
      "v_mfma_f32_16x16x32_bf16 a[148:151], v[60:63], v[84:87], a[148:151]\n\t"
      "buffer_load_dwordx4 v[20:23], v0, s[36:39], s48 offen offset:1024 sc1\n\t"
      "v_mfma_f32_16x16x32_bf16 a[92:95], v[64:67], v[72:75], a[92:95]\n\t"
      "v_mfma_f32_16x16x32_bf16 a[112:115], v[64:67], v[76:79], a[112:115]\n\t"
      "buffer_load_dwordx4 v[24:27], v0, s[36:39], s48 offen offset:2048 sc1\n\t"
      "v_mfma_f32_16x16x32_bf16 a[132:135], v[64:67], v[80:83], a[132:135]\n\t"
      "v_mfma_f32_16x16x32_bf16 a[152:155], v[64:67], v[84:87], a[152:155]\n\t"
      "buffer_load_dwordx4 v[28:31], v0, s[36:39], s48 offen offset:3072 sc1\n\t"
      "v_mfma_f32_16x16x32_bf16 a[96:99], v[68:71], v[72:75], a[96:99]\n\t"
      "v_mfma_f32_16x16x32_bf16 a[116:119], v[68:71], v[76:79], a[116:119]\n\t"
      "buffer_load_dwordx4 v[32:35], v1, s[36:39], s48 offen sc1\n\t"
      "v_mfma_f32_16x16x32_bf16 a[136:139], v[68:71], v[80:83], a[136:139]\n\t"
      "v_mfma_f32_16x16x32_bf16 a[156:159], v[68:71], v[84:87], a[156:159]\n\t"
      "s_add_u32 s50, s51, 28\n\t"
      "s_lshl_b32 s49, s50, 12\n\t"
      "s_add_u32 s49, s49, s53\n\t"
      "s_add_u32 s50, s50, 54\n\t"
      "s_mul_i32 s48, s50, 0x1400\n\t"
      "s_add_u32 s48, s48, s54\n\t"
      "s_waitcnt vmcnt(27)\n\t"
      "v_mfma_f32_16x16x32_bf16 a[80:83], v[88:91], v[108:111], a[80:83]\n\t"
      "v_mfma_f32_16x16x32_bf16 a[100:103], v[88:91], v[112:115], a[100:103]\n\t"
      "buffer_load_dwordx4 v[72:75], v0, s[36:39], s49 offen sc1\n\t"
      "v_mfma_f32_16x16x32_bf16 a[120:123], v[88:91], v[116:119], a[120:123]\n\t"
      "v_mfma_f32_16x16x32_bf16 a[140:143], v[88:91], v[120:123], a[140:143]\n\t"
      "buffer_load_dwordx4 v[76:79], v0, s[36:39], s49 offen offset:1024 sc1\n\t"
      "v_mfma_f32_16x16x32_bf16 a[84:87], v[92:95], v[108:111], a[84:87]\n\t"
      "v_mfma_f32_16x16x32_bf16 a[104:107], v[92:95], v[112:115], a[104:107]\n\t"
      "buffer_load_dwordx4 v[80:83], v0, s[36:39], s49 offen offset:2048 sc1\n\t"
      "v_mfma_f32_16x16x32_bf16 a[124:127], v[92:95], v[116:119], a[124:127]\n\t"
      "v_mfma_f32_16x16x32_bf16 a[144:147], v[92:95], v[120:123], a[144:147]\n\t"
      "buffer_load_dwordx4 v[84:87], v0, s[36:39], s49 offen offset:3072 sc1\n\t"
      "v_mfma_f32_16x16x32_bf16 a[88:91], v[96:99], v[108:111], a[88:91]\n\t"
      "v_mfma_f32_16x16x32_bf16 a[108:111], v[96:99], v[112:115], a[108:111]\n\t"
      "buffer_load_dwordx4 v[52:55], v0, s[36:39], s48 offen sc1\n\t"
      "v_mfma_f32_16x16x32_bf16 a[128:131], v[96:99], v[116:119], a[128:131]\n\t"
      "v_mfma_f32_16x16x32_bf16 a[148:151], v[96:99], v[120:123], a[148:151]\n\t"
      "buffer_load_dwordx4 v[56:59], v0, s[36:39], s48 offen offset:1024 sc1\n\t"
      "v_mfma_f32_16x16x32_bf16 a[92:95], v[100:103], v[108:111], a[92:95]\n\t"
      "v_mfma_f32_16x16x32_bf16 a[112:115], v[100:103], v[112:115], a[112:115]\n\t"
      "buffer_load_dwordx4 v[60:63], v0, s[36:39], s48 offen offset:2048 sc1\n\t"
      "v_mfma_f32_16x16x32_bf16 a[132:135], v[100:103], v[116:119], a[132:135]\n\t"
      "v_mfma_f32_16x16x32_bf16 a[152:155], v[100:103], v[120:123], a[152:155]\n\t"
      "buffer_load_dwordx4 v[64:67], v0, s[36:39], s48 offen offset:3072 sc1\n\t"
      "v_mfma_f32_16x16x32_bf16 a[96:99], v[104:107], v[108:111], a[96:99]\n\t"
      "v_mfma_f32_16x16x32_bf16 a[116:119], v[104:107], v[112:115], a[116:119]\n\t"
      "buffer_load_dwordx4 v[68:71], v1, s[36:39], s48 offen sc1\n\t"
      "v_mfma_f32_16x16x32_bf16 a[136:139], v[104:107], v[116:119], a[136:139]\n\t"
      "v_mfma_f32_16x16x32_bf16 a[156:159], v[104:107], v[120:123], a[156:159]\n\t"
      "s_add_u32 s50, s51, 32\n\t"
      "s_lshl_b32 s49, s50, 12\n\t"
      "s_add_u32 s49, s49, s53\n\t"
      "s_add_u32 s50, s50, 54\n\t"
      "s_mul_i32 s48, s50, 0x1400\n\t"
      "s_add_u32 s48, s48, s54\n\t"
      "s_waitcnt vmcnt(27)\n\t"
      "v_mfma_f32_16x16x32_bf16 a[80:83], v[124:127], v[144:147], a[80:83]\n\t"
      "s_waitcnt lgkmcnt(0)\n\t"
      "v_mfma_f32_16x16x32_bf16 a[100:103], v[124:127], v[148:151], a[100:103]\n\t"
      "buffer_load_dwordx4 v[108:111], v0, s[36:39], s49 offen sc1\n\t"
      "v_mfma_f32_16x16x32_bf16 a[120:123], v[124:127], v[152:155], a[120:123]\n\t"
      "s_barrier\n\t"
      "v_mfma_f32_16x16x32_bf16 a[140:143], v[124:127], v[156:159], a[140:143]\n\t"
      "buffer_load_dwordx4 v[112:115], v0, s[36:39], s49 offen offset:1024 sc1\n\t"
      "v_mfma_f32_16x16x32_bf16 a[84:87], v[128:131], v[144:147], a[84:87]\n\t"
      "ds_read_b128 v[196:199], v9\n\t"
      "v_mfma_f32_16x16x32_bf16 a[104:107], v[128:131], v[148:151], a[104:107]\n\t"
      "buffer_load_dwordx4 v[116:119], v0, s[36:39], s49 offen offset:2048 sc1\n\t"
      "v_mfma_f32_16x16x32_bf16 a[124:127], v[128:131], v[152:155], a[124:127]\n\t"
      "ds_read_b128 v[200:203], v9 offset:1024\n\t"
      "v_mfma_f32_16x16x32_bf16 a[144:147], v[128:131], v[156:159], a[144:147]\n\t"
      "buffer_load_dwordx4 v[120:123], v0, s[36:39], s49 offen offset:3072 sc1\n\t"
      "v_mfma_f32_16x16x32_bf16 a[88:91], v[132:135], v[144:147], a[88:91]\n\t"
      "ds_read_b128 v[204:207], v9 offset:2048\n\t"
      "ds_read_b128 v[208:211], v9 offset:3072\n\t"
      "v_mfma_f32_16x16x32_bf16 a[108:111], v[132:135], v[148:151], a[108:111]\n\t"
      "buffer_load_dwordx4 v[88:91], v0, s[36:39], s48 offen sc1\n\t"
      "v_mfma_f32_16x16x32_bf16 a[128:131], v[132:135], v[152:155], a[128:131]\n\t"
      "ds_read_b128 v[212:215], v9 offset:4096\n\t"
      "v_mfma_f32_16x16x32_bf16 a[148:151], v[132:135], v[156:159], a[148:151]\n\t"
      "buffer_load_dwordx4 v[92:95], v0, s[36:39], s48 offen offset:1024 sc1\n\t"
      "v_mfma_f32_16x16x32_bf16 a[92:95], v[136:139], v[144:147], a[92:95]\n\t"
      "v_mfma_f32_16x16x32_bf16 a[112:115], v[136:139], v[148:151], a[112:115]\n\t"
      "buffer_load_dwordx4 v[96:99], v0, s[36:39], s48 offen offset:2048 sc1\n\t"
      "v_mfma_f32_16x16x32_bf16 a[132:135], v[136:139], v[152:155], a[132:135]\n\t"
      "v_mfma_f32_16x16x32_bf16 a[152:155], v[136:139], v[156:159], a[152:155]\n\t"
      "buffer_load_dwordx4 v[100:103], v0, s[36:39], s48 offen offset:3072 sc1\n\t"
      "v_mfma_f32_16x16x32_bf16 a[96:99], v[140:143], v[144:147], a[96:99]\n\t"
      "v_mfma_f32_16x16x32_bf16 a[116:119], v[140:143], v[148:151], a[116:119]\n\t"
      "buffer_load_dwordx4 v[104:107], v1, s[36:39], s48 offen sc1\n\t"
      "v_mfma_f32_16x16x32_bf16 a[136:139], v[140:143], v[152:155], a[136:139]\n\t"
      "v_mfma_f32_16x16x32_bf16 a[156:159], v[140:143], v[156:159], a[156:159]\n\t"
      "s_add_u32 s50, s51, 36\n\t"
      "s_lshl_b32 s49, s50, 12\n\t"
      "s_add_u32 s49, s49, s53\n\t"
      "s_add_u32 s50, s50, 54\n\t"
      "s_mul_i32 s48, s50, 0x1400\n\t"
      "s_add_u32 s48, s48, s54\n\t"
      "s_waitcnt vmcnt(27)\n\t"
      "v_mfma_f32_16x16x32_bf16 a[80:83], v[160:163], v[180:183], a[80:83]\n\t"
      "ds_read_b128 v[216:219], v9 offset:5120\n\t"
      "v_mfma_f32_16x16x32_bf16 a[100:103], v[160:163], v[184:187], a[100:103]\n\t"
      "buffer_load_dwordx4 v[144:147], v0, s[36:39], s49 offen sc1\n\t"
      "v_mfma_f32_16x16x32_bf16 a[120:123], v[160:163], v[188:191], a[120:123]\n\t"
      "ds_read_b128 v[220:223], v9 offset:6144\n\t"
      "v_mfma_f32_16x16x32_bf16 a[140:143], v[160:163], v[192:195], a[140:143]\n\t"
      "buffer_load_dwordx4 v[148:151], v0, s[36:39], s49 offen offset:1024 sc1\n\t"
      "v_mfma_f32_16x16x32_bf16 a[84:87], v[164:167], v[180:183], a[84:87]\n\t"
      "ds_read_b128 v[224:227], v9 offset:7168\n\t"
      "v_mfma_f32_16x16x32_bf16 a[104:107], v[164:167], v[184:187], a[104:107]\n\t"
      "buffer_load_dwordx4 v[152:155], v0, s[36:39], s49 offen offset:2048 sc1\n\t"
      "v_mfma_f32_16x16x32_bf16 a[124:127], v[164:167], v[188:191], a[124:127]\n\t"
      "ds_read_b128 v[228:231], v9 offset:8192\n\t"
      "v_mfma_f32_16x16x32_bf16 a[144:147], v[164:167], v[192:195], a[144:147]\n\t"
      "buffer_load_dwordx4 v[156:159], v0, s[36:39], s49 offen offset:3072 sc1\n\t"
      "v_mfma_f32_16x16x32_bf16 a[88:91], v[168:171], v[180:183], a[88:91]\n\t"
      "ds_read_b128 v[232:235], v9 offset:9216\n\t"
      "v_mfma_f32_16x16x32_bf16 a[108:111], v[168:171], v[184:187], a[108:111]\n\t"
      "buffer_load_dwordx4 v[124:127], v0, s[36:39], s48 offen sc1\n\t"
      "v_mfma_f32_16x16x32_bf16 a[128:131], v[168:171], v[188:191], a[128:131]\n\t"
      "v_mfma_f32_16x16x32_bf16 a[148:151], v[168:171], v[192:195], a[148:151]\n\t"
      "buffer_load_dwordx4 v[128:131], v0, s[36:39], s48 offen offset:1024 sc1\n\t"
      "v_mfma_f32_16x16x32_bf16 a[92:95], v[172:175], v[180:183], a[92:95]\n\t"
      "v_mfma_f32_16x16x32_bf16 a[112:115], v[172:175], v[184:187], a[112:115]\n\t"
      "buffer_load_dwordx4 v[132:135], v0, s[36:39], s48 offen offset:2048 sc1\n\t"
      "v_mfma_f32_16x16x32_bf16 a[132:135], v[172:175], v[188:191], a[132:135]\n\t"
      "v_mfma_f32_16x16x32_bf16 a[152:155], v[172:175], v[192:195], a[152:155]\n\t"
      "buffer_load_dwordx4 v[136:139], v0, s[36:39], s48 offen offset:3072 sc1\n\t"
      "v_mfma_f32_16x16x32_bf16 a[96:99], v[176:179], v[180:183], a[96:99]\n\t"
      "v_mfma_f32_16x16x32_bf16 a[116:119], v[176:179], v[184:187], a[116:119]\n\t"
      "buffer_load_dwordx4 v[140:143], v1, s[36:39], s48 offen sc1\n\t"
      "v_mfma_f32_16x16x32_bf16 a[136:139], v[176:179], v[188:191], a[136:139]\n\t"
      "v_mfma_f32_16x16x32_bf16 a[156:159], v[176:179], v[192:195], a[156:159]\n\t"
      "s_add_u32 s50, s51, 40\n\t"
      "s_lshl_b32 s49, s50, 12\n\t"
      "s_add_u32 s49, s49, s53\n\t"
      "s_add_u32 s50, s50, 54\n\t"
      "s_mul_i32 s48, s50, 0x1400\n\t"
      "s_add_u32 s48, s48, s54\n\t"
      "s_waitcnt vmcnt(27)\n\t"
      "v_mfma_f32_16x16x32_bf16 a[80:83], v[16:19], v[36:39], a[80:83]\n\t"
      "s_waitcnt lgkmcnt(0)\n\t"
      "v_mfma_f32_16x16x32_bf16 a[100:103], v[16:19], v[40:43], a[100:103]\n\t"
      "buffer_load_dwordx4 v[180:183], v0, s[36:39], s49 offen sc1\n\t"
      "v_mfma_f32_16x16x32_bf16 a[120:123], v[16:19], v[44:47], a[120:123]\n\t"
      "v_pk_add_f32 v[196:197], v[196:197], v[216:217]\n\t"
      "v_mfma_f32_16x16x32_bf16 a[140:143], v[16:19], v[48:51], a[140:143]\n\t"
      "buffer_load_dwordx4 v[184:187], v0, s[36:39], s49 offen offset:1024 sc1\n\t"
      "v_mfma_f32_16x16x32_bf16 a[84:87], v[20:23], v[36:39], a[84:87]\n\t"
      "v_pk_add_f32 v[198:199], v[198:199], v[218:219]\n\t"
      "v_mfma_f32_16x16x32_bf16 a[104:107], v[20:23], v[40:43], a[104:107]\n\t"
      "buffer_load_dwordx4 v[188:191], v0, s[36:39], s49 offen offset:2048 sc1\n\t"
      "v_mfma_f32_16x16x32_bf16 a[124:127], v[20:23], v[44:47], a[124:127]\n\t"
      "v_pk_add_f32 v[200:201], v[200:201], v[220:221]\n\t"
      "v_mfma_f32_16x16x32_bf16 a[144:147], v[20:23], v[48:51], a[144:147]\n\t"
      "buffer_load_dwordx4 v[192:195], v0, s[36:39], s49 offen offset:3072 sc1\n\t"
      "v_mfma_f32_16x16x32_bf16 a[88:91], v[24:27], v[36:39], a[88:91]\n\t"
      "v_pk_add_f32 v[202:203], v[202:203], v[222:223]\n\t"
      "v_pk_add_f32 v[204:205], v[204:205], v[224:225]\n\t"
      "v_mfma_f32_16x16x32_bf16 a[108:111], v[24:27], v[40:43], a[108:111]\n\t"
      "buffer_load_dwordx4 v[160:163], v0, s[36:39], s48 offen sc1\n\t"
      "v_mfma_f32_16x16x32_bf16 a[128:131], v[24:27], v[44:47], a[128:131]\n\t"
      "v_pk_add_f32 v[206:207], v[206:207], v[226:227]\n\t"
      "v_pk_add_f32 v[208:209], v[208:209], v[228:229]\n\t"
      "v_mfma_f32_16x16x32_bf16 a[148:151], v[24:27], v[48:51], a[148:151]\n\t"
      "buffer_load_dwordx4 v[164:167], v0, s[36:39], s48 offen offset:1024 sc1\n\t"
      "v_mfma_f32_16x16x32_bf16 a[92:95], v[28:31], v[36:39], a[92:95]\n\t"
      "v_pk_add_f32 v[210:211], v[210:211], v[230:231]\n\t"
      "v_pk_add_f32 v[212:213], v[212:213], v[232:233]\n\t"
      "v_mfma_f32_16x16x32_bf16 a[112:115], v[28:31], v[40:43], a[112:115]\n\t"
      "buffer_load_dwordx4 v[168:171], v0, s[36:39], s48 offen offset:2048 sc1\n\t"
      "v_mfma_f32_16x16x32_bf16 a[132:135], v[28:31], v[44:47], a[132:135]\n\t"
      "v_pk_add_f32 v[214:215], v[214:215], v[234:235]\n\t"
      "v_mfma_f32_16x16x32_bf16 a[152:155], v[28:31], v[48:51], a[152:155]\n\t"
      "buffer_load_dwordx4 v[172:175], v0, s[36:39], s48 offen offset:3072 sc1\n\t"
      "v_mfma_f32_16x16x32_bf16 a[96:99], v[32:35], v[36:39], a[96:99]\n\t"
      "v_mfma_f32_16x16x32_bf16 a[116:119], v[32:35], v[40:43], a[116:119]\n\t"
      "buffer_load_dwordx4 v[176:179], v1, s[36:39], s48 offen sc1\n\t"
      "v_mfma_f32_16x16x32_bf16 a[136:139], v[32:35], v[44:47], a[136:139]\n\t"
      "v_mfma_f32_16x16x32_bf16 a[156:159], v[32:35], v[48:51], a[156:159]\n\t"
      "s_add_u32 s50, s51, 44\n\t"
      "s_lshl_b32 s49, s50, 12\n\t"
      "s_add_u32 s49, s49, s53\n\t"
      "s_add_u32 s50, s50, 54\n\t"
      "s_mul_i32 s48, s50, 0x1400\n\t"
      "s_add_u32 s48, s48, s54\n\t"
      "s_waitcnt vmcnt(27)\n\t"
      "v_mfma_f32_16x16x32_bf16 a[80:83], v[52:55], v[72:75], a[80:83]\n\t"
      "ds_read_b128 v[216:219], v9 offset:10240\n\t"
      "v_mfma_f32_16x16x32_bf16 a[100:103], v[52:55], v[76:79], a[100:103]\n\t"
      "buffer_load_dwordx4 v[36:39], v0, s[36:39], s49 offen sc1\n\t"
      "v_mfma_f32_16x16x32_bf16 a[120:123], v[52:55], v[80:83], a[120:123]\n\t"
      "ds_read_b128 v[220:223], v9 offset:11264\n\t"
      "v_mfma_f32_16x16x32_bf16 a[140:143], v[52:55], v[84:87], a[140:143]\n\t"
      "buffer_load_dwordx4 v[40:43], v0, s[36:39], s49 offen offset:1024 sc1\n\t"
      "v_mfma_f32_16x16x32_bf16 a[84:87], v[56:59], v[72:75], a[84:87]\n\t"
      "ds_read_b128 v[224:227], v9 offset:12288\n\t"
      "v_mfma_f32_16x16x32_bf16 a[104:107], v[56:59], v[76:79], a[104:107]\n\t"
      "buffer_load_dwordx4 v[44:47], v0, s[36:39], s49 offen offset:2048 sc1\n\t"
      "v_mfma_f32_16x16x32_bf16 a[124:127], v[56:59], v[80:83], a[124:127]\n\t"
      "ds_read_b128 v[228:231], v9 offset:13312\n\t"
      "v_mfma_f32_16x16x32_bf16 a[144:147], v[56:59], v[84:87], a[144:147]\n\t"
      "buffer_load_dwordx4 v[48:51], v0, s[36:39], s49 offen offset:3072 sc1\n\t"
      "v_mfma_f32_16x16x32_bf16 a[88:91], v[60:63], v[72:75], a[88:91]\n\t"
      "ds_read_b128 v[232:235], v9 offset:14336\n\t"
      "v_mfma_f32_16x16x32_bf16 a[108:111], v[60:63], v[76:79], a[108:111]\n\t"
      "buffer_load_dwordx4 v[16:19], v0, s[36:39], s48 offen sc1\n\t"
      "v_mfma_f32_16x16x32_bf16 a[128:131], v[60:63], v[80:83], a[128:131]\n\t"
      "v_mfma_f32_16x16x32_bf16 a[148:151], v[60:63], v[84:87], a[148:151]\n\t"
      "buffer_load_dwordx4 v[20:23], v0, s[36:39], s48 offen offset:1024 sc1\n\t"
      "v_mfma_f32_16x16x32_bf16 a[92:95], v[64:67], v[72:75], a[92:95]\n\t"
      "v_mfma_f32_16x16x32_bf16 a[112:115], v[64:67], v[76:79], a[112:115]\n\t"
      "buffer_load_dwordx4 v[24:27], v0, s[36:39], s48 offen offset:2048 sc1\n\t"
      "v_mfma_f32_16x16x32_bf16 a[132:135], v[64:67], v[80:83], a[132:135]\n\t"
      "v_mfma_f32_16x16x32_bf16 a[152:155], v[64:67], v[84:87], a[152:155]\n\t"
      "buffer_load_dwordx4 v[28:31], v0, s[36:39], s48 offen offset:3072 sc1\n\t"
      "v_mfma_f32_16x16x32_bf16 a[96:99], v[68:71], v[72:75], a[96:99]\n\t"
      "v_mfma_f32_16x16x32_bf16 a[116:119], v[68:71], v[76:79], a[116:119]\n\t"
      "buffer_load_dwordx4 v[32:35], v1, s[36:39], s48 offen sc1\n\t"
      "v_mfma_f32_16x16x32_bf16 a[136:139], v[68:71], v[80:83], a[136:139]\n\t"
      "v_mfma_f32_16x16x32_bf16 a[156:159], v[68:71], v[84:87], a[156:159]\n\t"
      "s_add_u32 s50, s51, 48\n\t"
      "s_lshl_b32 s49, s50, 12\n\t"
      "s_add_u32 s49, s49, s53\n\t"
      "s_add_u32 s50, s50, 54\n\t"
      "s_mul_i32 s48, s50, 0x1400\n\t"
      "s_add_u32 s48, s48, s54\n\t"
      "s_waitcnt vmcnt(27)\n\t"
      "v_mfma_f32_16x16x32_bf16 a[80:83], v[88:91], v[108:111], a[80:83]\n\t"
      "s_waitcnt lgkmcnt(0)\n\t"
      "v_mfma_f32_16x16x32_bf16 a[100:103], v[88:91], v[112:115], a[100:103]\n\t"
      "buffer_load_dwordx4 v[72:75], v0, s[36:39], s49 offen sc1\n\t"
      "v_mfma_f32_16x16x32_bf16 a[120:123], v[88:91], v[116:119], a[120:123]\n\t"
      "v_pk_add_f32 v[196:197], v[196:197], v[216:217]\n\t"
      "v_mfma_f32_16x16x32_bf16 a[140:143], v[88:91], v[120:123], a[140:143]\n\t"
      "buffer_load_dwordx4 v[76:79], v0, s[36:39], s49 offen offset:1024 sc1\n\t"
      "v_mfma_f32_16x16x32_bf16 a[84:87], v[92:95], v[108:111], a[84:87]\n\t"
      "v_pk_add_f32 v[198:199], v[198:199], v[218:219]\n\t"
      "v_mfma_f32_16x16x32_bf16 a[104:107], v[92:95], v[112:115], a[104:107]\n\t"
      "buffer_load_dwordx4 v[80:83], v0, s[36:39], s49 offen offset:2048 sc1\n\t"
      "v_mfma_f32_16x16x32_bf16 a[124:127], v[92:95], v[116:119], a[124:127]\n\t"
      "v_pk_add_f32 v[200:201], v[200:201], v[220:221]\n\t"
      "v_mfma_f32_16x16x32_bf16 a[144:147], v[92:95], v[120:123], a[144:147]\n\t"
      "buffer_load_dwordx4 v[84:87], v0, s[36:39], s49 offen offset:3072 sc1\n\t"
      "v_mfma_f32_16x16x32_bf16 a[88:91], v[96:99], v[108:111], a[88:91]\n\t"
      "v_pk_add_f32 v[202:203], v[202:203], v[222:223]\n\t"
      "v_pk_add_f32 v[204:205], v[204:205], v[224:225]\n\t"
      "v_mfma_f32_16x16x32_bf16 a[108:111], v[96:99], v[112:115], a[108:111]\n\t"
      "buffer_load_dwordx4 v[52:55], v0, s[36:39], s48 offen sc1\n\t"
      "v_mfma_f32_16x16x32_bf16 a[128:131], v[96:99], v[116:119], a[128:131]\n\t"
      "v_pk_add_f32 v[206:207], v[206:207], v[226:227]\n\t"
      "v_pk_add_f32 v[208:209], v[208:209], v[228:229]\n\t"
      "v_mfma_f32_16x16x32_bf16 a[148:151], v[96:99], v[120:123], a[148:151]\n\t"
      "buffer_load_dwordx4 v[56:59], v0, s[36:39], s48 offen offset:1024 sc1\n\t"
      "v_mfma_f32_16x16x32_bf16 a[92:95], v[100:103], v[108:111], a[92:95]\n\t"
      "v_pk_add_f32 v[210:211], v[210:211], v[230:231]\n\t"
      "v_pk_add_f32 v[212:213], v[212:213], v[232:233]\n\t"
      "v_mfma_f32_16x16x32_bf16 a[112:115], v[100:103], v[112:115], a[112:115]\n\t"
      "buffer_load_dwordx4 v[60:63], v0, s[36:39], s48 offen offset:2048 sc1\n\t"
      "v_mfma_f32_16x16x32_bf16 a[132:135], v[100:103], v[116:119], a[132:135]\n\t"
      "v_pk_add_f32 v[214:215], v[214:215], v[234:235]\n\t"
      "v_mfma_f32_16x16x32_bf16 a[152:155], v[100:103], v[120:123], a[152:155]\n\t"
      "buffer_load_dwordx4 v[64:67], v0, s[36:39], s48 offen offset:3072 sc1\n\t"
      "v_mfma_f32_16x16x32_bf16 a[96:99], v[104:107], v[108:111], a[96:99]\n\t"
      "v_mfma_f32_16x16x32_bf16 a[116:119], v[104:107], v[112:115], a[116:119]\n\t"
      "buffer_load_dwordx4 v[68:71], v1, s[36:39], s48 offen sc1\n\t"
      "v_mfma_f32_16x16x32_bf16 a[136:139], v[104:107], v[116:119], a[136:139]\n\t"
      "v_mfma_f32_16x16x32_bf16 a[156:159], v[104:107], v[120:123], a[156:159]\n\t"
      "s_add_u32 s50, s51, 52\n\t"
      "s_lshl_b32 s49, s50, 12\n\t"
      "s_add_u32 s49, s49, s53\n\t"
      "s_add_u32 s50, s50, 54\n\t"
      "s_mul_i32 s48, s50, 0x1400\n\t"
      "s_add_u32 s48, s48, s54\n\t"
      "s_waitcnt vmcnt(27)\n\t"
      "v_mfma_f32_16x16x32_bf16 a[80:83], v[124:127], v[144:147], a[80:83]\n\t"
      "ds_read_b128 v[216:219], v9 offset:15360\n\t"
      "v_mfma_f32_16x16x32_bf16 a[100:103], v[124:127], v[148:151], a[100:103]\n\t"
      "buffer_load_dwordx4 v[108:111], v0, s[36:39], s49 offen sc1\n\t"
      "v_mfma_f32_16x16x32_bf16 a[120:123], v[124:127], v[152:155], a[120:123]\n\t"
      "ds_read_b128 v[220:223], v9 offset:16384\n\t"
      "v_mfma_f32_16x16x32_bf16 a[140:143], v[124:127], v[156:159], a[140:143]\n\t"
      "buffer_load_dwordx4 v[112:115], v0, s[36:39], s49 offen offset:1024 sc1\n\t"
      "v_mfma_f32_16x16x32_bf16 a[84:87], v[128:131], v[144:147], a[84:87]\n\t"
      "ds_read_b128 v[224:227], v9 offset:17408\n\t"
      "v_mfma_f32_16x16x32_bf16 a[104:107], v[128:131], v[148:151], a[104:107]\n\t"
      "buffer_load_dwordx4 v[116:119], v0, s[36:39], s49 offen offset:2048 sc1\n\t"
      "v_mfma_f32_16x16x32_bf16 a[124:127], v[128:131], v[152:155], a[124:127]\n\t"
      "ds_read_b128 v[228:231], v9 offset:18432\n\t"
      "v_mfma_f32_16x16x32_bf16 a[144:147], v[128:131], v[156:159], a[144:147]\n\t"
      "buffer_load_dwordx4 v[120:123], v0, s[36:39], s49 offen offset:3072 sc1\n\t"
      "v_mfma_f32_16x16x32_bf16 a[88:91], v[132:135], v[144:147], a[88:91]\n\t"
      "ds_read_b128 v[232:235], v9 offset:19456\n\t"
      "v_mfma_f32_16x16x32_bf16 a[108:111], v[132:135], v[148:151], a[108:111]\n\t"
      "buffer_load_dwordx4 v[88:91], v0, s[36:39], s48 offen sc1\n\t"
      "v_mfma_f32_16x16x32_bf16 a[128:131], v[132:135], v[152:155], a[128:131]\n\t"
      "v_mfma_f32_16x16x32_bf16 a[148:151], v[132:135], v[156:159], a[148:151]\n\t"
      "buffer_load_dwordx4 v[92:95], v0, s[36:39], s48 offen offset:1024 sc1\n\t"
      "v_mfma_f32_16x16x32_bf16 a[92:95], v[136:139], v[144:147], a[92:95]\n\t"
      "v_mfma_f32_16x16x32_bf16 a[112:115], v[136:139], v[148:151], a[112:115]\n\t"
      "buffer_load_dwordx4 v[96:99], v0, s[36:39], s48 offen offset:2048 sc1\n\t"
      "v_mfma_f32_16x16x32_bf16 a[132:135], v[136:139], v[152:155], a[132:135]\n\t"
      "v_mfma_f32_16x16x32_bf16 a[152:155], v[136:139], v[156:159], a[152:155]\n\t"
      "buffer_load_dwordx4 v[100:103], v0, s[36:39], s48 offen offset:3072 sc1\n\t"
      "v_mfma_f32_16x16x32_bf16 a[96:99], v[140:143], v[144:147], a[96:99]\n\t"
      "v_mfma_f32_16x16x32_bf16 a[116:119], v[140:143], v[148:151], a[116:119]\n\t"
      "buffer_load_dwordx4 v[104:107], v1, s[36:39], s48 offen sc1\n\t"
      "v_mfma_f32_16x16x32_bf16 a[136:139], v[140:143], v[152:155], a[136:139]\n\t"
      "v_mfma_f32_16x16x32_bf16 a[156:159], v[140:143], v[156:159], a[156:159]\n\t"
      "s_add_u32 s50, s51, 56\n\t"
      "s_lshl_b32 s49, s50, 12\n\t"
      "s_add_u32 s49, s49, s53\n\t"
      "s_add_u32 s50, s50, 54\n\t"
      "s_mul_i32 s48, s50, 0x1400\n\t"
      "s_add_u32 s48, s48, s54\n\t"
      "s_waitcnt vmcnt(27)\n\t"
      "v_mfma_f32_16x16x32_bf16 a[80:83], v[160:163], v[180:183], a[80:83]\n\t"
      "s_waitcnt lgkmcnt(0)\n\t"
      "v_mfma_f32_16x16x32_bf16 a[100:103], v[160:163], v[184:187], a[100:103]\n\t"
      "buffer_load_dwordx4 v[144:147], v0, s[36:39], s49 offen sc1\n\t"
      "v_mfma_f32_16x16x32_bf16 a[120:123], v[160:163], v[188:191], a[120:123]\n\t"
      "v_pk_add_f32 v[196:197], v[196:197], v[216:217]\n\t"
      "v_mfma_f32_16x16x32_bf16 a[140:143], v[160:163], v[192:195], a[140:143]\n\t"
      "buffer_load_dwordx4 v[148:151], v0, s[36:39], s49 offen offset:1024 sc1\n\t"
      "v_mfma_f32_16x16x32_bf16 a[84:87], v[164:167], v[180:183], a[84:87]\n\t"
      "v_pk_add_f32 v[198:199], v[198:199], v[218:219]\n\t"
      "v_mfma_f32_16x16x32_bf16 a[104:107], v[164:167], v[184:187], a[104:107]\n\t"
      "buffer_load_dwordx4 v[152:155], v0, s[36:39], s49 offen offset:2048 sc1\n\t"
      "v_mfma_f32_16x16x32_bf16 a[124:127], v[164:167], v[188:191], a[124:127]\n\t"
      "v_pk_add_f32 v[200:201], v[200:201], v[220:221]\n\t"
      "v_mfma_f32_16x16x32_bf16 a[144:147], v[164:167], v[192:195], a[144:147]\n\t"
      "buffer_load_dwordx4 v[156:159], v0, s[36:39], s49 offen offset:3072 sc1\n\t"
      "v_mfma_f32_16x16x32_bf16 a[88:91], v[168:171], v[180:183], a[88:91]\n\t"
      "v_pk_add_f32 v[202:203], v[202:203], v[222:223]\n\t"
      "v_pk_add_f32 v[204:205], v[204:205], v[224:225]\n\t"
      "v_mfma_f32_16x16x32_bf16 a[108:111], v[168:171], v[184:187], a[108:111]\n\t"
      "buffer_load_dwordx4 v[124:127], v0, s[36:39], s48 offen sc1\n\t"
      "v_mfma_f32_16x16x32_bf16 a[128:131], v[168:171], v[188:191], a[128:131]\n\t"
      "v_pk_add_f32 v[206:207], v[206:207], v[226:227]\n\t"
      "v_pk_add_f32 v[208:209], v[208:209], v[228:229]\n\t"
      "v_mfma_f32_16x16x32_bf16 a[148:151], v[168:171], v[192:195], a[148:151]\n\t"
      "buffer_load_dwordx4 v[128:131], v0, s[36:39], s48 offen offset:1024 sc1\n\t"
      "v_mfma_f32_16x16x32_bf16 a[92:95], v[172:175], v[180:183], a[92:95]\n\t"
      "v_pk_add_f32 v[210:211], v[210:211], v[230:231]\n\t"
      "v_pk_add_f32 v[212:213], v[212:213], v[232:233]\n\t"
      "v_mfma_f32_16x16x32_bf16 a[112:115], v[172:175], v[184:187], a[112:115]\n\t"
      "buffer_load_dwordx4 v[132:135], v0, s[36:39], s48 offen offset:2048 sc1\n\t"
      "v_mfma_f32_16x16x32_bf16 a[132:135], v[172:175], v[188:191], a[132:135]\n\t"
      "v_pk_add_f32 v[214:215], v[214:215], v[234:235]\n\t"
      "v_mfma_f32_16x16x32_bf16 a[152:155], v[172:175], v[192:195], a[152:155]\n\t"
      "buffer_load_dwordx4 v[136:139], v0, s[36:39], s48 offen offset:3072 sc1\n\t"
      "v_mfma_f32_16x16x32_bf16 a[96:99], v[176:179], v[180:183], a[96:99]\n\t"
      "v_mfma_f32_16x16x32_bf16 a[116:119], v[176:179], v[184:187], a[116:119]\n\t"
      "buffer_load_dwordx4 v[140:143], v1, s[36:39], s48 offen sc1\n\t"
      "v_mfma_f32_16x16x32_bf16 a[136:139], v[176:179], v[188:191], a[136:139]\n\t"
      "v_mfma_f32_16x16x32_bf16 a[156:159], v[176:179], v[192:195], a[156:159]\n\t"
      "s_add_u32 s50, s51, 60\n\t"
      "s_lshl_b32 s49, s50, 12\n\t"
      "s_add_u32 s49, s49, s53\n\t"
      "s_add_u32 s50, s50, 54\n\t"
      "s_mul_i32 s48, s50, 0x1400\n\t"
      "s_add_u32 s48, s48, s54\n\t"
      "s_waitcnt vmcnt(27)\n\t"
      "v_mfma_f32_16x16x32_bf16 a[80:83], v[16:19], v[36:39], a[80:83]\n\t"
      "v_mfma_f32_16x16x32_bf16 a[100:103], v[16:19], v[40:43], a[100:103]\n\t"
      "buffer_load_dwordx4 v[180:183], v0, s[36:39], s49 offen sc1\n\t"
      "v_mfma_f32_16x16x32_bf16 a[120:123], v[16:19], v[44:47], a[120:123]\n\t"
      "v_mfma_f32_16x16x32_bf16 a[140:143], v[16:19], v[48:51], a[140:143]\n\t"
      "buffer_load_dwordx4 v[184:187], v0, s[36:39], s49 offen offset:1024 sc1\n\t"
      "v_mfma_f32_16x16x32_bf16 a[84:87], v[20:23], v[36:39], a[84:87]\n\t"
      "v_mfma_f32_16x16x32_bf16 a[104:107], v[20:23], v[40:43], a[104:107]\n\t"
      "buffer_load_dwordx4 v[188:191], v0, s[36:39], s49 offen offset:2048 sc1\n\t"
      "v_mfma_f32_16x16x32_bf16 a[124:127], v[20:23], v[44:47], a[124:127]\n\t"
      "v_mfma_f32_16x16x32_bf16 a[144:147], v[20:23], v[48:51], a[144:147]\n\t"
      "buffer_load_dwordx4 v[192:195], v0, s[36:39], s49 offen offset:3072 sc1\n\t"
      "v_mfma_f32_16x16x32_bf16 a[88:91], v[24:27], v[36:39], a[88:91]\n\t"
      "v_mfma_f32_16x16x32_bf16 a[108:111], v[24:27], v[40:43], a[108:111]\n\t"
      "buffer_load_dwordx4 v[160:163], v0, s[36:39], s48 offen sc1\n\t"
      "v_mfma_f32_16x16x32_bf16 a[128:131], v[24:27], v[44:47], a[128:131]\n\t"
      "v_mfma_f32_16x16x32_bf16 a[148:151], v[24:27], v[48:51], a[148:151]\n\t"
      "buffer_load_dwordx4 v[164:167], v0, s[36:39], s48 offen offset:1024 sc1\n\t"
      "v_mfma_f32_16x16x32_bf16 a[92:95], v[28:31], v[36:39], a[92:95]\n\t"
      "v_mfma_f32_16x16x32_bf16 a[112:115], v[28:31], v[40:43], a[112:115]\n\t"
      "buffer_load_dwordx4 v[168:171], v0, s[36:39], s48 offen offset:2048 sc1\n\t"
      "v_mfma_f32_16x16x32_bf16 a[132:135], v[28:31], v[44:47], a[132:135]\n\t"
      "v_mfma_f32_16x16x32_bf16 a[152:155], v[28:31], v[48:51], a[152:155]\n\t"
      "buffer_load_dwordx4 v[172:175], v0, s[36:39], s48 offen offset:3072 sc1\n\t"
      "v_mfma_f32_16x16x32_bf16 a[96:99], v[32:35], v[36:39], a[96:99]\n\t"
      "v_mfma_f32_16x16x32_bf16 a[116:119], v[32:35], v[40:43], a[116:119]\n\t"
      "buffer_load_dwordx4 v[176:179], v1, s[36:39], s48 offen sc1\n\t"
      "v_mfma_f32_16x16x32_bf16 a[136:139], v[32:35], v[44:47], a[136:139]\n\t"
      "v_mfma_f32_16x16x32_bf16 a[156:159], v[32:35], v[48:51], a[156:159]\n\t"
      "s_add_u32 s50, s51, 64\n\t"
      "s_lshl_b32 s49, s50, 12\n\t"
      "s_add_u32 s49, s49, s53\n\t"
      "s_add_u32 s50, s50, 54\n\t"
      "s_mul_i32 s48, s50, 0x1400\n\t"
      "s_add_u32 s48, s48, s54\n\t"
      "s_waitcnt vmcnt(27)\n\t"
      "v_mfma_f32_16x16x32_bf16 a[80:83], v[52:55], v[72:75], a[80:83]\n\t"
      "v_mul_f32 v216, 0xbfb8aa3b, v196\n\t"
      "v_mfma_f32_16x16x32_bf16 a[100:103], v[52:55], v[76:79], a[100:103]\n\t"
      "buffer_load_dwordx4 v[36:39], v0, s[36:39], s49 offen sc1\n\t"
      "v_mfma_f32_16x16x32_bf16 a[120:123], v[52:55], v[80:83], a[120:123]\n\t"
      "v_mul_f32 v217, 0xbfb8aa3b, v197\n\t"
      "v_mfma_f32_16x16x32_bf16 a[140:143], v[52:55], v[84:87], a[140:143]\n\t"
      "buffer_load_dwordx4 v[40:43], v0, s[36:39], s49 offen offset:1024 sc1\n\t"
      "v_mfma_f32_16x16x32_bf16 a[84:87], v[56:59], v[72:75], a[84:87]\n\t"
      "v_mul_f32 v218, 0xbfb8aa3b, v198\n\t"
      "v_mfma_f32_16x16x32_bf16 a[104:107], v[56:59], v[76:79], a[104:107]\n\t"
      "buffer_load_dwordx4 v[44:47], v0, s[36:39], s49 offen offset:2048 sc1\n\t"
      "v_mfma_f32_16x16x32_bf16 a[124:127], v[56:59], v[80:83], a[124:127]\n\t"
      "v_mul_f32 v219, 0xbfb8aa3b, v199\n\t"
      "v_mfma_f32_16x16x32_bf16 a[144:147], v[56:59], v[84:87], a[144:147]\n\t"
      "buffer_load_dwordx4 v[48:51], v0, s[36:39], s49 offen offset:3072 sc1\n\t"
      "v_mfma_f32_16x16x32_bf16 a[88:91], v[60:63], v[72:75], a[88:91]\n\t"
      "v_mul_f32 v220, 0xbfb8aa3b, v200\n\t"
      "v_mul_f32 v221, 0xbfb8aa3b, v201\n\t"
      "v_mfma_f32_16x16x32_bf16 a[108:111], v[60:63], v[76:79], a[108:111]\n\t"
      "buffer_load_dwordx4 v[16:19], v0, s[36:39], s48 offen sc1\n\t"
      "v_mfma_f32_16x16x32_bf16 a[128:131], v[60:63], v[80:83], a[128:131]\n\t"
      "v_mul_f32 v222, 0xbfb8aa3b, v202\n\t"
      "v_mul_f32 v223, 0xbfb8aa3b, v203\n\t"
      "v_mfma_f32_16x16x32_bf16 a[148:151], v[60:63], v[84:87], a[148:151]\n\t"
      "buffer_load_dwordx4 v[20:23], v0, s[36:39], s48 offen offset:1024 sc1\n\t"
      "v_mfma_f32_16x16x32_bf16 a[92:95], v[64:67], v[72:75], a[92:95]\n\t"
      "v_mul_f32 v224, 0xbfb8aa3b, v204\n\t"
      "v_mul_f32 v225, 0xbfb8aa3b, v205\n\t"
      "v_mfma_f32_16x16x32_bf16 a[112:115], v[64:67], v[76:79], a[112:115]\n\t"
      "buffer_load_dwordx4 v[24:27], v0, s[36:39], s48 offen offset:2048 sc1\n\t"
      "v_mfma_f32_16x16x32_bf16 a[132:135], v[64:67], v[80:83], a[132:135]\n\t"
      "v_mul_f32 v226, 0xbfb8aa3b, v206\n\t"
      "v_mul_f32 v227, 0xbfb8aa3b, v207\n\t"
      "v_mfma_f32_16x16x32_bf16 a[152:155], v[64:67], v[84:87], a[152:155]\n\t"
      "buffer_load_dwordx4 v[28:31], v0, s[36:39], s48 offen offset:3072 sc1\n\t"
      "v_mfma_f32_16x16x32_bf16 a[96:99], v[68:71], v[72:75], a[96:99]\n\t"
      "v_mul_f32 v228, 0xbfb8aa3b, v208\n\t"
      "v_mul_f32 v229, 0xbfb8aa3b, v209\n\t"
      "v_mfma_f32_16x16x32_bf16 a[116:119], v[68:71], v[76:79], a[116:119]\n\t"
      "buffer_load_dwordx4 v[32:35], v1, s[36:39], s48 offen sc1\n\t"
      "v_mfma_f32_16x16x32_bf16 a[136:139], v[68:71], v[80:83], a[136:139]\n\t"
      "v_mul_f32 v230, 0xbfb8aa3b, v210\n\t"
      "v_mul_f32 v231, 0xbfb8aa3b, v211\n\t"
      "v_mfma_f32_16x16x32_bf16 a[156:159], v[68:71], v[84:87], a[156:159]\n\t"
      "v_mul_f32 v232, 0xbfb8aa3b, v212\n\t"
      "v_mul_f32 v233, 0xbfb8aa3b, v213\n\t"
      "v_mul_f32 v234, 0xbfb8aa3b, v214\n\t"
      "v_mul_f32 v235, 0xbfb8aa3b, v215\n\t"
      "s_add_u32 s50, s51, 0x44\n\t"
      "s_lshl_b32 s49, s50, 12\n\t"
      "s_add_u32 s49, s49, s53\n\t"
      "s_add_u32 s50, s50, 54\n\t"
      "s_mul_i32 s48, s50, 0x1400\n\t"
      "s_add_u32 s48, s48, s54\n\t"
      "s_waitcnt vmcnt(27)\n\t"
      "v_mfma_f32_16x16x32_bf16 a[80:83], v[88:91], v[108:111], a[80:83]\n\t"
      "v_exp_f32 v216, v216\n\t"
      "v_mfma_f32_16x16x32_bf16 a[100:103], v[88:91], v[112:115], a[100:103]\n\t"
      "buffer_load_dwordx4 v[72:75], v0, s[36:39], s49 offen sc1\n\t"
      "v_mfma_f32_16x16x32_bf16 a[120:123], v[88:91], v[116:119], a[120:123]\n\t"
      "v_exp_f32 v217, v217\n\t"
      "v_mfma_f32_16x16x32_bf16 a[140:143], v[88:91], v[120:123], a[140:143]\n\t"
      "buffer_load_dwordx4 v[76:79], v0, s[36:39], s49 offen offset:1024 sc1\n\t"
      "v_mfma_f32_16x16x32_bf16 a[84:87], v[92:95], v[108:111], a[84:87]\n\t"
      "v_exp_f32 v218, v218\n\t"
      "v_mfma_f32_16x16x32_bf16 a[104:107], v[92:95], v[112:115], a[104:107]\n\t"
      "buffer_load_dwordx4 v[80:83], v0, s[36:39], s49 offen offset:2048 sc1\n\t"
      "v_mfma_f32_16x16x32_bf16 a[124:127], v[92:95], v[116:119], a[124:127]\n\t"
      "v_exp_f32 v219, v219\n\t"
      "v_mfma_f32_16x16x32_bf16 a[144:147], v[92:95], v[120:123], a[144:147]\n\t"
      "buffer_load_dwordx4 v[84:87], v0, s[36:39], s49 offen offset:3072 sc1\n\t"
      "v_mfma_f32_16x16x32_bf16 a[88:91], v[96:99], v[108:111], a[88:91]\n\t"
      "v_exp_f32 v220, v220\n\t"
      "v_exp_f32 v221, v221\n\t"
      "v_mfma_f32_16x16x32_bf16 a[108:111], v[96:99], v[112:115], a[108:111]\n\t"
      "buffer_load_dwordx4 v[52:55], v0, s[36:39], s48 offen sc1\n\t"
      "v_mfma_f32_16x16x32_bf16 a[128:131], v[96:99], v[116:119], a[128:131]\n\t"
      "v_exp_f32 v222, v222\n\t"
      "v_exp_f32 v223, v223\n\t"
      "v_mfma_f32_16x16x32_bf16 a[148:151], v[96:99], v[120:123], a[148:151]\n\t"
      "buffer_load_dwordx4 v[56:59], v0, s[36:39], s48 offen offset:1024 sc1\n\t"
      "v_mfma_f32_16x16x32_bf16 a[92:95], v[100:103], v[108:111], a[92:95]\n\t"
      "v_exp_f32 v224, v224\n\t"
      "v_exp_f32 v225, v225\n\t"
      "v_mfma_f32_16x16x32_bf16 a[112:115], v[100:103], v[112:115], a[112:115]\n\t"
      "buffer_load_dwordx4 v[60:63], v0, s[36:39], s48 offen offset:2048 sc1\n\t"
      "v_mfma_f32_16x16x32_bf16 a[132:135], v[100:103], v[116:119], a[132:135]\n\t"
      "v_exp_f32 v226, v226\n\t"
      "v_exp_f32 v227, v227\n\t"
      "v_mfma_f32_16x16x32_bf16 a[152:155], v[100:103], v[120:123], a[152:155]\n\t"
      "buffer_load_dwordx4 v[64:67], v0, s[36:39], s48 offen offset:3072 sc1\n\t"
      "v_mfma_f32_16x16x32_bf16 a[96:99], v[104:107], v[108:111], a[96:99]\n\t"
      "v_exp_f32 v228, v228\n\t"
      "v_exp_f32 v229, v229\n\t"
      "v_mfma_f32_16x16x32_bf16 a[116:119], v[104:107], v[112:115], a[116:119]\n\t"
      "buffer_load_dwordx4 v[68:71], v1, s[36:39], s48 offen sc1\n\t"
      "v_mfma_f32_16x16x32_bf16 a[136:139], v[104:107], v[116:119], a[136:139]\n\t"
      "v_exp_f32 v230, v230\n\t"
      "v_exp_f32 v231, v231\n\t"
      "v_mfma_f32_16x16x32_bf16 a[156:159], v[104:107], v[120:123], a[156:159]\n\t"
      "v_exp_f32 v232, v232\n\t"
      "v_exp_f32 v233, v233\n\t"
      "v_exp_f32 v234, v234\n\t"
      "v_exp_f32 v235, v235\n\t"
      "s_add_u32 s50, s51, 0x48\n\t"
      "s_lshl_b32 s49, s50, 12\n\t"
      "s_add_u32 s49, s49, s53\n\t"
      "s_add_u32 s50, s50, 54\n\t"
      "s_mul_i32 s48, s50, 0x1400\n\t"
      "s_add_u32 s48, s48, s54\n\t"
      "s_waitcnt vmcnt(27)\n\t"
      "v_mfma_f32_16x16x32_bf16 a[80:83], v[124:127], v[144:147], a[80:83]\n\t"
      "v_add_f32 v216, 0x3f800000, v216\n\t"
      "v_mfma_f32_16x16x32_bf16 a[100:103], v[124:127], v[148:151], a[100:103]\n\t"
      "buffer_load_dwordx4 v[108:111], v0, s[36:39], s49 offen sc1\n\t"
      "v_mfma_f32_16x16x32_bf16 a[120:123], v[124:127], v[152:155], a[120:123]\n\t"
      "v_add_f32 v217, 0x3f800000, v217\n\t"
      "v_mfma_f32_16x16x32_bf16 a[140:143], v[124:127], v[156:159], a[140:143]\n\t"
      "buffer_load_dwordx4 v[112:115], v0, s[36:39], s49 offen offset:1024 sc1\n\t"
      "v_mfma_f32_16x16x32_bf16 a[84:87], v[128:131], v[144:147], a[84:87]\n\t"
      "v_add_f32 v218, 0x3f800000, v218\n\t"
      "v_mfma_f32_16x16x32_bf16 a[104:107], v[128:131], v[148:151], a[104:107]\n\t"
      "buffer_load_dwordx4 v[116:119], v0, s[36:39], s49 offen offset:2048 sc1\n\t"
      "v_mfma_f32_16x16x32_bf16 a[124:127], v[128:131], v[152:155], a[124:127]\n\t"
      "v_add_f32 v219, 0x3f800000, v219\n\t"
      "v_mfma_f32_16x16x32_bf16 a[144:147], v[128:131], v[156:159], a[144:147]\n\t"
      "buffer_load_dwordx4 v[120:123], v0, s[36:39], s49 offen offset:3072 sc1\n\t"
      "v_mfma_f32_16x16x32_bf16 a[88:91], v[132:135], v[144:147], a[88:91]\n\t"
      "v_add_f32 v220, 0x3f800000, v220\n\t"
      "v_add_f32 v221, 0x3f800000, v221\n\t"
      "v_mfma_f32_16x16x32_bf16 a[108:111], v[132:135], v[148:151], a[108:111]\n\t"
      "buffer_load_dwordx4 v[88:91], v0, s[36:39], s48 offen sc1\n\t"
      "v_mfma_f32_16x16x32_bf16 a[128:131], v[132:135], v[152:155], a[128:131]\n\t"
      "v_add_f32 v222, 0x3f800000, v222\n\t"
      "v_add_f32 v223, 0x3f800000, v223\n\t"
      "v_mfma_f32_16x16x32_bf16 a[148:151], v[132:135], v[156:159], a[148:151]\n\t"
      "buffer_load_dwordx4 v[92:95], v0, s[36:39], s48 offen offset:1024 sc1\n\t"
      "v_mfma_f32_16x16x32_bf16 a[92:95], v[136:139], v[144:147], a[92:95]\n\t"
      "v_add_f32 v224, 0x3f800000, v224\n\t"
      "v_add_f32 v225, 0x3f800000, v225\n\t"
      "v_mfma_f32_16x16x32_bf16 a[112:115], v[136:139], v[148:151], a[112:115]\n\t"
      "buffer_load_dwordx4 v[96:99], v0, s[36:39], s48 offen offset:2048 sc1\n\t"
      "v_mfma_f32_16x16x32_bf16 a[132:135], v[136:139], v[152:155], a[132:135]\n\t"
      "v_add_f32 v226, 0x3f800000, v226\n\t"
      "v_add_f32 v227, 0x3f800000, v227\n\t"
      "v_mfma_f32_16x16x32_bf16 a[152:155], v[136:139], v[156:159], a[152:155]\n\t"
      "buffer_load_dwordx4 v[100:103], v0, s[36:39], s48 offen offset:3072 sc1\n\t"
      "v_mfma_f32_16x16x32_bf16 a[96:99], v[140:143], v[144:147], a[96:99]\n\t"
      "v_add_f32 v228, 0x3f800000, v228\n\t"
      "v_add_f32 v229, 0x3f800000, v229\n\t"
      "v_mfma_f32_16x16x32_bf16 a[116:119], v[140:143], v[148:151], a[116:119]\n\t"
      "buffer_load_dwordx4 v[104:107], v1, s[36:39], s48 offen sc1\n\t"
      "v_mfma_f32_16x16x32_bf16 a[136:139], v[140:143], v[152:155], a[136:139]\n\t"
      "v_add_f32 v230, 0x3f800000, v230\n\t"
      "v_add_f32 v231, 0x3f800000, v231\n\t"
      "v_mfma_f32_16x16x32_bf16 a[156:159], v[140:143], v[156:159], a[156:159]\n\t"
      "v_add_f32 v232, 0x3f800000, v232\n\t"
      "v_add_f32 v233, 0x3f800000, v233\n\t"
      "v_add_f32 v234, 0x3f800000, v234\n\t"
      "v_add_f32 v235, 0x3f800000, v235\n\t"
      "s_add_u32 s50, s51, 0x4c\n\t"
      "s_lshl_b32 s49, s50, 12\n\t"
      "s_add_u32 s49, s49, s53\n\t"
      "s_add_u32 s50, s50, 54\n\t"
      "s_mul_i32 s48, s50, 0x1400\n\t"
      "s_add_u32 s48, s48, s54\n\t"
      "s_waitcnt vmcnt(27)\n\t"
      "v_mfma_f32_16x16x32_bf16 a[80:83], v[160:163], v[180:183], a[80:83]\n\t"
      "v_rcp_f32 v216, v216\n\t"
      "v_mfma_f32_16x16x32_bf16 a[100:103], v[160:163], v[184:187], a[100:103]\n\t"
      "buffer_load_dwordx4 v[144:147], v0, s[36:39], s49 offen sc1\n\t"
      "v_mfma_f32_16x16x32_bf16 a[120:123], v[160:163], v[188:191], a[120:123]\n\t"
      "v_rcp_f32 v217, v217\n\t"
      "v_mfma_f32_16x16x32_bf16 a[140:143], v[160:163], v[192:195], a[140:143]\n\t"
      "buffer_load_dwordx4 v[148:151], v0, s[36:39], s49 offen offset:1024 sc1\n\t"
      "v_mfma_f32_16x16x32_bf16 a[84:87], v[164:167], v[180:183], a[84:87]\n\t"
      "v_rcp_f32 v218, v218\n\t"
      "v_mfma_f32_16x16x32_bf16 a[104:107], v[164:167], v[184:187], a[104:107]\n\t"
      "buffer_load_dwordx4 v[152:155], v0, s[36:39], s49 offen offset:2048 sc1\n\t"
      "v_mfma_f32_16x16x32_bf16 a[124:127], v[164:167], v[188:191], a[124:127]\n\t"
      "v_rcp_f32 v219, v219\n\t"
      "v_mfma_f32_16x16x32_bf16 a[144:147], v[164:167], v[192:195], a[144:147]\n\t"
      "buffer_load_dwordx4 v[156:159], v0, s[36:39], s49 offen offset:3072 sc1\n\t"
      "v_mfma_f32_16x16x32_bf16 a[88:91], v[168:171], v[180:183], a[88:91]\n\t"
      "v_rcp_f32 v220, v220\n\t"
      "v_rcp_f32 v221, v221\n\t"
      "v_mfma_f32_16x16x32_bf16 a[108:111], v[168:171], v[184:187], a[108:111]\n\t"
      "buffer_load_dwordx4 v[124:127], v0, s[36:39], s48 offen sc1\n\t"
      "v_mfma_f32_16x16x32_bf16 a[128:131], v[168:171], v[188:191], a[128:131]\n\t"
      "v_rcp_f32 v222, v222\n\t"
      "v_rcp_f32 v223, v223\n\t"
      "v_mfma_f32_16x16x32_bf16 a[148:151], v[168:171], v[192:195], a[148:151]\n\t"
      "buffer_load_dwordx4 v[128:131], v0, s[36:39], s48 offen offset:1024 sc1\n\t"
      "v_mfma_f32_16x16x32_bf16 a[92:95], v[172:175], v[180:183], a[92:95]\n\t"
      "v_rcp_f32 v224, v224\n\t"
      "v_rcp_f32 v225, v225\n\t"
      "v_mfma_f32_16x16x32_bf16 a[112:115], v[172:175], v[184:187], a[112:115]\n\t"
      "buffer_load_dwordx4 v[132:135], v0, s[36:39], s48 offen offset:2048 sc1\n\t"
      "v_mfma_f32_16x16x32_bf16 a[132:135], v[172:175], v[188:191], a[132:135]\n\t"
      "v_rcp_f32 v226, v226\n\t"
      "v_rcp_f32 v227, v227\n\t"
      "v_mfma_f32_16x16x32_bf16 a[152:155], v[172:175], v[192:195], a[152:155]\n\t"
      "buffer_load_dwordx4 v[136:139], v0, s[36:39], s48 offen offset:3072 sc1\n\t"
      "v_mfma_f32_16x16x32_bf16 a[96:99], v[176:179], v[180:183], a[96:99]\n\t"
      "v_rcp_f32 v228, v228\n\t"
      "v_rcp_f32 v229, v229\n\t"
      "v_mfma_f32_16x16x32_bf16 a[116:119], v[176:179], v[184:187], a[116:119]\n\t"
      "buffer_load_dwordx4 v[140:143], v1, s[36:39], s48 offen sc1\n\t"
      "v_mfma_f32_16x16x32_bf16 a[136:139], v[176:179], v[188:191], a[136:139]\n\t"
      "v_rcp_f32 v230, v230\n\t"
      "v_rcp_f32 v231, v231\n\t"
      "v_mfma_f32_16x16x32_bf16 a[156:159], v[176:179], v[192:195], a[156:159]\n\t"
      "v_rcp_f32 v232, v232\n\t"
      "v_rcp_f32 v233, v233\n\t"
      "v_rcp_f32 v234, v234\n\t"
      "v_rcp_f32 v235, v235\n\t"
      "s_add_u32 s50, s51, 0x50\n\t"
      "s_lshl_b32 s49, s50, 12\n\t"
      "s_add_u32 s49, s49, s53\n\t"
      "s_add_u32 s50, s50, 54\n\t"
      "s_mul_i32 s48, s50, 0x1400\n\t"
      "s_add_u32 s48, s48, s54\n\t"
      "s_waitcnt vmcnt(27)\n\t"
      "v_mfma_f32_16x16x32_bf16 a[80:83], v[16:19], v[36:39], a[80:83]\n\t"
      "v_mul_f32 v196, v196, v216\n\t"
      "v_mfma_f32_16x16x32_bf16 a[100:103], v[16:19], v[40:43], a[100:103]\n\t"
      "buffer_load_dwordx4 v[180:183], v0, s[36:39], s49 offen sc1\n\t"
      "v_mfma_f32_16x16x32_bf16 a[120:123], v[16:19], v[44:47], a[120:123]\n\t"
      "v_mul_f32 v197, v197, v217\n\t"
      "v_mfma_f32_16x16x32_bf16 a[140:143], v[16:19], v[48:51], a[140:143]\n\t"
      "buffer_load_dwordx4 v[184:187], v0, s[36:39], s49 offen offset:1024 sc1\n\t"
      "v_mfma_f32_16x16x32_bf16 a[84:87], v[20:23], v[36:39], a[84:87]\n\t"
      "v_mul_f32 v198, v198, v218\n\t"
      "v_mfma_f32_16x16x32_bf16 a[104:107], v[20:23], v[40:43], a[104:107]\n\t"
      "buffer_load_dwordx4 v[188:191], v0, s[36:39], s49 offen offset:2048 sc1\n\t"
      "v_mfma_f32_16x16x32_bf16 a[124:127], v[20:23], v[44:47], a[124:127]\n\t"
      "v_mul_f32 v199, v199, v219\n\t"
      "v_mfma_f32_16x16x32_bf16 a[144:147], v[20:23], v[48:51], a[144:147]\n\t"
      "buffer_load_dwordx4 v[192:195], v0, s[36:39], s49 offen offset:3072 sc1\n\t"
      "v_mfma_f32_16x16x32_bf16 a[88:91], v[24:27], v[36:39], a[88:91]\n\t"
      "v_mul_f32 v200, v200, v220\n\t"
      "v_mul_f32 v201, v201, v221\n\t"
      "v_mfma_f32_16x16x32_bf16 a[108:111], v[24:27], v[40:43], a[108:111]\n\t"
      "buffer_load_dwordx4 v[160:163], v0, s[36:39], s48 offen sc1\n\t"
      "v_mfma_f32_16x16x32_bf16 a[128:131], v[24:27], v[44:47], a[128:131]\n\t"
      "v_mul_f32 v202, v202, v222\n\t"
      "v_mul_f32 v203, v203, v223\n\t"
      "v_mfma_f32_16x16x32_bf16 a[148:151], v[24:27], v[48:51], a[148:151]\n\t"
      "buffer_load_dwordx4 v[164:167], v0, s[36:39], s48 offen offset:1024 sc1\n\t"
      "v_mfma_f32_16x16x32_bf16 a[92:95], v[28:31], v[36:39], a[92:95]\n\t"
      "v_mul_f32 v204, v204, v224\n\t"
      "v_mul_f32 v205, v205, v225\n\t"
      "v_mfma_f32_16x16x32_bf16 a[112:115], v[28:31], v[40:43], a[112:115]\n\t"
      "buffer_load_dwordx4 v[168:171], v0, s[36:39], s48 offen offset:2048 sc1\n\t"
      "v_mfma_f32_16x16x32_bf16 a[132:135], v[28:31], v[44:47], a[132:135]\n\t"
      "v_mul_f32 v206, v206, v226\n\t"
      "v_mul_f32 v207, v207, v227\n\t"
      "v_mfma_f32_16x16x32_bf16 a[152:155], v[28:31], v[48:51], a[152:155]\n\t"
      "buffer_load_dwordx4 v[172:175], v0, s[36:39], s48 offen offset:3072 sc1\n\t"
      "v_mfma_f32_16x16x32_bf16 a[96:99], v[32:35], v[36:39], a[96:99]\n\t"
      "v_mul_f32 v208, v208, v228\n\t"
      "v_mul_f32 v209, v209, v229\n\t"
      "v_mfma_f32_16x16x32_bf16 a[116:119], v[32:35], v[40:43], a[116:119]\n\t"
      "buffer_load_dwordx4 v[176:179], v1, s[36:39], s48 offen sc1\n\t"
      "v_mfma_f32_16x16x32_bf16 a[136:139], v[32:35], v[44:47], a[136:139]\n\t"
      "v_mul_f32 v210, v210, v230\n\t"
      "v_mul_f32 v211, v211, v231\n\t"
      "v_mfma_f32_16x16x32_bf16 a[156:159], v[32:35], v[48:51], a[156:159]\n\t"
      "v_mul_f32 v212, v212, v232\n\t"
      "v_mul_f32 v213, v213, v233\n\t"
      "v_mul_f32 v214, v214, v234\n\t"
      "v_mul_f32 v215, v215, v235\n\t"
      "s_add_u32 s50, s51, 0x54\n\t"
      "s_lshl_b32 s49, s50, 12\n\t"
      "s_add_u32 s49, s49, s53\n\t"
      "s_add_u32 s50, s50, 54\n\t"
      "s_mul_i32 s48, s50, 0x1400\n\t"
      "s_add_u32 s48, s48, s54\n\t"
      "s_waitcnt vmcnt(27)\n\t"
      "v_mfma_f32_16x16x32_bf16 a[80:83], v[52:55], v[72:75], a[80:83]\n\t"
      "v_lshlrev_b32 v216, 16, v236\n\t"
      "v_mfma_f32_16x16x32_bf16 a[100:103], v[52:55], v[76:79], a[100:103]\n\t"
      "buffer_load_dwordx4 v[36:39], v0, s[36:39], s49 offen sc1\n\t"
      "v_mfma_f32_16x16x32_bf16 a[120:123], v[52:55], v[80:83], a[120:123]\n\t"
      "v_and_b32 v217, 0xffff0000, v236\n\t"
      "v_mfma_f32_16x16x32_bf16 a[140:143], v[52:55], v[84:87], a[140:143]\n\t"
      "buffer_load_dwordx4 v[40:43], v0, s[36:39], s49 offen offset:1024 sc1\n\t"
      "v_mfma_f32_16x16x32_bf16 a[84:87], v[56:59], v[72:75], a[84:87]\n\t"
      "v_lshlrev_b32 v218, 16, v237\n\t"
      "v_mfma_f32_16x16x32_bf16 a[104:107], v[56:59], v[76:79], a[104:107]\n\t"
      "buffer_load_dwordx4 v[44:47], v0, s[36:39], s49 offen offset:2048 sc1\n\t"
      "v_mfma_f32_16x16x32_bf16 a[124:127], v[56:59], v[80:83], a[124:127]\n\t"
      "v_and_b32 v219, 0xffff0000, v237\n\t"
      "v_mfma_f32_16x16x32_bf16 a[144:147], v[56:59], v[84:87], a[144:147]\n\t"
      "buffer_load_dwordx4 v[48:51], v0, s[36:39], s49 offen offset:3072 sc1\n\t"
      "v_mfma_f32_16x16x32_bf16 a[88:91], v[60:63], v[72:75], a[88:91]\n\t"
      "v_lshlrev_b32 v220, 16, v238\n\t"
      "v_and_b32 v221, 0xffff0000, v238\n\t"
      "v_mfma_f32_16x16x32_bf16 a[108:111], v[60:63], v[76:79], a[108:111]\n\t"
      "buffer_load_dwordx4 v[16:19], v0, s[36:39], s48 offen sc1\n\t"
      "v_mfma_f32_16x16x32_bf16 a[128:131], v[60:63], v[80:83], a[128:131]\n\t"
      "v_lshlrev_b32 v222, 16, v239\n\t"
      "v_and_b32 v223, 0xffff0000, v239\n\t"
      "v_mfma_f32_16x16x32_bf16 a[148:151], v[60:63], v[84:87], a[148:151]\n\t"
      "buffer_load_dwordx4 v[20:23], v0, s[36:39], s48 offen offset:1024 sc1\n\t"
      "v_mfma_f32_16x16x32_bf16 a[92:95], v[64:67], v[72:75], a[92:95]\n\t"
      "v_lshlrev_b32 v224, 16, v240\n\t"
      "v_and_b32 v225, 0xffff0000, v240\n\t"
      "v_mfma_f32_16x16x32_bf16 a[112:115], v[64:67], v[76:79], a[112:115]\n\t"
      "buffer_load_dwordx4 v[24:27], v0, s[36:39], s48 offen offset:2048 sc1\n\t"
      "v_mfma_f32_16x16x32_bf16 a[132:135], v[64:67], v[80:83], a[132:135]\n\t"
      "v_lshlrev_b32 v226, 16, v241\n\t"
      "v_and_b32 v227, 0xffff0000, v241\n\t"
      "v_mfma_f32_16x16x32_bf16 a[152:155], v[64:67], v[84:87], a[152:155]\n\t"
      "buffer_load_dwordx4 v[28:31], v0, s[36:39], s48 offen offset:3072 sc1\n\t"
      "v_mfma_f32_16x16x32_bf16 a[96:99], v[68:71], v[72:75], a[96:99]\n\t"
      "v_lshlrev_b32 v228, 16, v242\n\t"
      "v_and_b32 v229, 0xffff0000, v242\n\t"
      "v_mfma_f32_16x16x32_bf16 a[116:119], v[68:71], v[76:79], a[116:119]\n\t"
      "buffer_load_dwordx4 v[32:35], v1, s[36:39], s48 offen sc1\n\t"
      "v_mfma_f32_16x16x32_bf16 a[136:139], v[68:71], v[80:83], a[136:139]\n\t"
      "v_lshlrev_b32 v230, 16, v243\n\t"
      "v_and_b32 v231, 0xffff0000, v243\n\t"
      "v_mfma_f32_16x16x32_bf16 a[156:159], v[68:71], v[84:87], a[156:159]\n\t"
      "v_lshlrev_b32 v232, 16, v244\n\t"
      "v_and_b32 v233, 0xffff0000, v244\n\t"
      "v_lshlrev_b32 v234, 16, v245\n\t"
      "v_and_b32 v235, 0xffff0000, v245\n\t"
      "s_add_u32 s50, s51, 0x58\n\t"
      "s_lshl_b32 s49, s50, 12\n\t"
      "s_add_u32 s49, s49, s53\n\t"
      "s_add_u32 s50, s50, 54\n\t"
      "s_mul_i32 s48, s50, 0x1400\n\t"
      "s_add_u32 s48, s48, s54\n\t"
      "s_waitcnt vmcnt(27)\n\t"
      "v_mfma_f32_16x16x32_bf16 a[80:83], v[88:91], v[108:111], a[80:83]\n\t"
      "v_mfma_f32_16x16x32_bf16 a[100:103], v[88:91], v[112:115], a[100:103]\n\t"
      "buffer_load_dwordx4 v[72:75], v0, s[36:39], s49 offen sc1\n\t"
      "v_mfma_f32_16x16x32_bf16 a[120:123], v[88:91], v[116:119], a[120:123]\n\t"
      "v_mfma_f32_16x16x32_bf16 a[140:143], v[88:91], v[120:123], a[140:143]\n\t"
      "buffer_load_dwordx4 v[76:79], v0, s[36:39], s49 offen offset:1024 sc1\n\t"
      "v_mfma_f32_16x16x32_bf16 a[84:87], v[92:95], v[108:111], a[84:87]\n\t"
      "v_mfma_f32_16x16x32_bf16 a[104:107], v[92:95], v[112:115], a[104:107]\n\t"
      "buffer_load_dwordx4 v[80:83], v0, s[36:39], s49 offen offset:2048 sc1\n\t"
      "v_mfma_f32_16x16x32_bf16 a[124:127], v[92:95], v[116:119], a[124:127]\n\t"
      "v_mfma_f32_16x16x32_bf16 a[144:147], v[92:95], v[120:123], a[144:147]\n\t"
      "buffer_load_dwordx4 v[84:87], v0, s[36:39], s49 offen offset:3072 sc1\n\t"
      "v_mfma_f32_16x16x32_bf16 a[88:91], v[96:99], v[108:111], a[88:91]\n\t"
      "v_mfma_f32_16x16x32_bf16 a[108:111], v[96:99], v[112:115], a[108:111]\n\t"
      "buffer_load_dwordx4 v[52:55], v0, s[36:39], s48 offen sc1\n\t"
      "v_mfma_f32_16x16x32_bf16 a[128:131], v[96:99], v[116:119], a[128:131]\n\t"
      "v_mfma_f32_16x16x32_bf16 a[148:151], v[96:99], v[120:123], a[148:151]\n\t"
      "buffer_load_dwordx4 v[56:59], v0, s[36:39], s48 offen offset:1024 sc1\n\t"
      "v_mfma_f32_16x16x32_bf16 a[92:95], v[100:103], v[108:111], a[92:95]\n\t"
      "v_mfma_f32_16x16x32_bf16 a[112:115], v[100:103], v[112:115], a[112:115]\n\t"
      "buffer_load_dwordx4 v[60:63], v0, s[36:39], s48 offen offset:2048 sc1\n\t"
      "v_mfma_f32_16x16x32_bf16 a[132:135], v[100:103], v[116:119], a[132:135]\n\t"
      "v_mfma_f32_16x16x32_bf16 a[152:155], v[100:103], v[120:123], a[152:155]\n\t"
      "buffer_load_dwordx4 v[64:67], v0, s[36:39], s48 offen offset:3072 sc1\n\t"
      "v_mfma_f32_16x16x32_bf16 a[96:99], v[104:107], v[108:111], a[96:99]\n\t"
      "v_mfma_f32_16x16x32_bf16 a[116:119], v[104:107], v[112:115], a[116:119]\n\t"
      "buffer_load_dwordx4 v[68:71], v1, s[36:39], s48 offen sc1\n\t"
      "v_mfma_f32_16x16x32_bf16 a[136:139], v[104:107], v[116:119], a[136:139]\n\t"
      "v_mfma_f32_16x16x32_bf16 a[156:159], v[104:107], v[120:123], a[156:159]\n\t"
      "s_add_u32 s50, s51, 0x5c\n\t"
      "s_lshl_b32 s49, s50, 12\n\t"
      "s_add_u32 s49, s49, s53\n\t"
      "s_add_u32 s50, s50, 54\n\t"
      "s_mul_i32 s48, s50, 0x1400\n\t"
      "s_add_u32 s48, s48, s54\n\t"
      "s_waitcnt vmcnt(27)\n\t"
      "v_mfma_f32_16x16x32_bf16 a[80:83], v[124:127], v[144:147], a[80:83]\n\t"
      "v_mfma_f32_16x16x32_bf16 a[100:103], v[124:127], v[148:151], a[100:103]\n\t"
      "buffer_load_dwordx4 v[108:111], v0, s[36:39], s49 offen sc1\n\t"
      "v_mfma_f32_16x16x32_bf16 a[120:123], v[124:127], v[152:155], a[120:123]\n\t"
      "v_mfma_f32_16x16x32_bf16 a[140:143], v[124:127], v[156:159], a[140:143]\n\t"
      "buffer_load_dwordx4 v[112:115], v0, s[36:39], s49 offen offset:1024 sc1\n\t"
      "v_mfma_f32_16x16x32_bf16 a[84:87], v[128:131], v[144:147], a[84:87]\n\t"
      "v_mfma_f32_16x16x32_bf16 a[104:107], v[128:131], v[148:151], a[104:107]\n\t"
      "buffer_load_dwordx4 v[116:119], v0, s[36:39], s49 offen offset:2048 sc1\n\t"
      "v_mfma_f32_16x16x32_bf16 a[124:127], v[128:131], v[152:155], a[124:127]\n\t"
      "v_mfma_f32_16x16x32_bf16 a[144:147], v[128:131], v[156:159], a[144:147]\n\t"
      "buffer_load_dwordx4 v[120:123], v0, s[36:39], s49 offen offset:3072 sc1\n\t"
      "v_mfma_f32_16x16x32_bf16 a[88:91], v[132:135], v[144:147], a[88:91]\n\t"
      "v_mfma_f32_16x16x32_bf16 a[108:111], v[132:135], v[148:151], a[108:111]\n\t"
      "buffer_load_dwordx4 v[88:91], v0, s[36:39], s48 offen sc1\n\t"
      "v_mfma_f32_16x16x32_bf16 a[128:131], v[132:135], v[152:155], a[128:131]\n\t"
      "v_mfma_f32_16x16x32_bf16 a[148:151], v[132:135], v[156:159], a[148:151]\n\t"
      "buffer_load_dwordx4 v[92:95], v0, s[36:39], s48 offen offset:1024 sc1\n\t"
      "v_mfma_f32_16x16x32_bf16 a[92:95], v[136:139], v[144:147], a[92:95]\n\t"
      "v_mfma_f32_16x16x32_bf16 a[112:115], v[136:139], v[148:151], a[112:115]\n\t"
      "buffer_load_dwordx4 v[96:99], v0, s[36:39], s48 offen offset:2048 sc1\n\t"
      "v_mfma_f32_16x16x32_bf16 a[132:135], v[136:139], v[152:155], a[132:135]\n\t"
      "v_mfma_f32_16x16x32_bf16 a[152:155], v[136:139], v[156:159], a[152:155]\n\t"
      "buffer_load_dwordx4 v[100:103], v0, s[36:39], s48 offen offset:3072 sc1\n\t"
      "v_mfma_f32_16x16x32_bf16 a[96:99], v[140:143], v[144:147], a[96:99]\n\t"
      "v_mfma_f32_16x16x32_bf16 a[116:119], v[140:143], v[148:151], a[116:119]\n\t"
      "buffer_load_dwordx4 v[104:107], v1, s[36:39], s48 offen sc1\n\t"
      "v_mfma_f32_16x16x32_bf16 a[136:139], v[140:143], v[152:155], a[136:139]\n\t"
      "v_mfma_f32_16x16x32_bf16 a[156:159], v[140:143], v[156:159], a[156:159]\n\t"
      "s_add_u32 s50, s51, 0x60\n\t"
      "s_lshl_b32 s49, s50, 12\n\t"
      "s_add_u32 s49, s49, s53\n\t"
      "s_add_u32 s50, s50, 54\n\t"
      "s_mul_i32 s48, s50, 0x1400\n\t"
      "s_add_u32 s48, s48, s54\n\t"
      "s_waitcnt vmcnt(27)\n\t"
      "v_mfma_f32_16x16x32_bf16 a[80:83], v[160:163], v[180:183], a[80:83]\n\t"
      "v_mfma_f32_16x16x32_bf16 a[100:103], v[160:163], v[184:187], a[100:103]\n\t"
      "buffer_load_dwordx4 v[144:147], v0, s[36:39], s49 offen sc1\n\t"
      "v_mfma_f32_16x16x32_bf16 a[120:123], v[160:163], v[188:191], a[120:123]\n\t"
      "v_mfma_f32_16x16x32_bf16 a[140:143], v[160:163], v[192:195], a[140:143]\n\t"
      "buffer_load_dwordx4 v[148:151], v0, s[36:39], s49 offen offset:1024 sc1\n\t"
      "v_mfma_f32_16x16x32_bf16 a[84:87], v[164:167], v[180:183], a[84:87]\n\t"
      "v_mfma_f32_16x16x32_bf16 a[104:107], v[164:167], v[184:187], a[104:107]\n\t"
      "buffer_load_dwordx4 v[152:155], v0, s[36:39], s49 offen offset:2048 sc1\n\t"
      "v_mfma_f32_16x16x32_bf16 a[124:127], v[164:167], v[188:191], a[124:127]\n\t"
      "v_mfma_f32_16x16x32_bf16 a[144:147], v[164:167], v[192:195], a[144:147]\n\t"
      "buffer_load_dwordx4 v[156:159], v0, s[36:39], s49 offen offset:3072 sc1\n\t"
      "v_mfma_f32_16x16x32_bf16 a[88:91], v[168:171], v[180:183], a[88:91]\n\t"
      "v_mfma_f32_16x16x32_bf16 a[108:111], v[168:171], v[184:187], a[108:111]\n\t"
      "buffer_load_dwordx4 v[124:127], v0, s[36:39], s48 offen sc1\n\t"
      "v_mfma_f32_16x16x32_bf16 a[128:131], v[168:171], v[188:191], a[128:131]\n\t"
      "v_mfma_f32_16x16x32_bf16 a[148:151], v[168:171], v[192:195], a[148:151]\n\t"
      "buffer_load_dwordx4 v[128:131], v0, s[36:39], s48 offen offset:1024 sc1\n\t"
      "v_mfma_f32_16x16x32_bf16 a[92:95], v[172:175], v[180:183], a[92:95]\n\t"
      "v_mfma_f32_16x16x32_bf16 a[112:115], v[172:175], v[184:187], a[112:115]\n\t"
      "buffer_load_dwordx4 v[132:135], v0, s[36:39], s48 offen offset:2048 sc1\n\t"
      "v_mfma_f32_16x16x32_bf16 a[132:135], v[172:175], v[188:191], a[132:135]\n\t"
      "v_mfma_f32_16x16x32_bf16 a[152:155], v[172:175], v[192:195], a[152:155]\n\t"
      "buffer_load_dwordx4 v[136:139], v0, s[36:39], s48 offen offset:3072 sc1\n\t"
      "v_mfma_f32_16x16x32_bf16 a[96:99], v[176:179], v[180:183], a[96:99]\n\t"
      "v_mfma_f32_16x16x32_bf16 a[116:119], v[176:179], v[184:187], a[116:119]\n\t"
      "buffer_load_dwordx4 v[140:143], v1, s[36:39], s48 offen sc1\n\t"
      "v_mfma_f32_16x16x32_bf16 a[136:139], v[176:179], v[188:191], a[136:139]\n\t"
      "v_mfma_f32_16x16x32_bf16 a[156:159], v[176:179], v[192:195], a[156:159]\n\t"
      "s_add_u32 s50, s51, 0x64\n\t"
      "s_lshl_b32 s49, s50, 12\n\t"
      "s_add_u32 s49, s49, s53\n\t"
      "s_add_u32 s50, s50, 54\n\t"
      "s_mul_i32 s48, s50, 0x1400\n\t"
      "s_add_u32 s48, s48, s54\n\t"
      "s_waitcnt vmcnt(27)\n\t"
      "v_mfma_f32_16x16x32_bf16 a[80:83], v[16:19], v[36:39], a[80:83]\n\t"
      "v_mfma_f32_16x16x32_bf16 a[100:103], v[16:19], v[40:43], a[100:103]\n\t"
      "buffer_load_dwordx4 v[180:183], v0, s[36:39], s49 offen sc1\n\t"
      "v_mfma_f32_16x16x32_bf16 a[120:123], v[16:19], v[44:47], a[120:123]\n\t"
      "v_mfma_f32_16x16x32_bf16 a[140:143], v[16:19], v[48:51], a[140:143]\n\t"
      "buffer_load_dwordx4 v[184:187], v0, s[36:39], s49 offen offset:1024 sc1\n\t"
      "v_mfma_f32_16x16x32_bf16 a[84:87], v[20:23], v[36:39], a[84:87]\n\t"
      "v_mfma_f32_16x16x32_bf16 a[104:107], v[20:23], v[40:43], a[104:107]\n\t"
      "buffer_load_dwordx4 v[188:191], v0, s[36:39], s49 offen offset:2048 sc1\n\t"
      "v_mfma_f32_16x16x32_bf16 a[124:127], v[20:23], v[44:47], a[124:127]\n\t"
      "v_mfma_f32_16x16x32_bf16 a[144:147], v[20:23], v[48:51], a[144:147]\n\t"
      "buffer_load_dwordx4 v[192:195], v0, s[36:39], s49 offen offset:3072 sc1\n\t"
      "v_mfma_f32_16x16x32_bf16 a[88:91], v[24:27], v[36:39], a[88:91]\n\t"
      "v_mfma_f32_16x16x32_bf16 a[108:111], v[24:27], v[40:43], a[108:111]\n\t"
      "buffer_load_dwordx4 v[160:163], v0, s[36:39], s48 offen sc1\n\t"
      "v_mfma_f32_16x16x32_bf16 a[128:131], v[24:27], v[44:47], a[128:131]\n\t"
      "v_mfma_f32_16x16x32_bf16 a[148:151], v[24:27], v[48:51], a[148:151]\n\t"
      "buffer_load_dwordx4 v[164:167], v0, s[36:39], s48 offen offset:1024 sc1\n\t"
      "v_mfma_f32_16x16x32_bf16 a[92:95], v[28:31], v[36:39], a[92:95]\n\t"
      "v_mfma_f32_16x16x32_bf16 a[112:115], v[28:31], v[40:43], a[112:115]\n\t"
      "buffer_load_dwordx4 v[168:171], v0, s[36:39], s48 offen offset:2048 sc1\n\t"
      "v_mfma_f32_16x16x32_bf16 a[132:135], v[28:31], v[44:47], a[132:135]\n\t"
      "v_mfma_f32_16x16x32_bf16 a[152:155], v[28:31], v[48:51], a[152:155]\n\t"
      "buffer_load_dwordx4 v[172:175], v0, s[36:39], s48 offen offset:3072 sc1\n\t"
      "v_mfma_f32_16x16x32_bf16 a[96:99], v[32:35], v[36:39], a[96:99]\n\t"
      "v_mfma_f32_16x16x32_bf16 a[116:119], v[32:35], v[40:43], a[116:119]\n\t"
      "buffer_load_dwordx4 v[176:179], v1, s[36:39], s48 offen sc1\n\t"
      "v_mfma_f32_16x16x32_bf16 a[136:139], v[32:35], v[44:47], a[136:139]\n\t"
      "v_mfma_f32_16x16x32_bf16 a[156:159], v[32:35], v[48:51], a[156:159]\n\t"
      "s_add_u32 s50, s51, 0x68\n\t"
      "s_lshl_b32 s49, s50, 12\n\t"
      "s_add_u32 s49, s49, s53\n\t"
      "s_add_u32 s50, s50, 54\n\t"
      "s_mul_i32 s48, s50, 0x1400\n\t"
      "s_add_u32 s48, s48, s54\n\t"
      "s_waitcnt vmcnt(27)\n\t"
      "v_mfma_f32_16x16x32_bf16 a[80:83], v[52:55], v[72:75], a[80:83]\n\t"
      "v_mfma_f32_16x16x32_bf16 a[100:103], v[52:55], v[76:79], a[100:103]\n\t"
      "buffer_load_dwordx4 v[36:39], v0, s[36:39], s49 offen sc1\n\t"
      "v_mfma_f32_16x16x32_bf16 a[120:123], v[52:55], v[80:83], a[120:123]\n\t"
      "v_mfma_f32_16x16x32_bf16 a[140:143], v[52:55], v[84:87], a[140:143]\n\t"
      "buffer_load_dwordx4 v[40:43], v0, s[36:39], s49 offen offset:1024 sc1\n\t"
      "v_mfma_f32_16x16x32_bf16 a[84:87], v[56:59], v[72:75], a[84:87]\n\t"
      "v_mfma_f32_16x16x32_bf16 a[104:107], v[56:59], v[76:79], a[104:107]\n\t"
      "buffer_load_dwordx4 v[44:47], v0, s[36:39], s49 offen offset:2048 sc1\n\t"
      "v_mfma_f32_16x16x32_bf16 a[124:127], v[56:59], v[80:83], a[124:127]\n\t"
      "v_mfma_f32_16x16x32_bf16 a[144:147], v[56:59], v[84:87], a[144:147]\n\t"
      "buffer_load_dwordx4 v[48:51], v0, s[36:39], s49 offen offset:3072 sc1\n\t"
      "v_mfma_f32_16x16x32_bf16 a[88:91], v[60:63], v[72:75], a[88:91]\n\t"
      "v_mfma_f32_16x16x32_bf16 a[108:111], v[60:63], v[76:79], a[108:111]\n\t"
      "buffer_load_dwordx4 v[16:19], v0, s[36:39], s48 offen sc1\n\t"
      "v_mfma_f32_16x16x32_bf16 a[128:131], v[60:63], v[80:83], a[128:131]\n\t"
      "v_mfma_f32_16x16x32_bf16 a[148:151], v[60:63], v[84:87], a[148:151]\n\t"
      "buffer_load_dwordx4 v[20:23], v0, s[36:39], s48 offen offset:1024 sc1\n\t"
      "v_mfma_f32_16x16x32_bf16 a[92:95], v[64:67], v[72:75], a[92:95]\n\t"
      "v_mfma_f32_16x16x32_bf16 a[112:115], v[64:67], v[76:79], a[112:115]\n\t"
      "buffer_load_dwordx4 v[24:27], v0, s[36:39], s48 offen offset:2048 sc1\n\t"
      "v_mfma_f32_16x16x32_bf16 a[132:135], v[64:67], v[80:83], a[132:135]\n\t"
      "v_mfma_f32_16x16x32_bf16 a[152:155], v[64:67], v[84:87], a[152:155]\n\t"
      "buffer_load_dwordx4 v[28:31], v0, s[36:39], s48 offen offset:3072 sc1\n\t"
      "v_mfma_f32_16x16x32_bf16 a[96:99], v[68:71], v[72:75], a[96:99]\n\t"
      "v_mfma_f32_16x16x32_bf16 a[116:119], v[68:71], v[76:79], a[116:119]\n\t"
      "buffer_load_dwordx4 v[32:35], v1, s[36:39], s48 offen sc1\n\t"
      "v_mfma_f32_16x16x32_bf16 a[136:139], v[68:71], v[80:83], a[136:139]\n\t"
      "v_mfma_f32_16x16x32_bf16 a[156:159], v[68:71], v[84:87], a[156:159]\n\t"
      "s_add_u32 s50, s51, 0x6c\n\t"
      "s_lshl_b32 s49, s50, 12\n\t"
      "s_add_u32 s49, s49, s53\n\t"
      "s_add_u32 s50, s50, 54\n\t"
      "s_mul_i32 s48, s50, 0x1400\n\t"
      "s_add_u32 s48, s48, s54\n\t"
      "s_waitcnt vmcnt(27)\n\t"
      "v_mfma_f32_16x16x32_bf16 a[80:83], v[88:91], v[108:111], a[80:83]\n\t"
      "v_mfma_f32_16x16x32_bf16 a[100:103], v[88:91], v[112:115], a[100:103]\n\t"
      "buffer_load_dwordx4 v[72:75], v0, s[36:39], s49 offen sc1\n\t"
      "v_mfma_f32_16x16x32_bf16 a[120:123], v[88:91], v[116:119], a[120:123]\n\t"
      "v_mfma_f32_16x16x32_bf16 a[140:143], v[88:91], v[120:123], a[140:143]\n\t"
      "buffer_load_dwordx4 v[76:79], v0, s[36:39], s49 offen offset:1024 sc1\n\t"
      "v_mfma_f32_16x16x32_bf16 a[84:87], v[92:95], v[108:111], a[84:87]\n\t"
      "v_mfma_f32_16x16x32_bf16 a[104:107], v[92:95], v[112:115], a[104:107]\n\t"
      "buffer_load_dwordx4 v[80:83], v0, s[36:39], s49 offen offset:2048 sc1\n\t"
      "v_mfma_f32_16x16x32_bf16 a[124:127], v[92:95], v[116:119], a[124:127]\n\t"
      "v_mfma_f32_16x16x32_bf16 a[144:147], v[92:95], v[120:123], a[144:147]\n\t"
      "buffer_load_dwordx4 v[84:87], v0, s[36:39], s49 offen offset:3072 sc1\n\t"
      "v_mfma_f32_16x16x32_bf16 a[88:91], v[96:99], v[108:111], a[88:91]\n\t"
      "v_mfma_f32_16x16x32_bf16 a[108:111], v[96:99], v[112:115], a[108:111]\n\t"
      "buffer_load_dwordx4 v[52:55], v0, s[36:39], s48 offen sc1\n\t"
      "v_mfma_f32_16x16x32_bf16 a[128:131], v[96:99], v[116:119], a[128:131]\n\t"
      "v_mfma_f32_16x16x32_bf16 a[148:151], v[96:99], v[120:123], a[148:151]\n\t"
      "buffer_load_dwordx4 v[56:59], v0, s[36:39], s48 offen offset:1024 sc1\n\t"
      "v_mfma_f32_16x16x32_bf16 a[92:95], v[100:103], v[108:111], a[92:95]\n\t"
      "v_mfma_f32_16x16x32_bf16 a[112:115], v[100:103], v[112:115], a[112:115]\n\t"
      "buffer_load_dwordx4 v[60:63], v0, s[36:39], s48 offen offset:2048 sc1\n\t"
      "v_mfma_f32_16x16x32_bf16 a[132:135], v[100:103], v[116:119], a[132:135]\n\t"
      "v_mfma_f32_16x16x32_bf16 a[152:155], v[100:103], v[120:123], a[152:155]\n\t"
      "buffer_load_dwordx4 v[64:67], v0, s[36:39], s48 offen offset:3072 sc1\n\t"
      "v_mfma_f32_16x16x32_bf16 a[96:99], v[104:107], v[108:111], a[96:99]\n\t"
      "v_mfma_f32_16x16x32_bf16 a[116:119], v[104:107], v[112:115], a[116:119]\n\t"
      "buffer_load_dwordx4 v[68:71], v1, s[36:39], s48 offen sc1\n\t"
      "v_mfma_f32_16x16x32_bf16 a[136:139], v[104:107], v[116:119], a[136:139]\n\t"
      "v_mfma_f32_16x16x32_bf16 a[156:159], v[104:107], v[120:123], a[156:159]\n\t"
      "s_add_u32 s50, s51, 0x70\n\t"
      "s_lshl_b32 s49, s50, 12\n\t"
      "s_add_u32 s49, s49, s53\n\t"
      "s_add_u32 s50, s50, 54\n\t"
      "s_mul_i32 s48, s50, 0x1400\n\t"
      "s_add_u32 s48, s48, s54\n\t"
      "s_waitcnt vmcnt(27)\n\t"
      "v_mfma_f32_16x16x32_bf16 a[80:83], v[124:127], v[144:147], a[80:83]\n\t"
      "v_mfma_f32_16x16x32_bf16 a[100:103], v[124:127], v[148:151], a[100:103]\n\t"
      "buffer_load_dwordx4 v[108:111], v0, s[36:39], s49 offen sc1\n\t"
      "v_mfma_f32_16x16x32_bf16 a[120:123], v[124:127], v[152:155], a[120:123]\n\t"
      "v_mfma_f32_16x16x32_bf16 a[140:143], v[124:127], v[156:159], a[140:143]\n\t"
      "buffer_load_dwordx4 v[112:115], v0, s[36:39], s49 offen offset:1024 sc1\n\t"
      "v_mfma_f32_16x16x32_bf16 a[84:87], v[128:131], v[144:147], a[84:87]\n\t"
      "v_mfma_f32_16x16x32_bf16 a[104:107], v[128:131], v[148:151], a[104:107]\n\t"
      "buffer_load_dwordx4 v[116:119], v0, s[36:39], s49 offen offset:2048 sc1\n\t"
      "v_mfma_f32_16x16x32_bf16 a[124:127], v[128:131], v[152:155], a[124:127]\n\t"
      "v_mfma_f32_16x16x32_bf16 a[144:147], v[128:131], v[156:159], a[144:147]\n\t"
      "buffer_load_dwordx4 v[120:123], v0, s[36:39], s49 offen offset:3072 sc1\n\t"
      "v_mfma_f32_16x16x32_bf16 a[88:91], v[132:135], v[144:147], a[88:91]\n\t"
      "v_mfma_f32_16x16x32_bf16 a[108:111], v[132:135], v[148:151], a[108:111]\n\t"
      "buffer_load_dwordx4 v[88:91], v0, s[36:39], s48 offen sc1\n\t"
      "v_mfma_f32_16x16x32_bf16 a[128:131], v[132:135], v[152:155], a[128:131]\n\t"
      "v_mfma_f32_16x16x32_bf16 a[148:151], v[132:135], v[156:159], a[148:151]\n\t"
      "buffer_load_dwordx4 v[92:95], v0, s[36:39], s48 offen offset:1024 sc1\n\t"
      "v_mfma_f32_16x16x32_bf16 a[92:95], v[136:139], v[144:147], a[92:95]\n\t"
      "v_mfma_f32_16x16x32_bf16 a[112:115], v[136:139], v[148:151], a[112:115]\n\t"
      "buffer_load_dwordx4 v[96:99], v0, s[36:39], s48 offen offset:2048 sc1\n\t"
      "v_mfma_f32_16x16x32_bf16 a[132:135], v[136:139], v[152:155], a[132:135]\n\t"
      "v_mfma_f32_16x16x32_bf16 a[152:155], v[136:139], v[156:159], a[152:155]\n\t"
      "buffer_load_dwordx4 v[100:103], v0, s[36:39], s48 offen offset:3072 sc1\n\t"
      "v_mfma_f32_16x16x32_bf16 a[96:99], v[140:143], v[144:147], a[96:99]\n\t"
      "v_mfma_f32_16x16x32_bf16 a[116:119], v[140:143], v[148:151], a[116:119]\n\t"
      "buffer_load_dwordx4 v[104:107], v1, s[36:39], s48 offen sc1\n\t"
      "v_mfma_f32_16x16x32_bf16 a[136:139], v[140:143], v[152:155], a[136:139]\n\t"
      "v_mfma_f32_16x16x32_bf16 a[156:159], v[140:143], v[156:159], a[156:159]\n\t"
      "s_add_u32 s50, s51, 0x74\n\t"
      "s_lshl_b32 s49, s50, 12\n\t"
      "s_add_u32 s49, s49, s53\n\t"
      "s_add_u32 s50, s50, 54\n\t"
      "s_mul_i32 s48, s50, 0x1400\n\t"
      "s_add_u32 s48, s48, s54\n\t"
      "s_waitcnt vmcnt(27)\n\t"
      "v_mfma_f32_16x16x32_bf16 a[80:83], v[160:163], v[180:183], a[80:83]\n\t"
      "v_mfma_f32_16x16x32_bf16 a[100:103], v[160:163], v[184:187], a[100:103]\n\t"
      "buffer_load_dwordx4 v[144:147], v0, s[36:39], s49 offen sc1\n\t"
      "v_mfma_f32_16x16x32_bf16 a[120:123], v[160:163], v[188:191], a[120:123]\n\t"
      "v_mfma_f32_16x16x32_bf16 a[140:143], v[160:163], v[192:195], a[140:143]\n\t"
      "buffer_load_dwordx4 v[148:151], v0, s[36:39], s49 offen offset:1024 sc1\n\t"
      "v_mfma_f32_16x16x32_bf16 a[84:87], v[164:167], v[180:183], a[84:87]\n\t"
      "v_mfma_f32_16x16x32_bf16 a[104:107], v[164:167], v[184:187], a[104:107]\n\t"
      "buffer_load_dwordx4 v[152:155], v0, s[36:39], s49 offen offset:2048 sc1\n\t"
      "v_mfma_f32_16x16x32_bf16 a[124:127], v[164:167], v[188:191], a[124:127]\n\t"
      "v_mfma_f32_16x16x32_bf16 a[144:147], v[164:167], v[192:195], a[144:147]\n\t"
      "buffer_load_dwordx4 v[156:159], v0, s[36:39], s49 offen offset:3072 sc1\n\t"
      "v_mfma_f32_16x16x32_bf16 a[88:91], v[168:171], v[180:183], a[88:91]\n\t"
      "v_mfma_f32_16x16x32_bf16 a[108:111], v[168:171], v[184:187], a[108:111]\n\t"
      "buffer_load_dwordx4 v[124:127], v0, s[36:39], s48 offen sc1\n\t"
      "v_mfma_f32_16x16x32_bf16 a[128:131], v[168:171], v[188:191], a[128:131]\n\t"
      "v_mfma_f32_16x16x32_bf16 a[148:151], v[168:171], v[192:195], a[148:151]\n\t"
      "buffer_load_dwordx4 v[128:131], v0, s[36:39], s48 offen offset:1024 sc1\n\t"
      "v_mfma_f32_16x16x32_bf16 a[92:95], v[172:175], v[180:183], a[92:95]\n\t"
      "v_mfma_f32_16x16x32_bf16 a[112:115], v[172:175], v[184:187], a[112:115]\n\t"
      "buffer_load_dwordx4 v[132:135], v0, s[36:39], s48 offen offset:2048 sc1\n\t"
      "v_mfma_f32_16x16x32_bf16 a[132:135], v[172:175], v[188:191], a[132:135]\n\t"
      "v_mfma_f32_16x16x32_bf16 a[152:155], v[172:175], v[192:195], a[152:155]\n\t"
      "buffer_load_dwordx4 v[136:139], v0, s[36:39], s48 offen offset:3072 sc1\n\t"
      "v_mfma_f32_16x16x32_bf16 a[96:99], v[176:179], v[180:183], a[96:99]\n\t"
      "v_mfma_f32_16x16x32_bf16 a[116:119], v[176:179], v[184:187], a[116:119]\n\t"
      "buffer_load_dwordx4 v[140:143], v1, s[36:39], s48 offen sc1\n\t"
      "v_mfma_f32_16x16x32_bf16 a[136:139], v[176:179], v[188:191], a[136:139]\n\t"
      "v_mfma_f32_16x16x32_bf16 a[156:159], v[176:179], v[192:195], a[156:159]\n\t"
      "s_add_u32 s50, s51, 0x78\n\t"
      "s_lshl_b32 s49, s50, 12\n\t"
      "s_add_u32 s49, s49, s53\n\t"
      "s_add_u32 s50, s50, 54\n\t"
      "s_mul_i32 s48, s50, 0x1400\n\t"
      "s_add_u32 s48, s48, s54\n\t"
      "s_waitcnt vmcnt(27)\n\t"
      "v_mfma_f32_16x16x32_bf16 a[80:83], v[16:19], v[36:39], a[80:83]\n\t"
      "v_mfma_f32_16x16x32_bf16 a[100:103], v[16:19], v[40:43], a[100:103]\n\t"
      "buffer_load_dwordx4 v[180:183], v0, s[36:39], s49 offen sc1\n\t"
      "v_mfma_f32_16x16x32_bf16 a[120:123], v[16:19], v[44:47], a[120:123]\n\t"
      "v_mfma_f32_16x16x32_bf16 a[140:143], v[16:19], v[48:51], a[140:143]\n\t"
      "buffer_load_dwordx4 v[184:187], v0, s[36:39], s49 offen offset:1024 sc1\n\t"
      "v_mfma_f32_16x16x32_bf16 a[84:87], v[20:23], v[36:39], a[84:87]\n\t"
      "v_mfma_f32_16x16x32_bf16 a[104:107], v[20:23], v[40:43], a[104:107]\n\t"
      "buffer_load_dwordx4 v[188:191], v0, s[36:39], s49 offen offset:2048 sc1\n\t"
      "v_mfma_f32_16x16x32_bf16 a[124:127], v[20:23], v[44:47], a[124:127]\n\t"
      "v_mfma_f32_16x16x32_bf16 a[144:147], v[20:23], v[48:51], a[144:147]\n\t"
      "buffer_load_dwordx4 v[192:195], v0, s[36:39], s49 offen offset:3072 sc1\n\t"
      "v_mfma_f32_16x16x32_bf16 a[88:91], v[24:27], v[36:39], a[88:91]\n\t"
      "v_mfma_f32_16x16x32_bf16 a[108:111], v[24:27], v[40:43], a[108:111]\n\t"
      "buffer_load_dwordx4 v[160:163], v0, s[36:39], s48 offen sc1\n\t"
      "v_mfma_f32_16x16x32_bf16 a[128:131], v[24:27], v[44:47], a[128:131]\n\t"
      "v_mfma_f32_16x16x32_bf16 a[148:151], v[24:27], v[48:51], a[148:151]\n\t"
      "buffer_load_dwordx4 v[164:167], v0, s[36:39], s48 offen offset:1024 sc1\n\t"
      "v_mfma_f32_16x16x32_bf16 a[92:95], v[28:31], v[36:39], a[92:95]\n\t"
      "v_mfma_f32_16x16x32_bf16 a[112:115], v[28:31], v[40:43], a[112:115]\n\t"
      "buffer_load_dwordx4 v[168:171], v0, s[36:39], s48 offen offset:2048 sc1\n\t"
      "v_mfma_f32_16x16x32_bf16 a[132:135], v[28:31], v[44:47], a[132:135]\n\t"
      "v_mfma_f32_16x16x32_bf16 a[152:155], v[28:31], v[48:51], a[152:155]\n\t"
      "buffer_load_dwordx4 v[172:175], v0, s[36:39], s48 offen offset:3072 sc1\n\t"
      "v_mfma_f32_16x16x32_bf16 a[96:99], v[32:35], v[36:39], a[96:99]\n\t"
      "v_mfma_f32_16x16x32_bf16 a[116:119], v[32:35], v[40:43], a[116:119]\n\t"
      "buffer_load_dwordx4 v[176:179], v1, s[36:39], s48 offen sc1\n\t"
      "v_mfma_f32_16x16x32_bf16 a[136:139], v[32:35], v[44:47], a[136:139]\n\t"
      "v_mfma_f32_16x16x32_bf16 a[156:159], v[32:35], v[48:51], a[156:159]\n\t"
      "s_add_u32 s50, s51, 0x7c\n\t"
      "s_lshl_b32 s49, s50, 12\n\t"
      "s_add_u32 s49, s49, s53\n\t"
      "s_add_u32 s50, s50, 54\n\t"
      "s_mul_i32 s48, s50, 0x1400\n\t"
      "s_add_u32 s48, s48, s54\n\t"
      "s_waitcnt vmcnt(27)\n\t"
      "v_mfma_f32_16x16x32_bf16 a[80:83], v[52:55], v[72:75], a[80:83]\n\t"
      "v_mfma_f32_16x16x32_bf16 a[100:103], v[52:55], v[76:79], a[100:103]\n\t"
      "buffer_load_dwordx4 v[36:39], v0, s[36:39], s49 offen sc1\n\t"
      "v_mfma_f32_16x16x32_bf16 a[120:123], v[52:55], v[80:83], a[120:123]\n\t"
      "v_mfma_f32_16x16x32_bf16 a[140:143], v[52:55], v[84:87], a[140:143]\n\t"
      "buffer_load_dwordx4 v[40:43], v0, s[36:39], s49 offen offset:1024 sc1\n\t"
      "v_mfma_f32_16x16x32_bf16 a[84:87], v[56:59], v[72:75], a[84:87]\n\t"
      "v_mfma_f32_16x16x32_bf16 a[104:107], v[56:59], v[76:79], a[104:107]\n\t"
      "buffer_load_dwordx4 v[44:47], v0, s[36:39], s49 offen offset:2048 sc1\n\t"
      "v_mfma_f32_16x16x32_bf16 a[124:127], v[56:59], v[80:83], a[124:127]\n\t"
      "v_mfma_f32_16x16x32_bf16 a[144:147], v[56:59], v[84:87], a[144:147]\n\t"
      "buffer_load_dwordx4 v[48:51], v0, s[36:39], s49 offen offset:3072 sc1\n\t"
      "v_mfma_f32_16x16x32_bf16 a[88:91], v[60:63], v[72:75], a[88:91]\n\t"
      "v_mfma_f32_16x16x32_bf16 a[108:111], v[60:63], v[76:79], a[108:111]\n\t"
      "buffer_load_dwordx4 v[16:19], v0, s[36:39], s48 offen sc1\n\t"
      "v_mfma_f32_16x16x32_bf16 a[128:131], v[60:63], v[80:83], a[128:131]\n\t"
      "v_mfma_f32_16x16x32_bf16 a[148:151], v[60:63], v[84:87], a[148:151]\n\t"
      "buffer_load_dwordx4 v[20:23], v0, s[36:39], s48 offen offset:1024 sc1\n\t"
      "v_mfma_f32_16x16x32_bf16 a[92:95], v[64:67], v[72:75], a[92:95]\n\t"
      "v_mfma_f32_16x16x32_bf16 a[112:115], v[64:67], v[76:79], a[112:115]\n\t"
      "buffer_load_dwordx4 v[24:27], v0, s[36:39], s48 offen offset:2048 sc1\n\t"
      "v_mfma_f32_16x16x32_bf16 a[132:135], v[64:67], v[80:83], a[132:135]\n\t"
      "v_mfma_f32_16x16x32_bf16 a[152:155], v[64:67], v[84:87], a[152:155]\n\t"
      "buffer_load_dwordx4 v[28:31], v0, s[36:39], s48 offen offset:3072 sc1\n\t"
      "v_mfma_f32_16x16x32_bf16 a[96:99], v[68:71], v[72:75], a[96:99]\n\t"
      "v_mfma_f32_16x16x32_bf16 a[116:119], v[68:71], v[76:79], a[116:119]\n\t"
      "buffer_load_dwordx4 v[32:35], v1, s[36:39], s48 offen sc1\n\t"
      "v_mfma_f32_16x16x32_bf16 a[136:139], v[68:71], v[80:83], a[136:139]\n\t"
      "v_mfma_f32_16x16x32_bf16 a[156:159], v[68:71], v[84:87], a[156:159]\n\t"
      "s_waitcnt vmcnt(27)\n\t"
      "v_mfma_f32_16x16x32_bf16 a[80:83], v[88:91], v[108:111], a[80:83]\n\t"
      "v_mfma_f32_16x16x32_bf16 a[100:103], v[88:91], v[112:115], a[100:103]\n\t"
      "v_mfma_f32_16x16x32_bf16 a[120:123], v[88:91], v[116:119], a[120:123]\n\t"
      "v_mfma_f32_16x16x32_bf16 a[140:143], v[88:91], v[120:123], a[140:143]\n\t"
      "v_mfma_f32_16x16x32_bf16 a[84:87], v[92:95], v[108:111], a[84:87]\n\t"
      "v_mfma_f32_16x16x32_bf16 a[104:107], v[92:95], v[112:115], a[104:107]\n\t"
      "v_mfma_f32_16x16x32_bf16 a[124:127], v[92:95], v[116:119], a[124:127]\n\t"
      "v_mfma_f32_16x16x32_bf16 a[144:147], v[92:95], v[120:123], a[144:147]\n\t"
      "v_mfma_f32_16x16x32_bf16 a[88:91], v[96:99], v[108:111], a[88:91]\n\t"
      "v_mfma_f32_16x16x32_bf16 a[108:111], v[96:99], v[112:115], a[108:111]\n\t"
      "v_mfma_f32_16x16x32_bf16 a[128:131], v[96:99], v[116:119], a[128:131]\n\t"
      "v_mfma_f32_16x16x32_bf16 a[148:151], v[96:99], v[120:123], a[148:151]\n\t"
      "v_mfma_f32_16x16x32_bf16 a[92:95], v[100:103], v[108:111], a[92:95]\n\t"
      "v_mfma_f32_16x16x32_bf16 a[112:115], v[100:103], v[112:115], a[112:115]\n\t"
      "v_mfma_f32_16x16x32_bf16 a[132:135], v[100:103], v[116:119], a[132:135]\n\t"
      "v_mfma_f32_16x16x32_bf16 a[152:155], v[100:103], v[120:123], a[152:155]\n\t"
      "v_mfma_f32_16x16x32_bf16 a[96:99], v[104:107], v[108:111], a[96:99]\n\t"
      "v_mfma_f32_16x16x32_bf16 a[116:119], v[104:107], v[112:115], a[116:119]\n\t"
      "v_mfma_f32_16x16x32_bf16 a[136:139], v[104:107], v[116:119], a[136:139]\n\t"
      "v_mfma_f32_16x16x32_bf16 a[156:159], v[104:107], v[120:123], a[156:159]\n\t"
      "s_waitcnt vmcnt(18)\n\t"
      "v_mfma_f32_16x16x32_bf16 a[80:83], v[124:127], v[144:147], a[80:83]\n\t"
      "v_mfma_f32_16x16x32_bf16 a[100:103], v[124:127], v[148:151], a[100:103]\n\t"
      "v_mfma_f32_16x16x32_bf16 a[120:123], v[124:127], v[152:155], a[120:123]\n\t"
      "v_mfma_f32_16x16x32_bf16 a[140:143], v[124:127], v[156:159], a[140:143]\n\t"
      "v_mfma_f32_16x16x32_bf16 a[84:87], v[128:131], v[144:147], a[84:87]\n\t"
      "v_mfma_f32_16x16x32_bf16 a[104:107], v[128:131], v[148:151], a[104:107]\n\t"
      "v_mfma_f32_16x16x32_bf16 a[124:127], v[128:131], v[152:155], a[124:127]\n\t"
      "v_mfma_f32_16x16x32_bf16 a[144:147], v[128:131], v[156:159], a[144:147]\n\t"
      "v_mfma_f32_16x16x32_bf16 a[88:91], v[132:135], v[144:147], a[88:91]\n\t"
      "v_mfma_f32_16x16x32_bf16 a[108:111], v[132:135], v[148:151], a[108:111]\n\t"
      "v_mfma_f32_16x16x32_bf16 a[128:131], v[132:135], v[152:155], a[128:131]\n\t"
      "v_mfma_f32_16x16x32_bf16 a[148:151], v[132:135], v[156:159], a[148:151]\n\t"
      "v_mfma_f32_16x16x32_bf16 a[92:95], v[136:139], v[144:147], a[92:95]\n\t"
      "v_mfma_f32_16x16x32_bf16 a[112:115], v[136:139], v[148:151], a[112:115]\n\t"
      "v_mfma_f32_16x16x32_bf16 a[132:135], v[136:139], v[152:155], a[132:135]\n\t"
      "v_mfma_f32_16x16x32_bf16 a[152:155], v[136:139], v[156:159], a[152:155]\n\t"
      "v_mfma_f32_16x16x32_bf16 a[96:99], v[140:143], v[144:147], a[96:99]\n\t"
      "v_mfma_f32_16x16x32_bf16 a[116:119], v[140:143], v[148:151], a[116:119]\n\t"
      "v_mfma_f32_16x16x32_bf16 a[136:139], v[140:143], v[152:155], a[136:139]\n\t"
      "v_mfma_f32_16x16x32_bf16 a[156:159], v[140:143], v[156:159], a[156:159]\n\t"
      "s_waitcnt vmcnt(9)\n\t"
      "v_mfma_f32_16x16x32_bf16 a[80:83], v[160:163], v[180:183], a[80:83]\n\t"
      "v_mfma_f32_16x16x32_bf16 a[100:103], v[160:163], v[184:187], a[100:103]\n\t"
      "v_mfma_f32_16x16x32_bf16 a[120:123], v[160:163], v[188:191], a[120:123]\n\t"
      "v_mfma_f32_16x16x32_bf16 a[140:143], v[160:163], v[192:195], a[140:143]\n\t"
      "v_mfma_f32_16x16x32_bf16 a[84:87], v[164:167], v[180:183], a[84:87]\n\t"
      "v_mfma_f32_16x16x32_bf16 a[104:107], v[164:167], v[184:187], a[104:107]\n\t"
      "v_mfma_f32_16x16x32_bf16 a[124:127], v[164:167], v[188:191], a[124:127]\n\t"
      "v_mfma_f32_16x16x32_bf16 a[144:147], v[164:167], v[192:195], a[144:147]\n\t"
      "v_mfma_f32_16x16x32_bf16 a[88:91], v[168:171], v[180:183], a[88:91]\n\t"
      "v_mfma_f32_16x16x32_bf16 a[108:111], v[168:171], v[184:187], a[108:111]\n\t"
      "v_mfma_f32_16x16x32_bf16 a[128:131], v[168:171], v[188:191], a[128:131]\n\t"
      "v_mfma_f32_16x16x32_bf16 a[148:151], v[168:171], v[192:195], a[148:151]\n\t"
      "v_mfma_f32_16x16x32_bf16 a[92:95], v[172:175], v[180:183], a[92:95]\n\t"
      "v_mfma_f32_16x16x32_bf16 a[112:115], v[172:175], v[184:187], a[112:115]\n\t"
      "v_mfma_f32_16x16x32_bf16 a[132:135], v[172:175], v[188:191], a[132:135]\n\t"
      "v_mfma_f32_16x16x32_bf16 a[152:155], v[172:175], v[192:195], a[152:155]\n\t"
      "v_mfma_f32_16x16x32_bf16 a[96:99], v[176:179], v[180:183], a[96:99]\n\t"
      "v_mfma_f32_16x16x32_bf16 a[116:119], v[176:179], v[184:187], a[116:119]\n\t"
      "v_mfma_f32_16x16x32_bf16 a[136:139], v[176:179], v[188:191], a[136:139]\n\t"
      "v_mfma_f32_16x16x32_bf16 a[156:159], v[176:179], v[192:195], a[156:159]\n\t"
      "s_waitcnt vmcnt(0)\n\t"
      "v_mfma_f32_16x16x32_bf16 a[80:83], v[16:19], v[36:39], a[80:83]\n\t"
      "v_mfma_f32_16x16x32_bf16 a[100:103], v[16:19], v[40:43], a[100:103]\n\t"
      "v_mfma_f32_16x16x32_bf16 a[120:123], v[16:19], v[44:47], a[120:123]\n\t"
      "v_mfma_f32_16x16x32_bf16 a[140:143], v[16:19], v[48:51], a[140:143]\n\t"
      "v_mfma_f32_16x16x32_bf16 a[84:87], v[20:23], v[36:39], a[84:87]\n\t"
      "v_mfma_f32_16x16x32_bf16 a[104:107], v[20:23], v[40:43], a[104:107]\n\t"
      "v_mfma_f32_16x16x32_bf16 a[124:127], v[20:23], v[44:47], a[124:127]\n\t"
      "v_mfma_f32_16x16x32_bf16 a[144:147], v[20:23], v[48:51], a[144:147]\n\t"
      "v_mfma_f32_16x16x32_bf16 a[88:91], v[24:27], v[36:39], a[88:91]\n\t"
      "v_mfma_f32_16x16x32_bf16 a[108:111], v[24:27], v[40:43], a[108:111]\n\t"
      "v_mfma_f32_16x16x32_bf16 a[128:131], v[24:27], v[44:47], a[128:131]\n\t"
      "v_mfma_f32_16x16x32_bf16 a[148:151], v[24:27], v[48:51], a[148:151]\n\t"
      "v_mfma_f32_16x16x32_bf16 a[92:95], v[28:31], v[36:39], a[92:95]\n\t"
      "v_mfma_f32_16x16x32_bf16 a[112:115], v[28:31], v[40:43], a[112:115]\n\t"
      "v_mfma_f32_16x16x32_bf16 a[132:135], v[28:31], v[44:47], a[132:135]\n\t"
      "v_mfma_f32_16x16x32_bf16 a[152:155], v[28:31], v[48:51], a[152:155]\n\t"
      "v_mfma_f32_16x16x32_bf16 a[96:99], v[32:35], v[36:39], a[96:99]\n\t"
      "v_mfma_f32_16x16x32_bf16 a[116:119], v[32:35], v[40:43], a[116:119]\n\t"
      "v_mfma_f32_16x16x32_bf16 a[136:139], v[32:35], v[44:47], a[136:139]\n\t"
      "v_mfma_f32_16x16x32_bf16 a[156:159], v[32:35], v[48:51], a[156:159]\n\t"
      "s_nop 7\n\t"
      "s_nop 7\n\t"
      "v_add_u32 v10, 0x14000, v8\n\t"
      "v_add_u32 v11, 0x14000, v9\n\t"
      "v_add_u32 v12, 0x14000, v14\n\t"
      "ds_write_b128 v10, a[80:83]\n\t"
      "ds_write_b128 v10, a[84:87] offset:1024\n\t"
      "ds_write_b128 v10, a[88:91] offset:2048\n\t"
      "ds_write_b128 v10, a[92:95] offset:3072\n\t"
      "ds_write_b128 v10, a[96:99] offset:4096\n\t"
      "ds_write_b128 v10, a[100:103] offset:20480\n\t"
      "ds_write_b128 v10, a[104:107] offset:21504\n\t"
      "ds_write_b128 v10, a[108:111] offset:22528\n\t"
      "ds_write_b128 v10, a[112:115] offset:23552\n\t"
      "ds_write_b128 v10, a[116:119] offset:24576\n\t"
      "ds_write_b128 v12, a[120:123]\n\t"
      "ds_write_b128 v12, a[124:127] offset:1024\n\t"
      "ds_write_b128 v12, a[128:131] offset:2048\n\t"
      "ds_write_b128 v12, a[132:135] offset:3072\n\t"
      "ds_write_b128 v12, a[136:139] offset:4096\n\t"
      "ds_write_b128 v12, a[140:143] offset:20480\n\t"
      "ds_write_b128 v12, a[144:147] offset:21504\n\t"
      "ds_write_b128 v12, a[148:151] offset:22528\n\t"
      "ds_write_b128 v12, a[152:155] offset:23552\n\t"
      "ds_write_b128 v12, a[156:159] offset:24576\n\t"
      "s_waitcnt lgkmcnt(0)\n\t"
      "s_barrier\n\t"
      "ds_read_b128 v[16:19], v11\n\t"
      "ds_read_b128 v[20:23], v11 offset:1024\n\t"
      "ds_read_b128 v[24:27], v11 offset:2048\n\t"
      "ds_read_b128 v[28:31], v11 offset:3072\n\t"
      "ds_read_b128 v[32:35], v11 offset:4096\n\t"
      "ds_read_b128 v[36:39], v11 offset:5120\n\t"
      "ds_read_b128 v[40:43], v11 offset:6144\n\t"
      "ds_read_b128 v[44:47], v11 offset:7168\n\t"
      "ds_read_b128 v[48:51], v11 offset:8192\n\t"
      "ds_read_b128 v[52:55], v11 offset:9216\n\t"
      "ds_read_b128 v[56:59], v11 offset:10240\n\t"
      "ds_read_b128 v[60:63], v11 offset:11264\n\t"
      "ds_read_b128 v[64:67], v11 offset:12288\n\t"
      "ds_read_b128 v[68:71], v11 offset:13312\n\t"
      "ds_read_b128 v[72:75], v11 offset:14336\n\t"
      "ds_read_b128 v[76:79], v11 offset:15360\n\t"
      "ds_read_b128 v[80:83], v11 offset:16384\n\t"
      "ds_read_b128 v[84:87], v11 offset:17408\n\t"
      "ds_read_b128 v[88:91], v11 offset:18432\n\t"
      "ds_read_b128 v[92:95], v11 offset:19456\n\t"
      "s_waitcnt lgkmcnt(10)\n\t"
      "v_pk_add_f32 v[16:17], v[16:17], v[36:37]\n\t"
      "v_pk_add_f32 v[18:19], v[18:19], v[38:39]\n\t"
      "v_pk_add_f32 v[20:21], v[20:21], v[40:41]\n\t"
      "v_pk_add_f32 v[22:23], v[22:23], v[42:43]\n\t"
      "v_pk_add_f32 v[24:25], v[24:25], v[44:45]\n\t"
      "v_pk_add_f32 v[26:27], v[26:27], v[46:47]\n\t"
      "v_pk_add_f32 v[28:29], v[28:29], v[48:49]\n\t"
      "v_pk_add_f32 v[30:31], v[30:31], v[50:51]\n\t"
      "v_pk_add_f32 v[32:33], v[32:33], v[52:53]\n\t"
      "v_pk_add_f32 v[34:35], v[34:35], v[54:55]\n\t"
      "s_waitcnt lgkmcnt(0)\n\t"
      "v_pk_add_f32 v[56:57], v[56:57], v[76:77]\n\t"
      "v_pk_add_f32 v[58:59], v[58:59], v[78:79]\n\t"
      "v_pk_add_f32 v[60:61], v[60:61], v[80:81]\n\t"
      "v_pk_add_f32 v[62:63], v[62:63], v[82:83]\n\t"
      "v_pk_add_f32 v[64:65], v[64:65], v[84:85]\n\t"
      "v_pk_add_f32 v[66:67], v[66:67], v[86:87]\n\t"
      "v_pk_add_f32 v[68:69], v[68:69], v[88:89]\n\t"
      "v_pk_add_f32 v[70:71], v[70:71], v[90:91]\n\t"
      "v_pk_add_f32 v[72:73], v[72:73], v[92:93]\n\t"
      "v_pk_add_f32 v[74:75], v[74:75], v[94:95]\n\t"
      "v_pk_add_f32 v[16:17], v[16:17], v[56:57]\n\t"
      "v_pk_add_f32 v[18:19], v[18:19], v[58:59]\n\t"
      "v_pk_add_f32 v[20:21], v[20:21], v[60:61]\n\t"
      "v_pk_add_f32 v[22:23], v[22:23], v[62:63]\n\t"
      "v_pk_add_f32 v[24:25], v[24:25], v[64:65]\n\t"
      "v_pk_add_f32 v[26:27], v[26:27], v[66:67]\n\t"
      "v_pk_add_f32 v[28:29], v[28:29], v[68:69]\n\t"
      "v_pk_add_f32 v[30:31], v[30:31], v[70:71]\n\t"
      "v_pk_add_f32 v[32:33], v[32:33], v[72:73]\n\t"
      "v_pk_add_f32 v[34:35], v[34:35], v[74:75]\n\t"
      "v_pk_fma_f32 v[216:217], v[16:17], v[196:197], v[216:217]\n\t"
      "v_pk_fma_f32 v[218:219], v[18:19], v[198:199], v[218:219]\n\t"
      "v_pk_fma_f32 v[220:221], v[20:21], v[200:201], v[220:221]\n\t"
      "v_pk_fma_f32 v[222:223], v[22:23], v[202:203], v[222:223]\n\t"
      "v_pk_fma_f32 v[224:225], v[24:25], v[204:205], v[224:225]\n\t"
      "v_pk_fma_f32 v[226:227], v[26:27], v[206:207], v[226:227]\n\t"
      "v_pk_fma_f32 v[228:229], v[28:29], v[208:209], v[228:229]\n\t"
      "v_pk_fma_f32 v[230:231], v[30:31], v[210:211], v[230:231]\n\t"
      "v_pk_fma_f32 v[232:233], v[32:33], v[212:213], v[232:233]\n\t"
      "v_pk_fma_f32 v[234:235], v[34:35], v[214:215], v[234:235]\n\t"
      "v_cvt_pk_bf16_f32 v236, v216, v217\n\t"
      "v_cvt_pk_bf16_f32 v237, v218, v219\n\t"
      "v_cvt_pk_bf16_f32 v238, v220, v221\n\t"
      "v_cvt_pk_bf16_f32 v239, v222, v223\n\t"
      "v_cvt_pk_bf16_f32 v240, v224, v225\n\t"
      "v_cvt_pk_bf16_f32 v241, v226, v227\n\t"
      "v_cvt_pk_bf16_f32 v242, v228, v229\n\t"
      "v_cvt_pk_bf16_f32 v243, v230, v231\n\t"
      "v_cvt_pk_bf16_f32 v244, v232, v233\n\t"
      "v_cvt_pk_bf16_f32 v245, v234, v235\n\t"
      "buffer_store_dwordx2 v[236:237], v3, s[44:47], s55 offen\n\t"
      "buffer_store_dwordx2 v[238:239], v3, s[44:47], s55 offen offset:32\n\t"
      "buffer_store_dwordx2 v[240:241], v3, s[44:47], s55 offen offset:64\n\t"
      "buffer_store_dwordx2 v[242:243], v3, s[44:47], s55 offen offset:96\n\t"
      "buffer_store_dwordx2 v[244:245], v3, s[44:47], s55 offen offset:128\n\t"
      :
      : "s"(kargs()), "s"(WS_W + cb * 182u * 5120u), "s"(WS_H + t2 * 54u * 4096u), "s"(WS_O + t2 * 128u * 4096u), "s"(w),
        "s"(t2 * 64u * 1280u + cb * 160u), "s"(WS_END + 131072u + 4096u + (b * 4 + w) * 64u), "s"(nonce), "s"(t2), "s"(WS_FL + 4096 + 54 * 4 + (cb >> 1) * 768),
        "s"(WS_FL + 8192 + tb64 * 128 + h * 4)
      :
      "v0","v1","v2","v3","v4","v5","v6","v7","v8","v9","v10","v11","v12","v13","v14","v15","v16","v17","v18","v19","v20","v21","v22","v23",
      "v24","v25","v26","v27","v28","v29","v30","v31","v32","v33","v34","v35","v36","v37","v38","v39","v40","v41","v42","v43","v44","v45","v46","v47",
      "v48","v49","v50","v51","v52","v53","v54","v55","v56","v57","v58","v59","v60","v61","v62","v63","v64","v65","v66","v67","v68","v69","v70","v71",
      "v72","v73","v74","v75","v76","v77","v78","v79","v80","v81","v82","v83","v84","v85","v86","v87","v88","v89","v90","v91","v92","v93","v94","v95",
      "v96","v97","v98","v99","v100","v101","v102","v103","v104","v105","v106","v107","v108","v109","v110","v111","v112","v113","v114","v115","v116","v117","v118","v119",
      "v120","v121","v122","v123","v124","v125","v126","v127","v128","v129","v130","v131","v132","v133","v134","v135","v136","v137","v138","v139","v140","v141","v142","v143",
      "v144","v145","v146","v147","v148","v149","v150","v151","v152","v153","v154","v155","v156","v157","v158","v159","v160","v161","v162","v163","v164","v165","v166","v167",
      "v168","v169","v170","v171","v172","v173","v174","v175","v176","v177","v178","v179","v180","v181","v182","v183","v184","v185","v186","v187","v188","v189","v190","v191",
      "v192","v193","v194","v195","v196","v197","v198","v199","v200","v201","v202","v203","v204","v205","v206","v207","v208","v209","v210","v211","v212","v213","v214","v215",
      "v216","v217","v218","v219","v220","v221","v222","v223","v224","v225","v226","v227","v228","v229","v230","v231","v232","v233","v234","v235","v236","v237","v238","v239",
      "v240","v241","v242","v243","v244","v245","v246","v247","v248","v249","v250","v251","v252","v253","v254","v255","a0","a1","a2","a3","a4","a5","a6","a7",
      "a8","a9","a10","a11","a12","a13","a14","a15","a16","a17","a18","a19","a20","a21","a22","a23","a24","a25","a26","a27","a28","a29","a30","a31",
      "a32","a33","a34","a35","a36","a37","a38","a39","a40","a41","a42","a43","a44","a45","a46","a47","a48","a49","a50","a51","a52","a53","a54","a55",
      "a56","a57","a58","a59","a60","a61","a62","a63","a64","a65","a66","a67","a68","a69","a70","a71","a72","a73","a74","a75","a76","a77","a78","a79",
      "a80","a81","a82","a83","a84","a85","a86","a87","a88","a89","a90","a91","a92","a93","a94","a95","a96","a97","a98","a99","a100","a101","a102","a103",
      "a104","a105","a106","a107","a108","a109","a110","a111","a112","a113","a114","a115","a116","a117","a118","a119","a120","a121","a122","a123","a124","a125","a126","a127",
      "a128","a129","a130","a131","a132","a133","a134","a135","a136","a137","a138","a139","a140","a141","a142","a143","a144","a145","a146","a147","a148","a149","a150","a151",
      "a152","a153","a154","a155","a156","a157","a158","a159","a160","a161","a162","a163","a164","a165","a166","a167","a168","a169","a170","a171","a172","a173","a174","a175",
      "a176","a177","a178","a179","a180","a181","a182","a183","a184","a185","a186","a187","a188","a189","a190","a191","a192","a193","a194","a195","a196","a197","a198","a199",
      "a200","a201","a202","a203","a204","a205","a206","a207","a208","a209","a210","a211","a212","a213","a214","a215","a216","a217","a218","a219","a220","a221","a222","a223",
      "a224","a225","a226","a227","a228","a229","a230","a231","a232","a233","a234","a235","a236","a237","a238","a239","a240","a241","a242","a243","a244","a245","a246","a247",
      "a248","a249","a250","a251","a252","a253","a254","a255","s36","s37","s38","s39","s40","s41","s42","s43","s44","s45","s46","s47","s48","s49","s50","s51",
      "s52","s53","s54","s55","s56","s57","s58","s59","s60","s61","s62","s63","s84","s85","vcc","scc","memory");
  }
  }
  asm volatile("K9end:");
}

static void launch_all(void* const* d_in, void* d_out, void* d_ws, hipStream_t stream) {
  (void)hipFuncSetAttribute((const void*)fused_block, hipFuncAttributeMaxDynamicSharedMemorySize, 163840);
  fused_block<<<256, 256, 163840, stream>>>((const u16*)d_in[0], (const u16*)d_in[1], (const u16*)d_in[2], (const u16*)d_in[3], (const u16*)d_in[4],
                                            (const u16*)d_in[6], (const u16*)d_in[7], (const u16*)d_in[8], (char*)d_ws, (u16*)d_out);
}

extern "C" void kernel_launch(void* const* d_in, const int* in_sizes, int n_in, void* d_out, int out_size, void* d_ws, size_t ws_size,
                              hipStream_t stream) {
  launch_all(d_in, d_out, d_ws, stream);
}
```
